```python
import torch

def _softmax(x, axis=-1):
    return torch.softmax(x, dim=axis)
def _gelu(x):
    return torch.nn.functional.gelu(x, approximate="tanh")
def _silu(x):
    return torch.nn.functional.silu(x)
def _sigmoid(x):
    return torch.sigmoid(x)
def _layernorm(x, axis=-1, eps=1e-5):
    m = x.mean(dim=axis, keepdim=True)
    v = x.var(dim=axis, keepdim=True, unbiased=False)
    return (x - m) / torch.sqrt(v + eps)
def _scatter_add(tgt, idx, src, axis=0):
    return tgt.clone().index_add_(axis, idx, src)
def _segment_sum(src, seg, n):
    out = torch.zeros((n,) + src.shape[1:], dtype=src.dtype)
    return out.index_add_(0, seg, src)
def _pad(x, pw):
    flat = tuple(v for pair in reversed(pw) for v in pair)
    return torch.nn.functional.pad(x, flat)

kernel_name = 'gated_mix__sliding_local_dilated__mha_block__prompt'

def setup_inputs():

    _w = (lambda a: (lambda t: t.to(torch.bfloat16) if t.is_floating_point() else t)(torch.as_tensor(a)))
    def _A_setup():
        B, S, D = 1, 2048, 448
        return {
            "x": _w((lambda *s: torch.randn(*s, dtype=torch.bfloat16) * 0.05)(B, S, D)),
            "wqkv": _w((lambda *s: torch.randn(*s, dtype=torch.bfloat16) * 1.6)(D, 3 * D)),
            "wo": _w((lambda *s: torch.randn(*s, dtype=torch.bfloat16) * 0.05)(D, D)),
        }
    _A_in = _A_setup()

    def _B_setup():
        B, S, D, H = 1, 2048, 448, 16
        return {
            "x": _w((lambda *s: torch.randn(*s, dtype=torch.bfloat16) * 0.05)(B, S, D)),
            "wqkv": _w((lambda *s: torch.randn(*s, dtype=torch.bfloat16) * 1.6)(D, 3 * D)),
            "wo": _w((lambda *s: torch.randn(*s, dtype=torch.bfloat16) * 0.1)(D, D)),
        }
    _B_in = _B_setup()

    return {
        "A_x": _A_in["x"],
        "A_wqkv": _A_in["wqkv"],
        "A_wo": _A_in["wo"],
        "B_x": _B_in["x"],
        "B_wqkv": _B_in["wqkv"],
        "B_wo": _B_in["wo"],
        "gate_w": _w((lambda *s: torch.randn(*s, dtype=torch.bfloat16) * 0.05)(448) * 16.0),
    }

def reference(A_x, A_wqkv, A_wo, B_x, B_wqkv, B_wo, gate_w):
    def _A_ref(x, wqkv, wo):
        B, S, D = x.shape
        H = 16; Dh = D // H
        W, dil = 8, 2
        qkv = (lambda x, *d: x.permute(*d))((x @ wqkv).reshape(B, S, 3, H, Dh), 2, 0, 3, 1, 4)
        q, k, v = qkv[0], qkv[1], qkv[2]
        scale = Dh ** -0.5
        pos = torch.arange(S)
        off = torch.arange(W) * dil
        idx = pos[:, None] - off[None, :]
        mask = idx >= 0
        idx_c = torch.where(mask, idx, 0).reshape(-1)
        kg = k[:, :, idx_c, :].reshape(B, H, S, W, Dh)
        vg = v[:, :, idx_c, :].reshape(B, H, S, W, Dh)
        logits = torch.einsum("bhsd,bhswd->bhsw", q, kg) * scale
        logits = torch.where(mask[None, None, :, :], logits, -1e9)
        attn = _softmax(logits, axis=-1)
        o = torch.einsum("bhsw,bhswd->bhsd", attn, vg)
        return (lambda x, *d: x.permute(*d))(o, 0, 2, 1, 3).reshape(B, S, D) @ wo

    def _B_ref(x, wqkv, wo):
        B, S, D = x.shape
        H = 16
        Dh = D // H
        qkv = (lambda x, *d: x.permute(*d))((x @ wqkv).reshape(B, S, 3, H, Dh), 2, 0, 3, 1, 4)
        q, k, v = qkv[0], qkv[1], qkv[2]
        scores = torch.einsum("bhqd,bhkd->bhqk", q, k) * (Dh ** -0.5)
        p = _softmax(scores, axis=-1)
        o = torch.einsum("bhqk,bhkd->bhqd", p, v)
        return (lambda x, *d: x.permute(*d))(o, 0, 2, 1, 3).reshape(B, S, D) @ wo

    x = A_x
    g = _sigmoid((x * gate_w).sum(-1, keepdims=True))
    a = _A_ref(x, A_wqkv, A_wo)
    b = _B_ref(x, B_wqkv, B_wo)
    return g * a + (1 - g) * b
```

```cpp
#include <hip/hip_runtime.h>
#include <hip/hip_bf16.h>
#include <stdint.h>

typedef short bf16x8 __attribute__((ext_vector_type(8)));
typedef short s16x4 __attribute__((ext_vector_type(4)));
typedef float f32x16 __attribute__((ext_vector_type(16)));
typedef float f32x4 __attribute__((ext_vector_type(4)));
typedef float f32x2 __attribute__((ext_vector_type(2)));
typedef unsigned u32x4 __attribute__((ext_vector_type(4)));
typedef unsigned u32x2 __attribute__((ext_vector_type(2)));
typedef __bf16 bf2 __attribute__((ext_vector_type(2)));
typedef unsigned short u16;

#define DI __device__ __forceinline__
#define MFMA(a, b, c) __builtin_amdgcn_mfma_f32_32x32x16_bf16((a), (b), (c), 0, 0, 0)
#define LDSP __attribute__((address_space(3)))
#define GLBP __attribute__((address_space(1)))

constexpr int SEQ = 2048, DM = 448, NH = 16, DH = 28, N3 = 1344;
constexpr float QSCALE = 0.18898223650461362f * 1.4426950408889634f;

constexpr size_t WS_Q = 0;
constexpr size_t WS_KV = 2u << 20;
constexpr size_t WS_O = 6u << 20;
constexpr size_t WS_G = 10u << 20;

DI unsigned pack2(float lo, float hi) {
  f32x2 v = {lo, hi};
  return __builtin_bit_cast(unsigned, __builtin_convertvector(v, bf2));
}
DI bf16x8 as_frag(u32x4 v) { return __builtin_bit_cast(bf16x8, v); }
DI float bf_lo(unsigned v) { return __uint_as_float(v << 16); }
DI float bf_hi(unsigned v) { return __uint_as_float(v & 0xffff0000u); }
DI float swap_max(float v) {
  auto rr = __builtin_amdgcn_permlane32_swap(__float_as_uint(v), __float_as_uint(v), false, false);
  return fmaxf(__uint_as_float(rr[0]), __uint_as_float(rr[1]));
}
DI s16x4 tr_read(const char LDSP* p) {
  return __builtin_amdgcn_ds_read_tr16_b64_v4i16((s16x4 LDSP*)p);
}
DI bf16x8 cat(s16x4 a, s16x4 b) { return __builtin_shufflevector(a, b, 0, 1, 2, 3, 4, 5, 6, 7); }
template <int S>
DI u32x4 pack_step(const f32x16& x, float sc) {
  u32x4 p;
  p[0] = pack2(x[8 * S + 0] * sc, x[8 * S + 1] * sc);
  p[1] = pack2(x[8 * S + 2] * sc, x[8 * S + 3] * sc);
  p[2] = pack2(x[8 * S + 4] * sc, x[8 * S + 5] * sc);
  p[3] = pack2(x[8 * S + 6] * sc, x[8 * S + 7] * sc);
  return p;
}
template <int S>
DI u32x4 pack_step1(const f32x16& x) {
  u32x4 p;
  p[0] = pack2(x[8 * S + 0], x[8 * S + 1]);
  p[1] = pack2(x[8 * S + 2], x[8 * S + 3]);
  p[2] = pack2(x[8 * S + 4], x[8 * S + 5]);
  p[3] = pack2(x[8 * S + 6], x[8 * S + 7]);
  return p;
}

constexpr int K1_XBYTES = 160 * 128;
constexpr int K1_WBYTES = 64 * 384;
constexpr int K1_STAGE = K1_XBYTES + K1_WBYTES;
constexpr int K1_LDS = 3 * K1_STAGE;

constexpr unsigned long long lmask(int i, bool prev) {
  unsigned long long m = 0;
  for (int l = 0; l < 64; ++l) {
    int r = l & 31, h = l >> 5;
    int kr = (i & 3) + 8 * (i >> 2) + 4 * h;
    int diff = r - kr + (prev ? 32 : 0);
    if (diff >= 0 && diff <= 14 && (diff & 1) == 0) m |= 1ull << l;
  }
  return m;
}
DI float sel_mask(float val, float other, unsigned long long mask) {
  return __builtin_amdgcn_inverse_ballot_w64(mask) ? val : other;
}

template <bool ISA>
DI void k1_body(const u16* __restrict__ x, const u16* __restrict__ W, const u16* __restrict__ gate_w,
                char* __restrict__ ws, int mt, int hp, char LDSP* smem) {
  const int tid = threadIdx.x, lane = tid & 63;
  const int w = __builtin_amdgcn_readfirstlane(tid >> 6);
  const int msub = w & 3, hb = w >> 2;
  const int r = lane & 31, h = lane >> 5;
  const int s0 = mt * 128;
  int xsrc[2], wsrc[3], hsrc = 0;
#pragma unroll
  for (int i = 0; i < 2; ++i) {
    int lr = 32 + (w * 2 + i) * 8 + (lane >> 3);
    int logical = (lane & 7) ^ ((lr >> 1) & 7);
    xsrc[i] = (s0 + lr - 32) * DM + logical * 8;
  }
  if (ISA) {
    int lr = 16 + w * 2 + (lane >> 5);
    int d = lane & 31;
    int logical = (d >> 2) ^ ((lr >> 1) & 7);
    int row = s0 + lr - 32; row = row < 0 ? 0 : row;
    hsrc = row * DM + logical * 8 + (d & 3) * 2;
  }
#pragma unroll
  for (int i = 0; i < 3; ++i) {
    int id = (w * 3 + i) * 64 + lane;
    int row = id / 24, phys = id % 24;
    int logical = phys ^ (((row >> 1) & 1) << 2);
    wsrc[i] = row * N3 + (logical >> 3) * DM + hp * 56 + (logical & 7) * 8;
  }
  auto issue = [&](int kc, int buf) {
    char LDSP* st = smem + buf * K1_STAGE;
#pragma unroll
    for (int i = 0; i < 2; ++i)
      __builtin_amdgcn_global_load_lds((const GLBP void*)(x + xsrc[i] + kc * 64), (LDSP void*)(st + (32 + (w * 2 + i) * 8) * 128), 16, 0, 0);
    if (ISA)
      __builtin_amdgcn_global_load_lds((const GLBP void*)(x + hsrc + kc * 64), (LDSP void*)(st + (16 + w * 2) * 128), 4, 0, 0);
#pragma unroll
    for (int i = 0; i < 3; ++i) {
      int idx = wsrc[i] + kc * 64 * N3;
      idx = idx > DM * N3 - 8 ? DM * N3 - 8 : idx;
      __builtin_amdgcn_global_load_lds((const GLBP void*)(W + idx), (LDSP void*)(st + K1_XBYTES + (w * 3 + i) * 1024), 16, 0, 0);
    }
  };
  int xoff[4], hoff[4];
  {
    int lr = 32 + 32 * msub + r, sw = (lr >> 1) & 7;
#pragma unroll
    for (int t = 0; t < 4; ++t) xoff[t] = lr * 128 + (((2 * t + h) ^ sw) << 4);
    int sw2 = (r >> 1) & 7;
#pragma unroll
    for (int t = 0; t < 4; ++t) hoff[t] = r * 128 + (((2 * t + h) ^ sw2) << 4);
  }
  const int q4 = (lane & 15) >> 2, p4 = lane & 3, g16 = (lane >> 4) & 1;
  const int woff = K1_XBYTES + (8 * h + q4) * 384 + ((56 * hb + 32 * g16 + 8 * p4) ^ (((q4 >> 1) & 1) << 6));
  const int extra = ISA ? (w == 0 || w == 6 ? 1 : (w == 1 || w == 7 ? 2 : 0)) : 0;

  f32x16 aq = {}, ak = {}, av = {}, ah = {};
  issue(0, 0);
  issue(1, 1);
#pragma unroll
  for (int c = 0; c < 7; ++c) {
    if (c < 6) {
      if (ISA) asm volatile("s_waitcnt vmcnt(6)" ::: "memory");
      else asm volatile("s_waitcnt vmcnt(5)" ::: "memory");
    } else {
      asm volatile("s_waitcnt vmcnt(0)" ::: "memory");
    }
    asm volatile("s_waitcnt lgkmcnt(0)" ::: "memory");
    __builtin_amdgcn_s_barrier();
    if (c + 2 < 7) issue(c + 2, (c + 2) % 3);
    const char LDSP* st = smem + (c % 3) * K1_STAGE;
#pragma unroll
    for (int t = 0; t < 4; ++t) {
      bf16x8 xf = *(const bf16x8 LDSP*)(st + xoff[t]);
      const char LDSP* wp = st + woff + t * 16 * 384;
      bf16x8 wq = cat(tr_read(wp), tr_read(wp + 4 * 384));
      bf16x8 wk = cat(tr_read(wp + 128), tr_read(wp + 4 * 384 + 128));
      bf16x8 wv = cat(tr_read(wp + 256), tr_read(wp + 4 * 384 + 256));
      aq = MFMA(wq, xf, aq);
      ak = MFMA(wk, xf, ak);
      av = MFMA(xf, wv, av);
      if (ISA) {
        if (extra) {
          bf16x8 xh = *(const bf16x8 LDSP*)(st + hoff[t]);
          if (extra == 1) ah = MFMA(wk, xh, ah);
          else ah = MFMA(xh, wv, ah);
        }
      }
    }
  }
  const int head = hp * 2 + hb;
  const int tile = mt * 4 + msub;
  u32x4 q0 = pack_step<0>(aq, QSCALE), q1 = pack_step<1>(aq, QSCALE);
  u32x4 k0 = pack_step1<0>(ak), k1 = pack_step1<1>(ak);
  u32x4 v0 = pack_step1<0>(av), v1 = pack_step1<1>(av);
  if (h) { q1[2] = 0; q1[3] = 0; }
  if (r >= DH) {
    unsigned f = r == DH ? 0x3F803F80u : 0u;
    v0 = u32x4{f, f, f, f}; v1 = v0;
  }
  if (!ISA) {
    if (h) { k1[2] = 0x3F803F80u; k1[3] = 0x00003F80u; }
    u32x4* Qf = (u32x4*)(ws + WS_Q) + ((head * 64 + tile) * 2) * 64 + lane;
    u32x4* KVf = (u32x4*)(ws + WS_KV) + ((head * 64 + tile) * 4) * 64 + lane;
    Qf[0] = q0; Qf[64] = q1;
    KVf[0] = k0; KVf[64] = k1; KVf[128] = v0; KVf[192] = v1;
    if (hp == 0) {
      const int row = s0 + (tid >> 2), part = tid & 3;
      const u32x4* xp = (const u32x4*)(x + row * DM + part * 112);
      const u32x4* gp = (const u32x4*)(gate_w + part * 112);
      float acc = 0.f;
#pragma unroll
      for (int i = 0; i < 14; ++i) {
        u32x4 a = xp[i], b = gp[i];
#pragma unroll
        for (int j = 0; j < 4; ++j) {
          acc = fmaf(bf_lo(a[j]), bf_lo(b[j]), acc);
          acc = fmaf(bf_hi(a[j]), bf_hi(b[j]), acc);
        }
      }
      acc += __shfl_xor(acc, 1);
      acc += __shfl_xor(acc, 2);
      if (part == 0) ((float*)(ws + WS_G))[row] = 1.f / (1.f + __expf(-acc));
    }
  } else {
    if (h) { k1[2] = 0; k1[3] = 0; }
    asm volatile("s_waitcnt lgkmcnt(0)" ::: "memory");
    __builtin_amdgcn_s_barrier();
    u32x4 LDSP* ex = (u32x4 LDSP*)smem;
    {
      u32x4 LDSP* dst = ex + ((hb * 5 + msub + 1) * 3) * 64 + lane;
      dst[0] = k0; dst[64] = k1; dst[128] = v1;
      if (extra == 1) {
        u32x4 hk0 = pack_step1<0>(ah), hk1 = pack_step1<1>(ah);
        if (h) { hk1[2] = 0; hk1[3] = 0; }
        u32x4 LDSP* d2 = ex + ((hb * 5) * 3) * 64 + lane;
        d2[0] = hk0; d2[64] = hk1;
      } else if (extra == 2) {
        u32x4 hv1 = pack_step1<1>(ah);
        if (r >= DH) { unsigned f = r == DH ? 0x3F803F80u : 0u; hv1 = u32x4{f, f, f, f}; }
        u32x4 LDSP* d2 = ex + ((hb * 5) * 3) * 64 + lane;
        d2[128] = hv1;
      }
    }
    __syncthreads();
    const u32x4 LDSP* src = ex + ((hb * 5 + msub) * 3) * 64 + lane;
    u32x4 pk0 = src[0], pk1 = src[64], pv1 = src[128];
    f32x16 sp = MFMA(as_frag(pk0), as_frag(q0), f32x16{});
    sp = MFMA(as_frag(pk1), as_frag(q1), sp);
    f32x16 sc = MFMA(as_frag(k0), as_frag(q0), f32x16{});
    sc = MFMA(as_frag(k1), as_frag(q1), sc);
    const bool first = (tile == 0);
    const float NEG = -3.0e38f;
    float m = NEG;
#define LM_C(i) { sc[i] = sel_mask(sc[i], NEG, lmask(i, false)); m = fmaxf(m, sc[i]); }
#define LM_P(i) { sp[i] = sel_mask(sp[i], NEG, first ? 0ull : lmask(i, true)); m = fmaxf(m, sp[i]); }
    LM_C(0) LM_C(1) LM_C(2) LM_C(3) LM_C(4) LM_C(5) LM_C(6) LM_C(7)
    LM_C(8) LM_C(9) LM_C(10) LM_C(11) LM_C(12) LM_C(13) LM_C(14) LM_C(15)
    LM_P(8) LM_P(9) LM_P(10) LM_P(11) LM_P(12) LM_P(13) LM_P(14) LM_P(15)
#undef LM_C
#undef LM_P
    m = swap_max(m);
#pragma unroll
    for (int i = 0; i < 16; ++i) sc[i] = __builtin_amdgcn_exp2f(sc[i] - m);
#pragma unroll
    for (int i = 8; i < 16; ++i) sp[i] = __builtin_amdgcn_exp2f(sp[i] - m);
    f32x16 o = MFMA(as_frag(pv1), as_frag(pack_step1<1>(sp)), f32x16{});
    o = MFMA(as_frag(v0), as_frag(pack_step1<0>(sc)), o);
    o = MFMA(as_frag(v1), as_frag(pack_step1<1>(sc)), o);
    float l = __shfl(o[12], 32 + r);
    float inv = 1.f / l;
    u32x4 o0 = pack_step<0>(o, inv), o1 = pack_step<1>(o, inv);
    if (h) { o1[2] = 0; o1[3] = 0; }
    u32x4* Of = (u32x4*)(ws + WS_O) + ((tile * 32 + head) * 2) * 64 + lane;
    Of[0] = o0; Of[64] = o1;
  }
}

__global__ __launch_bounds__(512, 2) void k1_qkv(const u16* __restrict__ x, const u16* __restrict__ WA, const u16* __restrict__ WB,
                                                 const u16* __restrict__ gate_w, char* __restrict__ ws) {
  extern __shared__ __attribute__((aligned(16))) char smem_[];
  char LDSP* smem = (char LDSP*)smem_;
  const int b = blockIdx.x, xcd = b & 7, idx = b >> 3;
  const int mt = (xcd & 1) * 8 + (idx & 7);
  const int nt = (xcd >> 1) * 4 + (idx >> 3);
  if (nt < 8) k1_body<true>(x, WA, gate_w, ws, mt, nt, smem);
  else k1_body<false>(x, WB, gate_w, ws, mt, nt - 8, smem);
}

constexpr int K2_LDS = 131072 + 4096 + 16;

__global__ __launch_bounds__(512, 2) void k2_attn(char* __restrict__ ws) {
  extern __shared__ __attribute__((aligned(16))) char smem_[];
  char LDSP* smem = (char LDSP*)smem_;
  f32x4 LDSP* part = (f32x4 LDSP*)smem;
  float LDSP* mx = (float LDSP*)(smem + 131072);
  int LDSP* flag = (int LDSP*)(smem + 131072 + 4096);
  const int tid = threadIdx.x, lane = tid & 63;
  const int w = __builtin_amdgcn_readfirstlane(tid >> 6);
  const int r = lane & 31, h = lane >> 5;
  const int b = blockIdx.x;
  const int head = (b & 7) * 2 + ((b >> 3) & 1), qb = b >> 4;
  const u32x4* Qf = (const u32x4*)(ws + WS_Q) + ((head * 64 + qb * 4) * 2) * 64 + lane;
  const u32x4* kv = (const u32x4*)(ws + WS_KV) + ((head * 64 + w * 8) * 4) * 64 + lane;
  u32x4 q[4][2];
#pragma unroll
  for (int i = 0; i < 4; ++i) { q[i][0] = Qf[i * 128]; q[i][1] = Qf[i * 128 + 64]; }
  if (tid == 0) *flag = 0;

  for (int attempt = 0; attempt < 2; ++attempt) {
    {
      if (h) {
#pragma unroll
        for (int i = 0; i < 4; ++i) { q[i][1][2] = 0; q[i][1][3] = 0; }
      }
      float m[4] = {-3.0e38f, -3.0e38f, -3.0e38f, -3.0e38f};
      const int nt = attempt ? 8 : 1;
      for (int t = 0; t < nt; ++t) {
        u32x4 k0 = kv[t * 256], k1 = kv[t * 256 + 64];
#pragma unroll
        for (int i = 0; i < 4; ++i) {
          f32x16 s = MFMA(as_frag(k0), as_frag(q[i][0]), f32x16{});
          s = MFMA(as_frag(k1), as_frag(q[i][1]), s);
#pragma unroll
          for (int j = 0; j < 16; ++j) m[i] = fmaxf(m[i], s[j]);
        }
      }
#pragma unroll
      for (int i = 0; i < 4; ++i) {
        m[i] = swap_max(m[i]);
        if (!h) mx[(i * 32 + r) * 8 + w] = m[i];
      }
      __syncthreads();
#pragma unroll
      for (int i = 0; i < 4; ++i) {
        f32x4 a = *(f32x4 LDSP*)(mx + (i * 32 + r) * 8), c4 = *(f32x4 LDSP*)(mx + (i * 32 + r) * 8 + 4);
        float c = fmaxf(fmaxf(fmaxf(a[0], a[1]), fmaxf(a[2], a[3])), fmaxf(fmaxf(c4[0], c4[1]), fmaxf(c4[2], c4[3])));
        c = -c;
        unsigned p01 = pack2(c, 0.f);
        float r1 = c - bf_lo(p01);
        unsigned p1 = pack2(r1, 0.f);
        float r2 = r1 - bf_lo(p1);
        unsigned p2 = pack2(r2, 0.f);
        if (h) { q[i][1][2] = (p01 & 0xffffu) | (p1 << 16); q[i][1][3] = p2 & 0xffffu; }
      }
    }
    f32x16 o[4] = {};
    u32x4 kc0 = kv[0], kc1 = kv[64], vc0 = kv[128], vc1 = kv[192];
    for (int t = 0; t < 8; ++t) {
      const int tn = t < 7 ? t + 1 : 7;
      u32x4 kn0 = kv[tn * 256], kn1 = kv[tn * 256 + 64], vn0 = kv[tn * 256 + 128], vn1 = kv[tn * 256 + 192];
#pragma unroll
      for (int i = 0; i < 4; ++i) {
        f32x16 s = MFMA(as_frag(kc0), as_frag(q[i][0]), f32x16{});
        s = MFMA(as_frag(kc1), as_frag(q[i][1]), s);
#pragma unroll
        for (int j = 0; j < 16; ++j) s[j] = __builtin_amdgcn_exp2f(s[j]);
        o[i] = MFMA(as_frag(vc0), as_frag(pack_step1<0>(s)), o[i]);
        o[i] = MFMA(as_frag(vc1), as_frag(pack_step1<1>(s)), o[i]);
      }
      kc0 = kn0; kc1 = kn1; vc0 = vn0; vc1 = vn1;
    }
#pragma unroll
    for (int i = 0; i < 4; ++i)
#pragma unroll
      for (int g = 0; g < 4; ++g)
        part[((w * 4 + i) * 4 + g) * 64 + lane] = f32x4{o[i][4 * g], o[i][4 * g + 1], o[i][4 * g + 2], o[i][4 * g + 3]};
    __syncthreads();
    const int ri = w >> 1, rs = w & 1;
    f32x4 a0 = {}, a1 = {};
    float l = 0.f;
#pragma unroll
    for (int ww = 0; ww < 8; ++ww) {
      a0 += part[((ww * 4 + ri) * 4 + 2 * rs) * 64 + lane];
      a1 += part[((ww * 4 + ri) * 4 + 2 * rs + 1) * 64 + lane];
      l += ((float LDSP*)(part + ((ww * 4 + ri) * 4 + 3) * 64 + 32 + r))[0];
    }
    bool bad = !(l < 1.0e30f);
    float am = fmaxf(fmaxf(fmaxf(fabsf(a0[0]), fabsf(a0[1])), fmaxf(fabsf(a0[2]), fabsf(a0[3]))),
                     fmaxf(fmaxf(fabsf(a1[0]), fabsf(a1[1])), fmaxf(fabsf(a1[2]), fabsf(a1[3]))));
    bad = bad || !(am < 3.0e38f) || (a0[0] != a0[0]) || (a0[1] != a0[1]) || (a0[2] != a0[2]) || (a0[3] != a0[3]) ||
          (a1[0] != a1[0]) || (a1[1] != a1[1]) || (a1[2] != a1[2]) || (a1[3] != a1[3]);
    if (attempt == 0) {
      if (bad) *flag = 1;
      __syncthreads();
      if (*flag) { __syncthreads(); continue; }
    }
    const float inv = 1.f / l;
    u32x4 ov;
    ov[0] = pack2(a0[0] * inv, a0[1] * inv);
    ov[1] = pack2(a0[2] * inv, a0[3] * inv);
    ov[2] = pack2(a1[0] * inv, a1[1] * inv);
    ov[3] = pack2(a1[2] * inv, a1[3] * inv);
    if (h && rs) { ov[2] = 0; ov[3] = 0; }
    ((u32x4*)(ws + WS_O))[(((qb * 4 + ri) * 32 + 16 + head) * 2 + rs) * 64 + lane] = ov;
    break;
  }
}

constexpr int K3_LDS = 896 * 128;

__global__ __launch_bounds__(256, 1) void k3_out(const char* __restrict__ ws, const u16* __restrict__ WoA, const u16* __restrict__ WoB,
                                                  u16* __restrict__ out) {
  extern __shared__ __attribute__((aligned(16))) char smem_[];
  char LDSP* smem = (char LDSP*)smem_;
  const int tid = threadIdx.x, lane = tid & 63;
  const int w = __builtin_amdgcn_readfirstlane(tid >> 6);
  const int r = lane & 31, h = lane >> 5;
  const int b = blockIdx.x;
  const int nb = b % 7, qb = b / 7;
  const int n0 = nb * 64;
  {
    const u16* Wsrc = (w >> 1) ? WoB : WoA;
    const int row_in = lane >> 3;
    const int logical = (lane & 7) ^ (((row_in >> 1) & 1) << 2);
    const u16* src = Wsrc + ((w & 1) * 224 + row_in) * DM + n0 + logical * 8;
    char LDSP* dst = smem + w * 224 * 128;
#pragma unroll
    for (int i = 0; i < 28; ++i)
      __builtin_amdgcn_global_load_lds((const GLBP void*)(src + i * 8 * DM), (LDSP void*)(dst + i * 1024), 16, 0, 0);
  }
  u32x4 of[2][16];
  {
    const u32x4* Of = (const u32x4*)(ws + WS_O) + ((qb * 2 * 32 + w * 8) * 2) * 64 + lane;
#pragma unroll
    for (int qt = 0; qt < 2; ++qt)
#pragma unroll
      for (int i = 0; i < 16; ++i) of[qt][i] = Of[qt * 32 * 2 * 64 + i * 64];
  }
  asm volatile("s_waitcnt vmcnt(0)" ::: "memory");
  const int q4 = (lane & 15) >> 2, p4 = lane & 3, g16 = (lane >> 4) & 1;
  const int wbase = w * 224 * 128 + (4 * h + q4) * 128;
  const int cb = 32 * g16 + 8 * p4, sw = ((q4 >> 1) & 1) << 6;
  f32x16 acc[2][2] = {};
#pragma unroll
  for (int hd = 0; hd < 8; ++hd) {
#pragma unroll
    for (int s = 0; s < 2; ++s) {
#pragma unroll
      for (int nt = 0; nt < 2; ++nt) {
        const char LDSP* p0 = smem + wbase + (28 * hd + 16 * s) * 128 + ((64 * nt + cb) ^ sw);
        const char LDSP* p1 = p0 + ((s == 1) ? (h ? 4 * 128 : 8 * 128) : 8 * 128);
        bf16x8 wf = cat(tr_read(p0), tr_read(p1));
#pragma unroll
        for (int qt = 0; qt < 2; ++qt) acc[qt][nt] = MFMA(wf, as_frag(of[qt][hd * 2 + s]), acc[qt][nt]);
      }
    }
  }
  __syncthreads();
  f32x4 LDSP* part = (f32x4 LDSP*)smem;
#pragma unroll
  for (int qt = 0; qt < 2; ++qt)
#pragma unroll
    for (int nt = 0; nt < 2; ++nt)
#pragma unroll
      for (int g = 0; g < 4; ++g)
        part[((w * 4 + qt * 2 + nt) * 4 + g) * 64 + lane] =
            f32x4{acc[qt][nt][4 * g], acc[qt][nt][4 * g + 1], acc[qt][nt][4 * g + 2], acc[qt][nt][4 * g + 3]};
  __syncthreads();
  const int qt = w >> 1, nt = w & 1;
  const int qrow = qb * 64 + qt * 32 + r;
  const float gt = ((const float*)(ws + WS_G))[qrow];
#pragma unroll
  for (int g = 0; g < 4; ++g) {
    f32x4 a = part[((0 * 4 + w) * 4 + g) * 64 + lane] + part[((1 * 4 + w) * 4 + g) * 64 + lane];
    f32x4 bb = part[((2 * 4 + w) * 4 + g) * 64 + lane] + part[((3 * 4 + w) * 4 + g) * 64 + lane];
    f32x4 o = a * gt + bb * (1.f - gt);
    u32x2 ov = {pack2(o[0], o[1]), pack2(o[2], o[3])};
    *(u32x2*)(out + qrow * DM + n0 + nt * 32 + 8 * g + 4 * h) = ov;
  }
}

extern "C" void kernel_launch(void* const* d_in, const int* in_sizes, int n_in, void* d_out, int out_size, void* d_ws,
                              size_t ws_size, hipStream_t stream) {
  const u16* x = (const u16*)d_in[0];
  const u16* WA = (const u16*)d_in[1];
  const u16* WoA = (const u16*)d_in[2];
  const u16* WB = (const u16*)d_in[4];
  const u16* WoB = (const u16*)d_in[5];
  const u16* gw = (const u16*)d_in[6];
  char* ws = (char*)d_ws;
  (void)hipFuncSetAttribute((const void*)k1_qkv, hipFuncAttributeMaxDynamicSharedMemorySize, K1_LDS);
  (void)hipFuncSetAttribute((const void*)k2_attn, hipFuncAttributeMaxDynamicSharedMemorySize, K2_LDS);
  (void)hipFuncSetAttribute((const void*)k3_out, hipFuncAttributeMaxDynamicSharedMemorySize, K3_LDS);
  k1_qkv<<<256, 512, K1_LDS, stream>>>(x, WA, WB, gw, ws);
  k2_attn<<<256, 512, K2_LDS, stream>>>(ws);
  k3_out<<<224, 256, K3_LDS, stream>>>(ws, WoA, WoB, (u16*)d_out);
}
```

```cpp
#include <hip/hip_runtime.h>
#include <hip/hip_bf16.h>
#include <stdint.h>
#include <type_traits>

typedef short bf16x8 __attribute__((ext_vector_type(8)));
typedef short s16x4 __attribute__((ext_vector_type(4)));
typedef float f32x16 __attribute__((ext_vector_type(16)));
typedef float f32x4 __attribute__((ext_vector_type(4)));
typedef float f32x2 __attribute__((ext_vector_type(2)));
typedef unsigned u32x4 __attribute__((ext_vector_type(4)));
typedef unsigned u32x2 __attribute__((ext_vector_type(2)));
typedef __bf16 bf2 __attribute__((ext_vector_type(2)));
typedef unsigned short u16;

#define DI __device__ __forceinline__
#define MFMA(a, b, c) __builtin_amdgcn_mfma_f32_32x32x16_bf16((a), (b), (c), 0, 0, 0)
#define LDSP __attribute__((address_space(3)))
#define GLBP __attribute__((address_space(1)))

constexpr int SEQ = 2048, DM = 448, NH = 16, DH = 28, N3 = 1344;
constexpr float QSCALE = 0.18898223650461362f * 1.4426950408889634f;

#define ST(k, i)
constexpr unsigned WS_Q = 0;
constexpr unsigned WS_K = 4u << 20;
constexpr unsigned WS_V = 8u << 20;
constexpr unsigned WS_O = 12u << 20;
constexpr unsigned WS_G = 16u << 20;
constexpr unsigned WS_W = 17u << 20;
constexpr unsigned WS_C = 19u << 20;
constexpr unsigned QK_BR = 64 * 56 * 512;

DI unsigned pack2(float lo, float hi) {
  f32x2 v = {lo, hi};
  return __builtin_bit_cast(unsigned, __builtin_convertvector(v, bf2));
}
DI bf16x8 as_frag(u32x4 v) { return __builtin_bit_cast(bf16x8, v); }
DI float bf_lo(unsigned v) { return __uint_as_float(v << 16); }
DI float bf_hi(unsigned v) { return __uint_as_float(v & 0xffff0000u); }
DI float swap_max(float v) {
  auto rr = __builtin_amdgcn_permlane32_swap(__float_as_uint(v), __float_as_uint(v), false, false);
  return fmaxf(__uint_as_float(rr[0]), __uint_as_float(rr[1]));
}
DI s16x4 tr_read(const char LDSP* p) {
  return __builtin_amdgcn_ds_read_tr16_b64_v4i16((s16x4 LDSP*)p);
}
DI bf16x8 cat(s16x4 a, s16x4 b) { return __builtin_shufflevector(a, b, 0, 1, 2, 3, 4, 5, 6, 7); }
template <int S>
DI u32x4 pack_step(const f32x16& x, float sc) {
  u32x4 p;
  p[0] = pack2(x[8 * S + 0] * sc, x[8 * S + 1] * sc);
  p[1] = pack2(x[8 * S + 2] * sc, x[8 * S + 3] * sc);
  p[2] = pack2(x[8 * S + 4] * sc, x[8 * S + 5] * sc);
  p[3] = pack2(x[8 * S + 6] * sc, x[8 * S + 7] * sc);
  return p;
}
template <int S>
DI u32x4 pack_step1(const f32x16& x) {
  u32x4 p;
  p[0] = pack2(x[8 * S + 0], x[8 * S + 1]);
  p[1] = pack2(x[8 * S + 2], x[8 * S + 3]);
  p[2] = pack2(x[8 * S + 4], x[8 * S + 5]);
  p[3] = pack2(x[8 * S + 6], x[8 * S + 7]);
  return p;
}

#define RAWBAR() do { asm volatile("" ::: "memory"); __builtin_amdgcn_s_barrier(); asm volatile("" ::: "memory"); } while (0)
#define WAITV(n) ((((n) & 15) | (((n) >> 4) << 14)) | 0x0F70)
#define WAITVL(n) ((((n) & 15) | (((n) >> 4) << 14)) | 0x0070)
DI u32x4 widen(unsigned a0, unsigned a1, unsigned b0, unsigned b1) {
  auto r0 = __builtin_amdgcn_permlane32_swap(a0, b0, false, false);
  auto r1 = __builtin_amdgcn_permlane32_swap(a1, b1, false, false);
  return u32x4{r0[0], r1[0], r0[1], r1[1]};
}

constexpr int K1_XBYTES = 128 * 128;
constexpr int K1_WBYTES = 64 * 384;
constexpr int K1_STAGE = K1_XBYTES + K1_WBYTES;
constexpr int K1_LDS = 4 * K1_STAGE;

template <bool V>
DI void k1_store(const f32x16& a, int ct, int tile, int lane, char* __restrict__ ws) {
  const int br = ct / 42, c42 = ct % 42, type = c42 / 14, cbase = (c42 % 14) * 32;
  if (!V) {
    const float sc = type == 0 ? QSCALE : 1.f;
    u32x4* p = (u32x4*)(ws + (type == 0 ? WS_Q : WS_K) + br * QK_BR) + (tile * 56 + (cbase >> 3)) * 32 + lane;
#pragma unroll
    for (int g = 0; g < 2; ++g)
      p[g * 64] = widen(pack2(a[8 * g] * sc, a[8 * g + 1] * sc), pack2(a[8 * g + 2] * sc, a[8 * g + 3] * sc),
                        pack2(a[8 * g + 4] * sc, a[8 * g + 5] * sc), pack2(a[8 * g + 6] * sc, a[8 * g + 7] * sc));
  } else {
    const unsigned f = cbase + (lane & 31), head = f / 28, d = f % 28;
    u32x4* p = (u32x4*)(ws + WS_V) + (((br * 16 + head) * 64 + tile) * 2) * 64 + d + (lane & 32);
    p[0] = pack_step1<0>(a);
    p[64] = pack_step1<1>(a);
    if (d < 4) {
      const unsigned c = d == 0 ? 0x3F803F80u : 0u;
      p[28] = u32x4{c, c, c, c};
      p[64 + 28] = u32x4{c, c, c, c};
    }
  }
}

template <int NV>
DI void k1_body(const u16* __restrict__ x, const u16* __restrict__ W, char* __restrict__ ws, int mt, int nt, char LDSP* smem) {
  const int tid = threadIdx.x, lane = tid & 63;
  const int w = __builtin_amdgcn_readfirstlane(tid >> 6);
  const int msub = w & 3, nh = w >> 2;
  const int r = lane & 31, h = lane >> 5;
  const int s0 = mt * 128;
  const u16* xs[2];
  const u16* wsrc[3];
#pragma unroll
  for (int i = 0; i < 2; ++i) {
    int lr = (w * 2 + i) * 8 + (lane >> 3);
    int logical = (lane & 7) ^ ((lr >> 1) & 7);
    xs[i] = x + (s0 + lr) * DM + logical * 8;
  }
#pragma unroll
  for (int i = 0; i < 3; ++i) {
    int id = (w * 3 + i) * 64 + lane;
    int row = id / 24, phys = id % 24;
    int logical = phys ^ (((row >> 1) & 1) << 2);
    wsrc[i] = W + row * N3 + (nt % 7) * 192 + logical * 8;
  }
  auto issue = [&](int kc) {
    char LDSP* st = smem + (kc & 3) * K1_STAGE;
#pragma unroll
    for (int i = 0; i < 2; ++i)
      __builtin_amdgcn_global_load_lds((const GLBP void*)(xs[i] + kc * 64), (LDSP void*)(st + (w * 2 + i) * 1024), 16, 0, 0);
#pragma unroll
    for (int i = 0; i < 3; ++i)
      __builtin_amdgcn_global_load_lds((const GLBP void*)(wsrc[i] + kc * 64 * N3), (LDSP void*)(st + K1_XBYTES + (w * 3 + i) * 1024), 16, 0, 0);
  };
  int xoff[4];
  {
    int lr = 32 * msub + r, sw = (lr >> 1) & 7;
#pragma unroll
    for (int t = 0; t < 4; ++t) xoff[t] = lr * 128 + (((2 * t + h) ^ sw) << 4);
  }
  const int q4 = (lane & 15) >> 2, p4 = lane & 3, g16 = (lane >> 4) & 1;
  int woff[3];
#pragma unroll
  for (int j = 0; j < 3; ++j)
    woff[j] = K1_XBYTES + (8 * h + q4) * 384 + ((64 * (3 * nh + j) + 32 * g16 + 8 * p4) ^ (((q4 >> 1) & 1) << 6));

  f32x16 acc[3] = {};
  bf16x8 xf[2][4];
  s16x4 wl[2][4][3], wh[2][4][3];
  auto readf = [&](int c, int m) {
    const char LDSP* st = smem + (c & 3) * K1_STAGE;
    const int t = m / 3, j = m % 3;
    if (j == 0) xf[c & 1][t] = *(const bf16x8 LDSP*)(st + xoff[t]);
    const char LDSP* wp = st + woff[j] + t * 16 * 384;
    wl[c & 1][t][j] = tr_read(wp);
    wh[c & 1][t][j] = tr_read(wp + 4 * 384);
  };
  auto issue1 = [&](int kc, int i) {
    char LDSP* st = smem + (kc & 3) * K1_STAGE;
    if (i < 2) __builtin_amdgcn_global_load_lds((const GLBP void*)(xs[i] + kc * 64), (LDSP void*)(st + (w * 2 + i) * 1024), 16, 0, 0);
    else __builtin_amdgcn_global_load_lds((const GLBP void*)(wsrc[i - 2] + kc * 64 * N3), (LDSP void*)(st + K1_XBYTES + (w * 3 + i - 2) * 1024), 16, 0, 0);
  };
  ST(0, 0);
  issue(0); issue(1); issue(2); issue(3);
  __builtin_amdgcn_s_waitcnt(WAITV(15));
  RAWBAR();
#pragma unroll
  for (int m = 0; m < 12; ++m) readf(0, m);
#pragma unroll
  for (int c = 0; c < 7; ++c) {
    if (c < 6) {
      if (c < 4) __builtin_amdgcn_s_waitcnt(WAITVL(10));
      else if (c == 4) __builtin_amdgcn_s_waitcnt(WAITVL(5));
      else __builtin_amdgcn_s_waitcnt(WAITVL(0));
      RAWBAR();
    }
    __builtin_amdgcn_sched_barrier(0);
#pragma unroll
    for (int m = 0; m < 12; ++m) {
      const int t = m / 3, j = m % 3;
      const bf16x8 wfr = cat(wl[c & 1][t][j], wh[c & 1][t][j]);
      if (j < 3 - NV) acc[j] = MFMA(wfr, xf[c & 1][t], acc[j]);
      else acc[j] = MFMA(xf[c & 1][t], wfr, acc[j]);
      __builtin_amdgcn_sched_barrier(0);
      if (c < 6) readf(c + 1, m);
      if (c + 4 < 7 && m >= 2 && m < 12 && (m & 1) == 0) issue1(c + 4, (m - 2) >> 1);
      __builtin_amdgcn_sched_barrier(0);
    }
  }
  ST(0, 1);
  const int tile = mt * 4 + msub;
#pragma unroll
  for (int j = 0; j < 3; ++j) {
    const int ct = nt * 6 + nh * 3 + j;
    if (j < 3 - NV) k1_store<false>(acc[j], ct, tile, lane, ws);
    else k1_store<true>(acc[j], ct, tile, lane, ws);
  }
}

__global__ __launch_bounds__(512, 2) void k1_qkv(const u16* __restrict__ x, const u16* __restrict__ WA, const u16* __restrict__ WB,
                                                 const u16* __restrict__ gate_w, const u16* __restrict__ WoA,
                                                 const u16* __restrict__ WoB, char* __restrict__ ws) {
  extern __shared__ __attribute__((aligned(16))) char smem_[];
  char LDSP* smem = (char LDSP*)smem_;
  const int b = blockIdx.x, xcd = b & 7, j = b >> 3;
  if (j < 28) {
    const int mt = 4 * (xcd & 3) + (j & 3);
    const int nt = 7 * (xcd >> 2) + (j >> 2);
    const int g3 = (nt * 2 + __builtin_amdgcn_readfirstlane(threadIdx.x >> 8)) % 14;
    const u16* W = nt < 7 ? WA : WB;
    if (g3 < 9) k1_body<0>(x, W, ws, mt, nt, smem);
    else if (g3 == 9) k1_body<2>(x, W, ws, mt, nt, smem);
    else k1_body<3>(x, W, ws, mt, nt, smem);
  } else {
    const int tid = threadIdx.x, sp = xcd * 4 + j - 28;
    {
      const int lane = tid & 63, r = lane & 31, h = lane >> 5;
#pragma unroll
      for (int e = 0; e < 4; ++e) {
        const int fid = sp * 32 + (tid >> 6) * 4 + e;
        const int sfr = fid & 1, vh = (fid >> 1) & 31, nt = (fid >> 6) & 1, nb = fid >> 7;
        const int n = 56 * nb + 32 * nt + r;
        const u16* Wo = (vh >> 4) ? WoB : WoA;
        unsigned v[8];
#pragma unroll
        for (int jj = 0; jj < 8; ++jj) {
          const int d = 16 * sfr + 8 * (jj >> 2) + 4 * h + (jj & 3);
          const bool ok = d < DH && n < DM;
          v[jj] = ok ? Wo[ok ? ((vh & 15) * DH + d) * DM + n : 0] : 0;
        }
        ((u32x4*)(ws + WS_W))[fid * 64 + lane] = u32x4{v[0] | (v[1] << 16), v[2] | (v[3] << 16), v[4] | (v[5] << 16), v[6] | (v[7] << 16)};
      }
    }
    if (tid == 0) *(unsigned*)(ws + WS_C + sp * 256) = 0u;
    const int row = sp * 64 + (tid >> 3), part = tid & 7;
    const u32x4* xp = (const u32x4*)(x + row * DM) + part;
    const u32x4* gp = (const u32x4*)(gate_w) + part;
    float acc = 0.f;
#pragma unroll
    for (int i = 0; i < 7; ++i) {
      u32x4 a = xp[i * 8], bb = gp[i * 8];
#pragma unroll
      for (int e = 0; e < 4; ++e) {
        acc = fmaf(bf_lo(a[e]), bf_lo(bb[e]), acc);
        acc = fmaf(bf_hi(a[e]), bf_hi(bb[e]), acc);
      }
    }
    acc += __shfl_xor(acc, 1);
    acc += __shfl_xor(acc, 2);
    acc += __shfl_xor(acc, 4);
    if (part == 0) ((float*)(ws + WS_G))[row] = 1.f / (1.f + __expf(-acc));
  }
}

constexpr int K2_LDS = 131072 + 4096 + 16384 + 16;

constexpr unsigned long long lmask(int i, bool prev) {
  unsigned long long m = 0;
  for (int l = 0; l < 64; ++l) {
    int r = l & 31, h = l >> 5;
    int kr = (i & 3) + 8 * (i >> 2) + 4 * h;
    int diff = r - kr + (prev ? 32 : 0);
    if (diff >= 0 && diff <= 14 && (diff & 1) == 0) m |= 1ull << l;
  }
  return m;
}
DI float sel_mask(float val, float other, unsigned long long mask) {
  return __builtin_amdgcn_inverse_ballot_w64(mask) ? val : other;
}
typedef __amdgpu_buffer_rsrc_t rsrc_t;
DI u32x4 bload(rsrc_t rs, unsigned voff, unsigned soff) {
  return __builtin_bit_cast(u32x4, __builtin_amdgcn_raw_buffer_load_b128(rs, voff, soff, 0));
}
template <int ODD>
DI void set_foreign(u32x4& f0, u32x4& f1, int h, unsigned a, unsigned b) {
  if (ODD) { if (!h) { f0[0] = a; f0[1] = b; } }
  else { if (h) { f1[2] = a; f1[3] = b; } }
}

DI u32x4 bload_sc1(rsrc_t rs, unsigned voff, unsigned soff) {
  return __builtin_bit_cast(u32x4, __builtin_amdgcn_raw_buffer_load_b128(rs, voff, soff, 16));
}
typedef unsigned v4u_t __attribute__((ext_vector_type(4)));
DI void bstore_sc1(u32x4 v, rsrc_t rs, unsigned voff, unsigned soff) {
  __builtin_amdgcn_raw_buffer_store_b128(__builtin_bit_cast(v4u_t, v), rs, voff, soff, 16);
}
typedef GLBP unsigned gu32;
DI void wait_count(gu32* cnt, unsigned want) {
  for (unsigned spins = 0; spins < (1u << 20); ++spins) {
    if (__hip_atomic_load(cnt, __ATOMIC_RELAXED, __HIP_MEMORY_SCOPE_AGENT) >= want) break;
    __builtin_amdgcn_s_sleep(1);
  }
}

template <int ODD>
DI void k2_body(char* __restrict__ ws, u16* __restrict__ out, char LDSP* smem) {
  f32x4 LDSP* part = (f32x4 LDSP*)smem;
  float LDSP* cw = (float LDSP*)(smem + 131072);
  f32x4 LDSP* apart = (f32x4 LDSP*)(smem + 131072 + 4096);
  int LDSP* flag = (int LDSP*)(smem + 131072 + 4096 + 16384);
  int LDSP* lcnt = flag + 1;
  const int tid = threadIdx.x, lane = tid & 63;
  const int w = __builtin_amdgcn_readfirstlane(tid >> 6);
  const int r = lane & 31, h = lane >> 5;
  const int b = blockIdx.x;
  const int head = (b & 7) * 2 + ODD, qb = b >> 4;
  const int oqb = (b & 7) * 2 + (b >> 7), oqh = (b >> 3) & 1, nb = (b >> 4) & 7;
  const unsigned p0 = (7 * head) >> 1;
  const rsrc_t rs = __builtin_amdgcn_make_buffer_rsrc(ws, 0, 32u << 20, 0x00020000);
  gu32* cntA = (gu32*)(ws + WS_C + qb * 256);
  gu32* cntB = (gu32*)(ws + WS_C + (16 + qb) * 256);
  gu32* ocntA = (gu32*)(ws + WS_C + oqb * 256);
  gu32* ocntB = (gu32*)(ws + WS_C + (16 + oqb) * 256);
  const unsigned l16 = lane * 16;
  const unsigned qoff = WS_Q + QK_BR + (qb * 4 * 56 + p0) * 512;
  const unsigned koff = WS_K + QK_BR + (w * 8 * 56 + p0) * 512;
  const unsigned voff = WS_V + ((16 + head) * 64 + w * 8) * 2048;
  ST(1, 0);
  ST(2, 0);
  u32x4 q[4][2];
  u32x4 kf[3][2], vf[3][2];
  auto loadkv = [&](int t) {
    if (t >= 4 && w >= 4) {
      const u32x4 LDSP* sp = (const u32x4 LDSP*)(smem + w * 16384 + (t - 4) * 4096) + lane;
      kf[t % 3][0] = sp[0]; kf[t % 3][1] = sp[64]; vf[t % 3][0] = sp[128]; vf[t % 3][1] = sp[192];
    } else {
      kf[t % 3][0] = bload(rs, l16, koff + t * 28672);
      kf[t % 3][1] = bload(rs, l16 + 1024, koff + t * 28672);
      vf[t % 3][0] = bload(rs, l16, voff + t * 2048);
      vf[t % 3][1] = bload(rs, l16 + 1024, voff + t * 2048);
    }
  };
  kf[0][0] = bload(rs, l16, koff);
  kf[0][1] = bload(rs, l16 + 1024, koff);
#pragma unroll
  for (int i = 0; i < 4; ++i) { q[i][0] = bload(rs, l16, qoff + i * 28672); q[i][1] = bload(rs, l16 + 1024, qoff + i * 28672); }
  vf[0][0] = bload(rs, l16, voff);
  vf[0][1] = bload(rs, l16 + 1024, voff);
  u32x4 lq0, lq1, lk0, lk1, lv0, lv1, pk0, pk1, pv1;
  const int ltile = qb * 4 + (w & 3);
  if (w >= 4) {
    const unsigned lqo = WS_Q + (ltile * 56 + p0) * 512, lko = WS_K + (ltile * 56 + p0) * 512, lvo = WS_V + (head * 64 + ltile) * 2048;
    const unsigned pd = ltile > 0 ? 1 : 0;
    lq0 = bload(rs, l16, lqo); lq1 = bload(rs, l16 + 1024, lqo);
    lk0 = bload(rs, l16, lko); lk1 = bload(rs, l16 + 1024, lko);
    pk0 = bload(rs, l16, lko - pd * 28672); pk1 = bload(rs, l16 + 1024, lko - pd * 28672);
    lv0 = bload(rs, l16, lvo); lv1 = bload(rs, l16 + 1024, lvo);
    pv1 = bload(rs, l16 + 1024, lvo - pd * 2048);
  }
  loadkv(1);
  if (tid == 0) { *flag = 0; *lcnt = 0; }
  __syncthreads();

  if (w >= 4) {
    __builtin_amdgcn_s_setprio(2);
    set_foreign<ODD>(lq0, lq1, h, 0u, 0u);
    f32x16 sp = MFMA(as_frag(pk0), as_frag(lq0), f32x16{});
    sp = MFMA(as_frag(pk1), as_frag(lq1), sp);
    f32x16 sc = MFMA(as_frag(lk0), as_frag(lq0), f32x16{});
    sc = MFMA(as_frag(lk1), as_frag(lq1), sc);
    const bool first = (ltile == 0);
    const float NEG = -3.0e38f;
    float m = NEG;
#define LM_C(i) { sc[i] = sel_mask(sc[i], NEG, lmask(i, false)); m = fmaxf(m, sc[i]); }
#define LM_P(i) { sp[i] = sel_mask(sp[i], NEG, first ? 0ull : lmask(i, true)); m = fmaxf(m, sp[i]); }
    LM_C(0) LM_C(1) LM_C(2) LM_C(3) LM_C(4) LM_C(5) LM_C(6) LM_C(7)
    LM_C(8) LM_C(9) LM_C(10) LM_C(11) LM_C(12) LM_C(13) LM_C(14) LM_C(15)
    LM_P(8) LM_P(9) LM_P(10) LM_P(11) LM_P(12) LM_P(13) LM_P(14) LM_P(15)
#undef LM_C
#undef LM_P
    m = swap_max(m);
#pragma unroll
    for (int i = 0; i < 16; ++i) sc[i] = __builtin_amdgcn_exp2f(sc[i] - m);
#pragma unroll
    for (int i = 8; i < 16; ++i) sp[i] = __builtin_amdgcn_exp2f(sp[i] - m);
    f32x16 o = MFMA(as_frag(pv1), as_frag(pack_step1<1>(sp)), f32x16{});
    o = MFMA(as_frag(lv0), as_frag(pack_step1<0>(sc)), o);
    o = MFMA(as_frag(lv1), as_frag(pack_step1<1>(sc)), o);
    float ll = __shfl(o[12], 32 + r);
    float inv = 1.f / ll;
    u32x4 o0 = pack_step<0>(o, inv), o1 = pack_step<1>(o, inv);
    if (h) { o1[2] = 0; o1[3] = 0; }
    const unsigned oo = WS_O + ((ltile * 32 + head) * 2) * 1024;
    bstore_sc1(o0, rs, l16, oo);
    bstore_sc1(o1, rs, l16 + 1024, oo);
#pragma unroll
    for (int e = 0; e < 16; ++e) {
      const int t = 4 + (e >> 2), f = e & 3;
      const char* gsrc = ws + (f < 2 ? koff + t * 28672 + f * 1024 : voff + t * 2048 + (f - 2) * 1024) + l16;
      const unsigned ldst = (unsigned)(size_t)(smem + w * 16384 + e * 1024);
      unsigned keep;
      asm volatile("s_mov_b32 %0, m0\n\ts_mov_b32 m0, %2\n\ts_nop 0\n\tglobal_load_lds_dwordx4 %1, off\n\ts_mov_b32 m0, %0"
                   : "=&s"(keep) : "v"(gsrc), "s"(ldst) : "memory");
    }
    asm volatile("s_waitcnt vmcnt(0)" ::: "memory");
    if (lane == 0) {
      if (__hip_atomic_fetch_add(lcnt, 1, __ATOMIC_RELAXED, __HIP_MEMORY_SCOPE_WORKGROUP) == 3)
        __hip_atomic_fetch_add(cntA, 1u, __ATOMIC_RELAXED, __HIP_MEMORY_SCOPE_AGENT);
    }
    __builtin_amdgcn_s_setprio(0);
  }

  auto fold_shift = [&](int i, float m) {
    m = swap_max(m);
    if (!h) cw[(i * 32 + r) * 8 + w] = m;
    const float c = -m;
    unsigned p01 = pack2(c, 0.f);
    float r1 = c - bf_lo(p01);
    unsigned p1 = pack2(r1, 0.f);
    float r2 = r1 - bf_lo(p1);
    unsigned p2 = pack2(r2, 0.f);
    set_foreign<ODD>(q[i][0], q[i][1], h, (p01 & 0xffffu) | (p1 << 16), p2 & 0xffffu);
  };
  const int ri = w >> 1, rs1 = w & 1;
  f32x4 a0, a1;
  float l;
  auto write_part = [&](f32x16 (&o)[4]) {
#pragma unroll
    for (int i = 0; i < 4; ++i)
#pragma unroll
      for (int g = 0; g < 4; ++g)
        part[((w * 4 + i) * 4 + g) * 64 + lane] = f32x4{o[i][4 * g], o[i][4 * g + 1], o[i][4 * g + 2], o[i][4 * g + 3]};
  };
  auto reduce = [&]() {
    const f32x4 c0 = *(f32x4 LDSP*)(cw + (ri * 32 + r) * 8), c1 = *(f32x4 LDSP*)(cw + (ri * 32 + r) * 8 + 4);
    const float cm = fmaxf(fmaxf(fmaxf(c0[0], c0[1]), fmaxf(c0[2], c0[3])), fmaxf(fmaxf(c1[0], c1[1]), fmaxf(c1[2], c1[3])));
    a0 = f32x4{}; a1 = f32x4{}; l = 0.f;
#pragma unroll
    for (int ww = 0; ww < 8; ++ww) {
      const float wt = __builtin_amdgcn_exp2f((ww < 4 ? c0[ww & 3] : c1[ww & 3]) - cm);
      a0 += part[((ww * 4 + ri) * 4 + 2 * rs1) * 64 + lane] * wt;
      a1 += part[((ww * 4 + ri) * 4 + 2 * rs1 + 1) * 64 + lane] * wt;
      l += ((float LDSP*)(part + ((ww * 4 + ri) * 4 + 3) * 64 + 32 + r))[0] * wt;
    }
  };
  const int qt = w & 1, kq = w >> 1;
  u32x4 wfr[2][8];
  auto loadW = [&]() {
#pragma unroll
    for (int nt = 0; nt < 2; ++nt) {
      const unsigned wo = WS_W + (((nb * 2 + nt) * 32 + 16 + 4 * kq) * 2) * 1024;
#pragma unroll
      for (int e = 0; e < 8; ++e) wfr[nt][e] = bload(rs, l16 + (e & 3) * 1024, wo + (e >> 2) * 4096);
    }
  };

#pragma unroll
  for (int i = 0; i < 4; ++i) {
    set_foreign<ODD>(q[i][0], q[i][1], h, 0u, 0u);
    f32x16 s = MFMA(as_frag(kf[0][0]), as_frag(q[i][0]), f32x16{});
    s = MFMA(as_frag(kf[0][1]), as_frag(q[i][1]), s);
    float m = s[0];
#pragma unroll
    for (int j = 1; j < 16; ++j) m = fmaxf(m, s[j]);
    fold_shift(i, m);
  }
  ST(1, 1);
  {
    f32x16 o[4] = {};
    set_foreign<ODD>(kf[0][0], kf[0][1], h, 0x3F803F80u, 0x00003F80u);
    f32x16 sa = MFMA(as_frag(kf[0][0]), as_frag(q[0][0]), f32x16{});
    sa = MFMA(as_frag(kf[0][1]), as_frag(q[0][1]), sa);
    f32x16 sb;
    u32x4 pa;
    u32x4 pbp;
    float e3[4];
#define SB __builtin_amdgcn_sched_barrier(0)
#define EXPQ(S, g) { S[4 * g] = __builtin_amdgcn_exp2f(S[4 * g]); S[4 * g + 1] = __builtin_amdgcn_exp2f(S[4 * g + 1]); \
                     S[4 * g + 2] = __builtin_amdgcn_exp2f(S[4 * g + 2]); S[4 * g + 3] = __builtin_amdgcn_exp2f(S[4 * g + 3]); }
    auto step = [&](auto nc, f32x16& sc, f32x16& sn) {
      constexpr int n = decltype(nc)::value;
      constexpr int t = n >> 2, i = n & 3;
      constexpr int t1 = ((n + 1) >> 2) % 3, i1 = (n + 1) & 3;
      constexpr int tp = n > 0 ? ((n - 1) >> 2) % 3 : 0, ip = n > 0 ? (n - 1) & 3 : 0;
      if ((n & 7) == 0 && n > 0 && w >= 4) ST(2, n >> 3);
      if (i == 1 && t + 2 < 8) loadkv(t + 2);
      if (i == 3 && n + 1 < 32) set_foreign<ODD>(kf[t1][0], kf[t1][1], h, 0x3F803F80u, 0x00003F80u);
      SB;
      if (n + 1 < 32) sn = MFMA(as_frag(kf[t1][0]), as_frag(q[i1][0]), f32x16{});
      SB;
      if (n > 0) { pbp[2] = pack2(e3[0], e3[1]); pbp[3] = pack2(e3[2], e3[3]); }
      EXPQ(sc, 0);
      SB;
      if (n + 1 < 32) sn = MFMA(as_frag(kf[t1][1]), as_frag(q[i1][1]), sn);
      SB;
      pa[0] = pack2(sc[0], sc[1]); pa[1] = pack2(sc[2], sc[3]);
      EXPQ(sc, 1);
      SB;
      if (n > 0) o[ip] = MFMA(as_frag(vf[tp][1]), as_frag(pbp), o[ip]);
      SB;
      pa[2] = pack2(sc[4], sc[5]); pa[3] = pack2(sc[6], sc[7]);
      EXPQ(sc, 2);
      SB;
      o[i] = MFMA(as_frag(vf[t % 3][0]), as_frag(pa), o[i]);
      SB;
      pbp[0] = pack2(sc[8], sc[9]); pbp[1] = pack2(sc[10], sc[11]);
      EXPQ(sc, 3);
      e3[0] = sc[12]; e3[1] = sc[13]; e3[2] = sc[14]; e3[3] = sc[15];
      SB;
    };
#define STEP2(n) step(std::integral_constant<int, n>{}, sa, sb); step(std::integral_constant<int, n + 1>{}, sb, sa);
    STEP2(0) STEP2(2) STEP2(4) STEP2(6) STEP2(8) STEP2(10) STEP2(12) STEP2(14)
    STEP2(16) STEP2(18) STEP2(20) STEP2(22) STEP2(24) STEP2(26) STEP2(28) STEP2(30)
#undef STEP2
    pbp[2] = pack2(e3[0], e3[1]); pbp[3] = pack2(e3[2], e3[3]);
    o[3] = MFMA(as_frag(vf[7 % 3][1]), as_frag(pbp), o[3]);
    ST(1, 2);
    write_part(o);
  }
  SB;
  if (w < 4) {
    ST(2, 1);
    if (lane == 0) wait_count(ocntA, 16);
    asm volatile("" ::: "memory");
    ST(2, 2);
    const int aq = oqb * 4 + oqh * 2 + (w & 1), an = w >> 1;
    const unsigned ao = WS_O + (aq * 64) * 1024, aw = WS_W + ((nb * 2 + an) * 64) * 1024;
    f32x16 acc = {};
    u32x4 fo[2][4], fw[2][4];
#pragma unroll
    for (int e = 0; e < 4; ++e) { fo[0][e] = bload_sc1(rs, l16 + e * 1024, ao); fw[0][e] = bload(rs, l16 + e * 1024, aw); }
#pragma unroll
    for (int c = 0; c < 8; ++c) {
      if (c < 7) {
#pragma unroll
        for (int e = 0; e < 4; ++e) {
          fo[(c + 1) & 1][e] = bload_sc1(rs, l16 + e * 1024, ao + (c + 1) * 4096);
          fw[(c + 1) & 1][e] = bload(rs, l16 + e * 1024, aw + (c + 1) * 4096);
        }
      }
      SB;
#pragma unroll
      for (int e = 0; e < 4; ++e) acc = MFMA(as_frag(fw[c & 1][e]), as_frag(fo[c & 1][e]), acc);
      SB;
    }
    ST(2, 3);
    const float ga = ((const float*)(ws + WS_G))[aq * 32 + r];
#pragma unroll
    for (int g = 0; g < 4; ++g)
      apart[(w * 4 + g) * 64 + lane] = f32x4{acc[4 * g] * ga, acc[4 * g + 1] * ga, acc[4 * g + 2] * ga, acc[4 * g + 3] * ga};
  }
  loadW();
  __syncthreads();
  reduce();
  ST(1, 3);
  {
    float am = fmaxf(fmaxf(fmaxf(fabsf(a0[0]), fabsf(a0[1])), fmaxf(fabsf(a0[2]), fabsf(a0[3]))),
                     fmaxf(fmaxf(fabsf(a1[0]), fabsf(a1[1])), fmaxf(fabsf(a1[2]), fabsf(a1[3]))));
    float chk = (a0[0] + a0[1] + a0[2] + a0[3]) * 0.f + (a1[0] + a1[1] + a1[2] + a1[3]) * 0.f;
    bool bad = !(l < 1.0e30f) || !(am < 3.0e38f) || !(chk == 0.f);
    if (bad) *flag = 1;
    __syncthreads();
  }
  if (__builtin_amdgcn_readfirstlane(*flag)) {
#pragma unroll
    for (int i = 0; i < 4; ++i) set_foreign<ODD>(q[i][0], q[i][1], h, 0u, 0u);
    float m[4] = {-3.0e38f, -3.0e38f, -3.0e38f, -3.0e38f};
#pragma unroll 1
    for (int t = 0; t < 8; ++t) {
      u32x4 k0 = bload(rs, l16, koff + t * 28672), k1 = bload(rs, l16 + 1024, koff + t * 28672);
#pragma unroll
      for (int i = 0; i < 4; ++i) {
        f32x16 s = MFMA(as_frag(k0), as_frag(q[i][0]), f32x16{});
        s = MFMA(as_frag(k1), as_frag(q[i][1]), s);
#pragma unroll
        for (int j = 0; j < 16; ++j) m[i] = fmaxf(m[i], s[j]);
      }
    }
#pragma unroll
    for (int i = 0; i < 4; ++i) fold_shift(i, m[i]);
    f32x16 o[4] = {};
#pragma unroll 1
    for (int t = 0; t < 8; ++t) {
      u32x4 k0 = bload(rs, l16, koff + t * 28672), k1 = bload(rs, l16 + 1024, koff + t * 28672);
      u32x4 v0 = bload(rs, l16, voff + t * 2048), v1 = bload(rs, l16 + 1024, voff + t * 2048);
      set_foreign<ODD>(k0, k1, h, 0x3F803F80u, 0x00003F80u);
#pragma unroll
      for (int i = 0; i < 4; ++i) {
        f32x16 s = MFMA(as_frag(k0), as_frag(q[i][0]), f32x16{});
        s = MFMA(as_frag(k1), as_frag(q[i][1]), s);
#pragma unroll
        for (int j = 0; j < 16; ++j) s[j] = __builtin_amdgcn_exp2f(s[j]);
        o[i] = MFMA(as_frag(v0), as_frag(pack_step1<0>(s)), o[i]);
        o[i] = MFMA(as_frag(v1), as_frag(pack_step1<1>(s)), o[i]);
      }
    }
    write_part(o);
    __syncthreads();
    reduce();
    loadW();
  }
  {
    const float inv = 1.f / l;
    u32x4 ov;
    ov[0] = pack2(a0[0] * inv, a0[1] * inv);
    ov[1] = pack2(a0[2] * inv, a0[3] * inv);
    ov[2] = pack2(a1[0] * inv, a1[1] * inv);
    ov[3] = pack2(a1[2] * inv, a1[3] * inv);
    if (h && rs1) { ov[2] = 0; ov[3] = 0; }
    bstore_sc1(ov, rs, l16, WS_O + (((qb * 4 + ri) * 32 + 16 + head) * 2 + rs1) * 1024);
  }
  const int oqt = oqb * 4 + oqh * 2 + qt;
  const unsigned ofo = WS_O + ((oqt * 32 + 16 + 4 * kq) * 2) * 1024;
  const float gt = 1.f - ((const float*)(ws + WS_G))[oqt * 32 + r];
  asm volatile("s_waitcnt vmcnt(0)" ::: "memory");
  __syncthreads();
  ST(1, 4);
  if (tid == 0) {
    __hip_atomic_fetch_add(cntB, 1u, __ATOMIC_RELAXED, __HIP_MEMORY_SCOPE_AGENT);
    wait_count(ocntB, 16);
  }
  __syncthreads();
  ST(1, 5);
  {
    u32x4 ofr[8];
    f32x16 acc[2] = {};
#pragma unroll
    for (int e = 0; e < 8; ++e) ofr[e] = bload_sc1(rs, l16 + (e & 3) * 1024, ofo + (e >> 2) * 4096);
#pragma unroll
    for (int e = 0; e < 8; ++e)
#pragma unroll
      for (int nt = 0; nt < 2; ++nt) acc[nt] = MFMA(as_frag(wfr[nt][e]), as_frag(ofr[e]), acc[nt]);
    ST(1, 6);
#pragma unroll
    for (int nt = 0; nt < 2; ++nt)
#pragma unroll
      for (int g = 0; g < 4; ++g)
        part[(((nt * 2 + qt) * 4 + kq) * 4 + g) * 64 + lane] =
            f32x4{acc[nt][4 * g] * gt, acc[nt][4 * g + 1] * gt, acc[nt][4 * g + 2] * gt, acc[nt][4 * g + 3] * gt};
  }
  __syncthreads();
  {
    const int ft = w & 3, fg = w >> 2;
    f32x4 s0 = apart[(ft * 4 + 2 * fg) * 64 + lane], s1 = apart[(ft * 4 + 2 * fg + 1) * 64 + lane];
#pragma unroll
    for (int k = 0; k < 4; ++k) {
      s0 += part[((ft * 4 + k) * 4 + 2 * fg) * 64 + lane];
      s1 += part[((ft * 4 + k) * 4 + 2 * fg + 1) * 64 + lane];
    }
    const u32x4 ov = widen(pack2(s0[0], s0[1]), pack2(s0[2], s0[3]), pack2(s1[0], s1[1]), pack2(s1[2], s1[3]));
    const int col = 32 * (ft >> 1) + 16 * fg + 8 * h;
    if (col < 56) *(u32x4*)(out + (oqb * 128 + oqh * 64 + (ft & 1) * 32 + r) * DM + nb * 56 + col) = ov;
  }
}

__global__ __launch_bounds__(512, 2) void k2_attn(char* __restrict__ ws, u16* __restrict__ out) {
  extern __shared__ __attribute__((aligned(16))) char smem_[];
  char LDSP* smem = (char LDSP*)smem_;
  if ((blockIdx.x >> 3) & 1) k2_body<1>(ws, out, smem);
  else k2_body<0>(ws, out, smem);
}

extern "C" void kernel_launch(void* const* d_in, const int* in_sizes, int n_in, void* d_out, int out_size, void* d_ws,
                              size_t ws_size, hipStream_t stream) {
  const u16* x = (const u16*)d_in[0];
  const u16* WA = (const u16*)d_in[1];
  const u16* WoA = (const u16*)d_in[2];
  const u16* WB = (const u16*)d_in[4];
  const u16* WoB = (const u16*)d_in[5];
  const u16* gw = (const u16*)d_in[6];
  char* ws = (char*)d_ws;
  (void)hipFuncSetAttribute((const void*)k1_qkv, hipFuncAttributeMaxDynamicSharedMemorySize, K1_LDS);
  (void)hipFuncSetAttribute((const void*)k2_attn, hipFuncAttributeMaxDynamicSharedMemorySize, K2_LDS);
  k1_qkv<<<256, 512, K1_LDS, stream>>>(x, WA, WB, gw, WoA, WoB, ws);
  k2_attn<<<256, 512, K2_LDS, stream>>>(ws, (u16*)d_out);
}
```

```cpp
#include <hip/hip_runtime.h>
#include <hip/hip_bf16.h>
#include <stdint.h>
#include <type_traits>

typedef short bf16x8 __attribute__((ext_vector_type(8)));
typedef short s16x4 __attribute__((ext_vector_type(4)));
typedef float f32x16 __attribute__((ext_vector_type(16)));
typedef float f32x4 __attribute__((ext_vector_type(4)));
typedef float f32x2 __attribute__((ext_vector_type(2)));
typedef unsigned u32x4 __attribute__((ext_vector_type(4)));
typedef unsigned u32x2 __attribute__((ext_vector_type(2)));
typedef __bf16 bf2 __attribute__((ext_vector_type(2)));
typedef unsigned short u16;

#define DI __device__ __forceinline__
#define MFMA(a, b, c) __builtin_amdgcn_mfma_f32_32x32x16_bf16((a), (b), (c), 0, 0, 0)
#define LDSP __attribute__((address_space(3)))
#define GLBP __attribute__((address_space(1)))

constexpr int SEQ = 2048, DM = 448, NH = 16, DH = 28, N3 = 1344;
constexpr float QSCALE = 0.18898223650461362f * 1.4426950408889634f;

#define ST(k, i)
constexpr unsigned WS_Q = 0;
constexpr unsigned WS_K = 4u << 20;
constexpr unsigned WS_V = 8u << 20;
constexpr unsigned WS_O = 12u << 20;
constexpr unsigned WS_G = 16u << 20;
constexpr unsigned WS_W = 17u << 20;
constexpr unsigned WS_C = 19u << 20;
constexpr unsigned F1B = WS_C + 4096, F1A = WS_C + 8192, F2A = WS_C + 12288, F2B = WS_C + 16384;
constexpr unsigned QK_BR = 64 * 56 * 512;

DI unsigned pack2(float lo, float hi) {
  f32x2 v = {lo, hi};
  return __builtin_bit_cast(unsigned, __builtin_convertvector(v, bf2));
}
DI bf16x8 as_frag(u32x4 v) { return __builtin_bit_cast(bf16x8, v); }
DI float bf_lo(unsigned v) { return __uint_as_float(v << 16); }
DI float bf_hi(unsigned v) { return __uint_as_float(v & 0xffff0000u); }
DI float swap_max(float v) {
  auto rr = __builtin_amdgcn_permlane32_swap(__float_as_uint(v), __float_as_uint(v), false, false);
  return fmaxf(__uint_as_float(rr[0]), __uint_as_float(rr[1]));
}
DI s16x4 tr_read(const char LDSP* p) {
  return __builtin_amdgcn_ds_read_tr16_b64_v4i16((s16x4 LDSP*)p);
}
DI bf16x8 cat(s16x4 a, s16x4 b) { return __builtin_shufflevector(a, b, 0, 1, 2, 3, 4, 5, 6, 7); }
template <int S>
DI u32x4 pack_step(const f32x16& x, float sc) {
  u32x4 p;
  p[0] = pack2(x[8 * S + 0] * sc, x[8 * S + 1] * sc);
  p[1] = pack2(x[8 * S + 2] * sc, x[8 * S + 3] * sc);
  p[2] = pack2(x[8 * S + 4] * sc, x[8 * S + 5] * sc);
  p[3] = pack2(x[8 * S + 6] * sc, x[8 * S + 7] * sc);
  return p;
}
template <int S>
DI u32x4 pack_step1(const f32x16& x) {
  u32x4 p;
  p[0] = pack2(x[8 * S + 0], x[8 * S + 1]);
  p[1] = pack2(x[8 * S + 2], x[8 * S + 3]);
  p[2] = pack2(x[8 * S + 4], x[8 * S + 5]);
  p[3] = pack2(x[8 * S + 6], x[8 * S + 7]);
  return p;
}

#define RAWBAR() do { asm volatile("" ::: "memory"); __builtin_amdgcn_s_barrier(); asm volatile("" ::: "memory"); } while (0)
#define WAITV(n) ((((n) & 15) | (((n) >> 4) << 14)) | 0x0F70)
#define WAITVL(n) ((((n) & 15) | (((n) >> 4) << 14)) | 0x0070)
DI u32x4 widen(unsigned a0, unsigned a1, unsigned b0, unsigned b1) {
  auto r0 = __builtin_amdgcn_permlane32_swap(a0, b0, false, false);
  auto r1 = __builtin_amdgcn_permlane32_swap(a1, b1, false, false);
  return u32x4{r0[0], r1[0], r0[1], r1[1]};
}

typedef __amdgpu_buffer_rsrc_t rsrc_t;
typedef unsigned v4u_t __attribute__((ext_vector_type(4)));
typedef unsigned long long u64;
typedef GLBP u64 gu64;
DI u32x4 bload(rsrc_t rs, unsigned voff, unsigned soff) {
  return __builtin_bit_cast(u32x4, __builtin_amdgcn_raw_buffer_load_b128(rs, voff, soff, 16));
}
DI void bstore(u32x4 v, rsrc_t rs, unsigned voff, unsigned soff) {
  __builtin_amdgcn_raw_buffer_store_b128(__builtin_bit_cast(v4u_t, v), rs, voff + soff, 0, 16);
}
DI void set_flag(char* ws, unsigned off, u64 tag) {
  __hip_atomic_store((gu64*)(ws + off), tag, __ATOMIC_RELAXED, __HIP_MEMORY_SCOPE_AGENT);
}
DI u64 read_tag(const char* ws) {
  u64 e;
  asm volatile("s_load_dwordx2 %0, %1, 0x0\n\ts_waitcnt lgkmcnt(0)" : "=s"(e) : "s"(ws + WS_C) : "memory");
  return e + 1;
}
DI void wait_flags256(char* ws, unsigned off, u64 tag, int lane) {
  gu64* f = (gu64*)(ws + off) + lane;
  for (unsigned spins = 0; spins < (1u << 20); ++spins) {
    bool ok = true;
#pragma unroll
    for (int k = 0; k < 4; ++k) ok &= __hip_atomic_load(f + 64 * k, __ATOMIC_RELAXED, __HIP_MEMORY_SCOPE_AGENT) == tag;
    if (__all(ok)) break;
    __builtin_amdgcn_s_sleep(2);
  }
  asm volatile("" ::: "memory");
}
DI void wait_flags16(char* ws, unsigned off, u64 tag, int lane) {
  gu64* f = (gu64*)(ws + off) + (lane & 15);
  for (unsigned spins = 0; spins < (1u << 20); ++spins) {
    if (__all(__hip_atomic_load(f, __ATOMIC_RELAXED, __HIP_MEMORY_SCOPE_AGENT) == tag)) break;
    __builtin_amdgcn_s_sleep(1);
  }
  asm volatile("" ::: "memory");
}

constexpr int P1_X = 7 * 16384;
constexpr int P1_WSLOT = 64 * 192;
constexpr int LDS_BYTES = P1_X + 4 * P1_WSLOT;

template <bool V>
DI void p1_store(const f32x16& a, int br, int ct, int tile, int lane, rsrc_t rs) {
  const int type = ct / 14, cbase = (ct % 14) * 32;
  if (!V) {
    const float sc = type == 0 ? QSCALE : 1.f;
    const unsigned so = (type == 0 ? WS_Q : WS_K) + br * QK_BR + (tile * 56 + (cbase >> 3)) * 512;
#pragma unroll
    for (int g = 0; g < 2; ++g)
      bstore(widen(pack2(a[8 * g] * sc, a[8 * g + 1] * sc), pack2(a[8 * g + 2] * sc, a[8 * g + 3] * sc),
                   pack2(a[8 * g + 4] * sc, a[8 * g + 5] * sc), pack2(a[8 * g + 6] * sc, a[8 * g + 7] * sc)),
             rs, lane * 16 + g * 1024, so);
  } else {
    const unsigned f = cbase + (lane & 31), head = f / 28, d = f % 28;
    const unsigned vo = (head * 64 * 2 * 64 + d + (lane & 32)) * 16;
    const unsigned so = WS_V + ((br * 16 * 64 + tile) * 2) * 1024;
    bstore(pack_step1<0>(a), rs, vo, so);
    bstore(pack_step1<1>(a), rs, vo + 1024, so);
    const unsigned c = d == 0 ? 0x3F803F80u : 0u;
    const unsigned po = d < 4 ? vo + 28 * 16 : 0x80000000u;
    bstore(u32x4{c, c, c, c}, rs, po, so);
    bstore(u32x4{c, c, c, c}, rs, po + 1024, so);
  }
}

template <int NT, int NV>
DI void p1_body(const u16* __restrict__ x, const u16* __restrict__ WA, const u16* __restrict__ WB, char* __restrict__ ws, rsrc_t rs,
                int mt, int nt, char LDSP* smem, u64& tag) {
  const int tid = threadIdx.x, lane = tid & 63;
  const int w = __builtin_amdgcn_readfirstlane(tid >> 6);
  const int msub = w & 3;
  const int j0 = NT == 2 ? 0 : 2;
  const int r = lane & 31, h = lane >> 5;
  constexpr int NS = 2 * (NT - NV) + 4 * NV;
  constexpr int NW = NT == 2 ? 1 : 2;
  const int wi0 = NT == 2 ? w : 2 * w - 4;
  const u16* xs[2];
  int wso[2];
#pragma unroll
  for (int i = 0; i < 2; ++i) {
    int lr = (w * 2 + i) * 8 + (lane >> 3);
    int logical = (lane & 7) ^ ((lr >> 1) & 7);
    xs[i] = x + (mt * 128 + lr) * DM + logical * 8;
    const int id = (wi0 + i) * 64 + lane;
    wso[i] = (id / 12) * N3 + nt * 96 + (id % 12) * 8;
  }
  auto issue1 = [&](int s, int i) {
    if (i < 2) {
      const u16* W = s < 7 ? WB : WA;
      if (i < NW)
        __builtin_amdgcn_global_load_lds((const GLBP void*)(W + wso[i] + (s % 7) * 64 * N3),
                                         (LDSP void*)(smem + P1_X + (s & 3) * P1_WSLOT + (wi0 + i) * 1024), 16, 0, 0);
    } else if (s < 7) {
      __builtin_amdgcn_global_load_lds((const GLBP void*)(xs[i - 2] + s * 64), (LDSP void*)(smem + s * 16384 + (w * 2 + i - 2) * 1024), 16, 0, 0);
    }
  };
  int xoff[4];
  {
    int lr = 32 * msub + r, sw = (lr >> 1) & 7;
#pragma unroll
    for (int t = 0; t < 4; ++t) xoff[t] = lr * 128 + (((2 * t + h) ^ sw) << 4);
  }
  const int q4 = (lane & 15) >> 2, p4 = lane & 3, g16 = (lane >> 4) & 1;
  const int woff = P1_X + (8 * h + q4) * 192 + 64 * j0 + 32 * g16 + 8 * p4;

  f32x16 acc[NT] = {};
  bf16x8 xf[2][4];
  s16x4 wl[2][4][NT], wh[2][4][NT];
  auto readf = [&](int s, int m) {
    const int t = m / NT, j = m % NT;
    if (j == 0) xf[s & 1][t] = *(const bf16x8 LDSP*)(smem + (s % 7) * 16384 + xoff[t]);
    const char LDSP* wp = smem + woff + (s & 3) * P1_WSLOT + t * 16 * 192 + j * 64;
    wl[s & 1][t][j] = tr_read(wp);
    wh[s & 1][t][j] = tr_read(wp + 4 * 192);
  };
#pragma unroll
  for (int s = 0; s < 4; ++s) { issue1(s, 2); issue1(s, 3); issue1(s, 0); issue1(s, 1); }
  tag = read_tag(ws);
  __builtin_amdgcn_s_waitcnt(WAITV(3 * (NW + 2)));
  RAWBAR();
#pragma unroll
  for (int m = 0; m < 4 * NT; ++m) readf(0, m);
#pragma unroll
  for (int s = 0; s < 14; ++s) {
    if (s < 13) {
      if (s < 4) __builtin_amdgcn_s_waitcnt(WAITVL(2 * NW + 4));
      else if (s == 4) __builtin_amdgcn_s_waitcnt(WAITVL(2 * NW + 2));
      else if (s < 7 || s == 10) __builtin_amdgcn_s_waitcnt(WAITVL(2 * NW));
      else if (s < 10) __builtin_amdgcn_s_waitcnt(WAITVL(2 * NW + NS));
      else if (s == 11) __builtin_amdgcn_s_waitcnt(WAITVL(NW));
      else __builtin_amdgcn_s_waitcnt(WAITVL(0));
      RAWBAR();
      if (s == 10 && tid == 0) set_flag(ws, F1B + blockIdx.x * 8, tag);
    }
    __builtin_amdgcn_sched_barrier(0);
#pragma unroll
    for (int m = 0; m < 4 * NT; ++m) {
      const int t = m / NT, j = m % NT;
      const bf16x8 wfr = cat(wl[s & 1][t][j], wh[s & 1][t][j]);
      if (j < NT - NV) acc[j] = MFMA(wfr, xf[s & 1][t], acc[j]);
      else acc[j] = MFMA(xf[s & 1][t], wfr, acc[j]);
      __builtin_amdgcn_sched_barrier(0);
      if (s < 13) readf(s + 1, m);
      if (s + 4 < 14 && j == 0) issue1(s + 4, (t + 2) & 3);
      __builtin_amdgcn_sched_barrier(0);
    }
    if (s == 6 || s == 13) {
#pragma unroll
      for (int j = 0; j < NT; ++j) {
        if (j < NT - NV) p1_store<false>(acc[j], s == 6, nt * 3 + j0 + j, mt * 4 + msub, lane, rs);
        else p1_store<true>(acc[j], s == 6, nt * 3 + j0 + j, mt * 4 + msub, lane, rs);
        acc[j] = f32x16{};
      }
      __builtin_amdgcn_sched_barrier(0);
    }
  }
}

DI void p1_spare(const u16* __restrict__ x, const u16* __restrict__ gate_w, const u16* __restrict__ WoA, const u16* __restrict__ WoB,
                 char* __restrict__ ws, rsrc_t rs, int sp, u64& tag) {
  const int tid = threadIdx.x, lane = tid & 63, r = lane & 31, h = lane >> 5;
  tag = read_tag(ws);
  __syncthreads();
  if (tid == 0) set_flag(ws, F1B + blockIdx.x * 8, tag);
#pragma unroll
  for (int e = 0; e < 4; ++e) {
    const int fid = sp * 32 + (tid >> 6) * 4 + e;
    const int sfr = fid & 1, vh = (fid >> 1) & 31, nt = (fid >> 6) & 1, nb = fid >> 7;
    const int n = 56 * nb + 32 * nt + r;
    const u16* Wo = (vh >> 4) ? WoB : WoA;
    unsigned v[8];
#pragma unroll
    for (int jj = 0; jj < 8; ++jj) {
      const int d = 16 * sfr + 8 * (jj >> 2) + 4 * h + (jj & 3);
      const bool ok = d < DH && n < DM;
      const unsigned tv = Wo[ok ? ((vh & 15) * DH + d) * DM + n : 0];
      v[jj] = ok ? tv : 0u;
    }
    bstore(u32x4{v[0] | (v[1] << 16), v[2] | (v[3] << 16), v[4] | (v[5] << 16), v[6] | (v[7] << 16)}, rs, WS_W + fid * 1024 + lane * 16, 0);
  }
  const int row = sp * 64 + (tid >> 3), part = tid & 7;
  const u32x4* xp = (const u32x4*)(x + row * DM) + part;
  const u32x4* gp = (const u32x4*)(gate_w) + part;
  float acc = 0.f;
#pragma unroll
  for (int i = 0; i < 7; ++i) {
    u32x4 a = xp[i * 8], bb = gp[i * 8];
#pragma unroll
    for (int e = 0; e < 4; ++e) {
      acc = fmaf(bf_lo(a[e]), bf_lo(bb[e]), acc);
      acc = fmaf(bf_hi(a[e]), bf_hi(bb[e]), acc);
    }
  }
  acc += __shfl_xor(acc, 1);
  acc += __shfl_xor(acc, 2);
  acc += __shfl_xor(acc, 4);
  if (part == 0)
    __hip_atomic_store((GLBP float*)(ws + WS_G) + row, 1.f / (1.f + __expf(-acc)), __ATOMIC_RELAXED, __HIP_MEMORY_SCOPE_AGENT);
}

constexpr unsigned long long lmask(int i, bool prev) {
  unsigned long long m = 0;
  for (int l = 0; l < 64; ++l) {
    int r = l & 31, h = l >> 5;
    int kr = (i & 3) + 8 * (i >> 2) + 4 * h;
    int diff = r - kr + (prev ? 32 : 0);
    if (diff >= 0 && diff <= 14 && (diff & 1) == 0) m |= 1ull << l;
  }
  return m;
}
DI float sel_mask(float val, float other, unsigned long long mask) {
  return __builtin_amdgcn_inverse_ballot_w64(mask) ? val : other;
}
template <int ODD>
DI void set_foreign(u32x4& f0, u32x4& f1, int h, unsigned a, unsigned b) {
  if (ODD) { if (!h) { f0[0] = a; f0[1] = b; } }
  else { if (h) { f1[2] = a; f1[3] = b; } }
}
DI float gload(char* ws, int idx) { return __hip_atomic_load((GLBP float*)(ws + WS_G) + idx, __ATOMIC_RELAXED, __HIP_MEMORY_SCOPE_AGENT); }

template <int ODD>
DI void p2_body(char* __restrict__ ws, u16* __restrict__ out, rsrc_t rs, char LDSP* smem, const u64 tag) {
  f32x4 LDSP* part = (f32x4 LDSP*)smem;
  float LDSP* cw = (float LDSP*)(smem + 131072);
  f32x4 LDSP* apart = (f32x4 LDSP*)(smem + 131072 + 4096);
  volatile int LDSP* ctl = (volatile int LDSP*)(smem + 131072 + 4096 + 16384);
  const int tid = threadIdx.x, lane = tid & 63;
  const int w = __builtin_amdgcn_readfirstlane(tid >> 6);
  const int r = lane & 31, h = lane >> 5;
  const int b = blockIdx.x;
  const int head = (b & 7) * 2 + ODD, qb = b >> 4;
  const int oqb = (b & 7) * 2 + (b >> 7), oqh = (b >> 3) & 1, nb = (b >> 4) & 7;
  const unsigned p0 = (7 * head) >> 1;
  const unsigned l16 = lane * 16;
  const unsigned qoff = WS_Q + QK_BR + (qb * 4 * 56 + p0) * 512;
  const unsigned koff = WS_K + QK_BR + (w * 8 * 56 + p0) * 512;
  const unsigned voff = WS_V + ((16 + head) * 64 + w * 8) * 2048;
  auto lds_arrive = [&](int idx, int last, unsigned flag_off) {
    if (lane == 0) {
      if (__hip_atomic_fetch_add((int LDSP*)(ctl + idx), 1, __ATOMIC_RELAXED, __HIP_MEMORY_SCOPE_WORKGROUP) == last) set_flag(ws, flag_off, tag);
    }
  };
  __syncthreads();
  if (w == 0) {
    if (lane < 4) ctl[lane] = 0;
    wait_flags256(ws, F1B, tag, lane);
    if (b == 0 && lane == 0) set_flag(ws, WS_C, tag);
  }
  __syncthreads();
  ST(1, 0);
  ST(2, 0);
  u32x4 q[4][2];
  u32x4 kf[4][2], vf[4][2];
  auto loadkv = [&](int t) {
    if (t >= 4 && w >= 4) {
      const u32x4 LDSP* sp = (const u32x4 LDSP*)(smem + w * 16384 + (t - 4) * 4096) + lane;
      kf[t & 3][0] = sp[0]; kf[t & 3][1] = sp[64]; vf[t & 3][0] = sp[128]; vf[t & 3][1] = sp[192];
    } else {
      kf[t & 3][0] = bload(rs, l16, koff + t * 28672);
      kf[t & 3][1] = bload(rs, l16 + 1024, koff + t * 28672);
      vf[t & 3][0] = bload(rs, l16, voff + t * 2048);
      vf[t & 3][1] = bload(rs, l16 + 1024, voff + t * 2048);
    }
  };
  if (w >= 4) {
    __builtin_amdgcn_s_setprio(2);
    {
      u32x4 st[16];
#pragma unroll
      for (int e = 0; e < 16; ++e) {
        const int t = 4 + (e >> 2), f = e & 3;
        st[e] = bload(rs, l16 + (f & 1) * 1024, f < 2 ? koff + t * 28672 : voff + t * 2048);
      }
#pragma unroll
      for (int e = 0; e < 16; ++e) ((u32x4 LDSP*)(smem + w * 16384))[e * 64 + lane] = st[e];
    }
    asm volatile("s_waitcnt vmcnt(0)" ::: "memory");
    lds_arrive(2, 7, F1A + b * 8);
    if (w == 4) {
      wait_flags256(ws, F1A, tag, lane);
      if (lane == 0) ctl[3] = 1;
    } else {
      for (unsigned spins = 0; spins < (1u << 24) && ctl[3] == 0; ++spins) __builtin_amdgcn_s_sleep(2);
      asm volatile("" ::: "memory");
    }
    const int ltile = qb * 4 + (w & 3);
    const unsigned lqo = WS_Q + (ltile * 56 + p0) * 512, lko = WS_K + (ltile * 56 + p0) * 512, lvo = WS_V + (head * 64 + ltile) * 2048;
    const unsigned pd = ltile > 0 ? 1 : 0;
    u32x4 lq0 = bload(rs, l16, lqo), lq1 = bload(rs, l16 + 1024, lqo);
    u32x4 lk0 = bload(rs, l16, lko), lk1 = bload(rs, l16 + 1024, lko);
    u32x4 pk0 = bload(rs, l16, lko - pd * 28672), pk1 = bload(rs, l16 + 1024, lko - pd * 28672);
    u32x4 lv0 = bload(rs, l16, lvo), lv1 = bload(rs, l16 + 1024, lvo);
    u32x4 pv1 = bload(rs, l16 + 1024, lvo - pd * 2048);
    set_foreign<ODD>(lq0, lq1, h, 0u, 0u);
    f32x16 sp = MFMA(as_frag(pk0), as_frag(lq0), f32x16{});
    sp = MFMA(as_frag(pk1), as_frag(lq1), sp);
    f32x16 sc = MFMA(as_frag(lk0), as_frag(lq0), f32x16{});
    sc = MFMA(as_frag(lk1), as_frag(lq1), sc);
    const bool first = (ltile == 0);
    const float NEG = -3.0e38f;
    float m = NEG;
#define LM_C(i) { sc[i] = sel_mask(sc[i], NEG, lmask(i, false)); m = fmaxf(m, sc[i]); }
#define LM_P(i) { sp[i] = sel_mask(sp[i], NEG, first ? 0ull : lmask(i, true)); m = fmaxf(m, sp[i]); }
    LM_C(0) LM_C(1) LM_C(2) LM_C(3) LM_C(4) LM_C(5) LM_C(6) LM_C(7)
    LM_C(8) LM_C(9) LM_C(10) LM_C(11) LM_C(12) LM_C(13) LM_C(14) LM_C(15)
    LM_P(8) LM_P(9) LM_P(10) LM_P(11) LM_P(12) LM_P(13) LM_P(14) LM_P(15)
#undef LM_C
#undef LM_P
    m = swap_max(m);
#pragma unroll
    for (int i = 0; i < 16; ++i) sc[i] = __builtin_amdgcn_exp2f(sc[i] - m);
#pragma unroll
    for (int i = 8; i < 16; ++i) sp[i] = __builtin_amdgcn_exp2f(sp[i] - m);
    f32x16 o = MFMA(as_frag(pv1), as_frag(pack_step1<1>(sp)), f32x16{});
    o = MFMA(as_frag(lv0), as_frag(pack_step1<0>(sc)), o);
    o = MFMA(as_frag(lv1), as_frag(pack_step1<1>(sc)), o);
    float ll = __shfl(o[12], 32 + r);
    float inv = 1.f / ll;
    u32x4 o0 = pack_step<0>(o, inv), o1 = pack_step<1>(o, inv);
    if (h) { o1[2] = 0; o1[3] = 0; }
    const unsigned oo = WS_O + ((ltile * 32 + head) * 2) * 1024;
    bstore(o0, rs, l16, oo);
    bstore(o1, rs, l16 + 1024, oo);
    asm volatile("s_waitcnt vmcnt(0)" ::: "memory");
    lds_arrive(1, 3, F2A + (qb * 16 + head) * 8);
    __builtin_amdgcn_s_setprio(0);
  }
  kf[0][0] = bload(rs, l16, koff);
  kf[0][1] = bload(rs, l16 + 1024, koff);
#pragma unroll
  for (int i = 0; i < 4; ++i) { q[i][0] = bload(rs, l16, qoff + i * 28672); q[i][1] = bload(rs, l16 + 1024, qoff + i * 28672); }
  vf[0][0] = bload(rs, l16, voff);
  vf[0][1] = bload(rs, l16 + 1024, voff);
  loadkv(1);
  loadkv(2);

  auto fold_shift = [&](int i, float m) {
    m = swap_max(m);
    if (!h) cw[(i * 32 + r) * 8 + w] = m;
    const float c = -m;
    unsigned p01 = pack2(c, 0.f);
    float r1 = c - bf_lo(p01);
    unsigned p1 = pack2(r1, 0.f);
    float r2 = r1 - bf_lo(p1);
    unsigned p2 = pack2(r2, 0.f);
    set_foreign<ODD>(q[i][0], q[i][1], h, (p01 & 0xffffu) | (p1 << 16), p2 & 0xffffu);
  };
  const int ri = w >> 1, rs1 = w & 1;
  f32x4 a0, a1;
  float l;
  auto write_part = [&](f32x16 (&o)[4]) {
#pragma unroll
    for (int i = 0; i < 4; ++i)
#pragma unroll
      for (int g = 0; g < 4; ++g)
        part[((w * 4 + i) * 4 + g) * 64 + lane] = f32x4{o[i][4 * g], o[i][4 * g + 1], o[i][4 * g + 2], o[i][4 * g + 3]};
  };
  auto reduce = [&]() {
    const f32x4 c0 = *(f32x4 LDSP*)(cw + (ri * 32 + r) * 8), c1 = *(f32x4 LDSP*)(cw + (ri * 32 + r) * 8 + 4);
    const float cm = fmaxf(fmaxf(fmaxf(c0[0], c0[1]), fmaxf(c0[2], c0[3])), fmaxf(fmaxf(c1[0], c1[1]), fmaxf(c1[2], c1[3])));
    a0 = f32x4{}; a1 = f32x4{}; l = 0.f;
#pragma unroll
    for (int ww = 0; ww < 8; ++ww) {
      const float wt = __builtin_amdgcn_exp2f((ww < 4 ? c0[ww & 3] : c1[ww & 3]) - cm);
      a0 += part[((ww * 4 + ri) * 4 + 2 * rs1) * 64 + lane] * wt;
      a1 += part[((ww * 4 + ri) * 4 + 2 * rs1 + 1) * 64 + lane] * wt;
      l += ((float LDSP*)(part + ((ww * 4 + ri) * 4 + 3) * 64 + 32 + r))[0] * wt;
    }
  };
  const int qt = w & 1, kq = w >> 1;
  u32x4 wfr[2][8];
  auto loadW = [&]() {
#pragma unroll
    for (int nt = 0; nt < 2; ++nt) {
      const unsigned wo = WS_W + (((nb * 2 + nt) * 32 + 16 + 4 * kq) * 2) * 1024;
#pragma unroll
      for (int e = 0; e < 8; ++e) wfr[nt][e] = bload(rs, l16 + (e & 3) * 1024, wo + (e >> 2) * 4096);
    }
  };

#pragma unroll
  for (int i = 0; i < 4; ++i) {
    set_foreign<ODD>(q[i][0], q[i][1], h, 0u, 0u);
    f32x16 s = MFMA(as_frag(kf[0][0]), as_frag(q[i][0]), f32x16{});
    s = MFMA(as_frag(kf[0][1]), as_frag(q[i][1]), s);
    float m = s[0];
#pragma unroll
    for (int j = 1; j < 16; ++j) m = fmaxf(m, s[j]);
    fold_shift(i, m);
  }
  if (w < 4) {
    asm volatile("s_waitcnt vmcnt(10)" ::: "memory");
    lds_arrive(2, 7, F1A + b * 8);
  }
  ST(1, 1);
  {
    f32x16 o[4] = {};
    set_foreign<ODD>(kf[0][0], kf[0][1], h, 0x3F803F80u, 0x00003F80u);
    f32x16 sa = MFMA(as_frag(kf[0][0]), as_frag(q[0][0]), f32x16{});
    sa = MFMA(as_frag(kf[0][1]), as_frag(q[0][1]), sa);
    f32x16 sb;
    u32x4 pa;
    u32x4 pbp;
    float e3[4];
#define SB __builtin_amdgcn_sched_barrier(0)
#define EXPQ(S, g) { S[4 * g] = __builtin_amdgcn_exp2f(S[4 * g]); S[4 * g + 1] = __builtin_amdgcn_exp2f(S[4 * g + 1]); \
                     S[4 * g + 2] = __builtin_amdgcn_exp2f(S[4 * g + 2]); S[4 * g + 3] = __builtin_amdgcn_exp2f(S[4 * g + 3]); }
    auto step = [&](auto nc, f32x16& sc, f32x16& sn) {
      constexpr int n = decltype(nc)::value;
      constexpr int t = n >> 2, i = n & 3;
      constexpr int t1 = ((n + 1) >> 2) & 3, i1 = (n + 1) & 3;
      constexpr int tp = n > 0 ? ((n - 1) >> 2) & 3 : 0, ip = n > 0 ? (n - 1) & 3 : 0;
      if ((n & 7) == 0 && n > 0 && w >= 4) ST(2, n >> 3);
      if (i == 1 && t + 3 < 8) loadkv(t + 3);
      if (i == 3 && n + 1 < 32) set_foreign<ODD>(kf[t1][0], kf[t1][1], h, 0x3F803F80u, 0x00003F80u);
      SB;
      if (n + 1 < 32) sn = MFMA(as_frag(kf[t1][0]), as_frag(q[i1][0]), f32x16{});
      SB;
      if (n > 0) { pbp[2] = pack2(e3[0], e3[1]); pbp[3] = pack2(e3[2], e3[3]); }
      EXPQ(sc, 0);
      SB;
      if (n + 1 < 32) sn = MFMA(as_frag(kf[t1][1]), as_frag(q[i1][1]), sn);
      SB;
      pa[0] = pack2(sc[0], sc[1]); pa[1] = pack2(sc[2], sc[3]);
      EXPQ(sc, 1);
      SB;
      if (n > 0) o[ip] = MFMA(as_frag(vf[tp][1]), as_frag(pbp), o[ip]);
      SB;
      pa[2] = pack2(sc[4], sc[5]); pa[3] = pack2(sc[6], sc[7]);
      EXPQ(sc, 2);
      SB;
      o[i] = MFMA(as_frag(vf[t & 3][0]), as_frag(pa), o[i]);
      SB;
      pbp[0] = pack2(sc[8], sc[9]); pbp[1] = pack2(sc[10], sc[11]);
      EXPQ(sc, 3);
      e3[0] = sc[12]; e3[1] = sc[13]; e3[2] = sc[14]; e3[3] = sc[15];
      SB;
    };
#define STEP2(n) step(std::integral_constant<int, n>{}, sa, sb); step(std::integral_constant<int, n + 1>{}, sb, sa);
    STEP2(0) STEP2(2) STEP2(4) STEP2(6) STEP2(8) STEP2(10) STEP2(12) STEP2(14)
    STEP2(16) STEP2(18) STEP2(20) STEP2(22) STEP2(24) STEP2(26) STEP2(28) STEP2(30)
#undef STEP2
    pbp[2] = pack2(e3[0], e3[1]); pbp[3] = pack2(e3[2], e3[3]);
    o[3] = MFMA(as_frag(vf[3][1]), as_frag(pbp), o[3]);
    ST(1, 2);
    write_part(o);
  }
  SB;
  if (w < 4) {
    ST(2, 1);
    for (unsigned spins = 0; spins < (1u << 24) && ctl[3] == 0; ++spins) __builtin_amdgcn_s_sleep(2);
    wait_flags16(ws, F2A + oqb * 128, tag, lane);
    ST(2, 2);
    const int aq = oqb * 4 + oqh * 2 + (w & 1), an = w >> 1;
    const unsigned ao = WS_O + (aq * 64) * 1024, aw = WS_W + ((nb * 2 + an) * 64) * 1024;
    f32x16 acc = {};
    u32x4 fo[2][4], fw[2][4];
#pragma unroll
    for (int e = 0; e < 4; ++e) { fo[0][e] = bload(rs, l16 + e * 1024, ao); fw[0][e] = bload(rs, l16 + e * 1024, aw); }
#pragma unroll
    for (int c = 0; c < 8; ++c) {
      if (c < 7) {
#pragma unroll
        for (int e = 0; e < 4; ++e) {
          fo[(c + 1) & 1][e] = bload(rs, l16 + e * 1024, ao + (c + 1) * 4096);
          fw[(c + 1) & 1][e] = bload(rs, l16 + e * 1024, aw + (c + 1) * 4096);
        }
      }
      SB;
#pragma unroll
      for (int e = 0; e < 4; ++e) acc = MFMA(as_frag(fw[c & 1][e]), as_frag(fo[c & 1][e]), acc);
      SB;
    }
    ST(2, 3);
    const float ga = gload(ws, aq * 32 + r);
#pragma unroll
    for (int g = 0; g < 4; ++g)
      apart[(w * 4 + g) * 64 + lane] = f32x4{acc[4 * g] * ga, acc[4 * g + 1] * ga, acc[4 * g + 2] * ga, acc[4 * g + 3] * ga};
  }
  loadW();
  __syncthreads();
  reduce();
  ST(1, 3);
  {
    float am = fmaxf(fmaxf(fmaxf(fabsf(a0[0]), fabsf(a0[1])), fmaxf(fabsf(a0[2]), fabsf(a0[3]))),
                     fmaxf(fmaxf(fabsf(a1[0]), fabsf(a1[1])), fmaxf(fabsf(a1[2]), fabsf(a1[3]))));
    float chk = (a0[0] + a0[1] + a0[2] + a0[3]) * 0.f + (a1[0] + a1[1] + a1[2] + a1[3]) * 0.f;
    bool bad = !(l < 1.0e30f) || !(am < 3.0e38f) || !(chk == 0.f);
    if (bad) ctl[0] = 1;
    __syncthreads();
  }
  if (__builtin_amdgcn_readfirstlane(ctl[0])) {
#pragma unroll
    for (int i = 0; i < 4; ++i) set_foreign<ODD>(q[i][0], q[i][1], h, 0u, 0u);
    float m[4] = {-3.0e38f, -3.0e38f, -3.0e38f, -3.0e38f};
#pragma unroll 1
    for (int t = 0; t < 8; ++t) {
      u32x4 k0 = bload(rs, l16, koff + t * 28672), k1 = bload(rs, l16 + 1024, koff + t * 28672);
#pragma unroll
      for (int i = 0; i < 4; ++i) {
        f32x16 s = MFMA(as_frag(k0), as_frag(q[i][0]), f32x16{});
        s = MFMA(as_frag(k1), as_frag(q[i][1]), s);
#pragma unroll
        for (int j = 0; j < 16; ++j) m[i] = fmaxf(m[i], s[j]);
      }
    }
#pragma unroll
    for (int i = 0; i < 4; ++i) fold_shift(i, m[i]);
    f32x16 o[4] = {};
#pragma unroll 1
    for (int t = 0; t < 8; ++t) {
      u32x4 k0 = bload(rs, l16, koff + t * 28672), k1 = bload(rs, l16 + 1024, koff + t * 28672);
      u32x4 v0 = bload(rs, l16, voff + t * 2048), v1 = bload(rs, l16 + 1024, voff + t * 2048);
      set_foreign<ODD>(k0, k1, h, 0x3F803F80u, 0x00003F80u);
#pragma unroll
      for (int i = 0; i < 4; ++i) {
        f32x16 s = MFMA(as_frag(k0), as_frag(q[i][0]), f32x16{});
        s = MFMA(as_frag(k1), as_frag(q[i][1]), s);
#pragma unroll
        for (int j = 0; j < 16; ++j) s[j] = __builtin_amdgcn_exp2f(s[j]);
        o[i] = MFMA(as_frag(v0), as_frag(pack_step1<0>(s)), o[i]);
        o[i] = MFMA(as_frag(v1), as_frag(pack_step1<1>(s)), o[i]);
      }
    }
    write_part(o);
    __syncthreads();
    reduce();
    loadW();
  }
  {
    const float inv = 1.f / l;
    u32x4 ov;
    ov[0] = pack2(a0[0] * inv, a0[1] * inv);
    ov[1] = pack2(a0[2] * inv, a0[3] * inv);
    ov[2] = pack2(a1[0] * inv, a1[1] * inv);
    ov[3] = pack2(a1[2] * inv, a1[3] * inv);
    if (h && rs1) { ov[2] = 0; ov[3] = 0; }
    bstore(ov, rs, l16, WS_O + (((qb * 4 + ri) * 32 + 16 + head) * 2 + rs1) * 1024);
  }
  const int oqt = oqb * 4 + oqh * 2 + qt;
  const unsigned ofo = WS_O + ((oqt * 32 + 16 + 4 * kq) * 2) * 1024;
  const float gt = 1.f - gload(ws, oqt * 32 + r);
  asm volatile("s_waitcnt vmcnt(0)" ::: "memory");
  __syncthreads();
  ST(1, 4);
  if (w == 0) {
    if (lane == 0) set_flag(ws, F2B + (qb * 16 + head) * 8, tag);
    wait_flags16(ws, F2B + oqb * 128, tag, lane);
  }
  __syncthreads();
  ST(1, 5);
  {
    u32x4 ofr[8];
    f32x16 acc[2] = {};
#pragma unroll
    for (int e = 0; e < 8; ++e) ofr[e] = bload(rs, l16 + (e & 3) * 1024, ofo + (e >> 2) * 4096);
#pragma unroll
    for (int e = 0; e < 8; ++e)
#pragma unroll
      for (int nt = 0; nt < 2; ++nt) acc[nt] = MFMA(as_frag(wfr[nt][e]), as_frag(ofr[e]), acc[nt]);
    ST(1, 6);
#pragma unroll
    for (int nt = 0; nt < 2; ++nt)
#pragma unroll
      for (int g = 0; g < 4; ++g)
        part[(((nt * 2 + qt) * 4 + kq) * 4 + g) * 64 + lane] =
            f32x4{acc[nt][4 * g] * gt, acc[nt][4 * g + 1] * gt, acc[nt][4 * g + 2] * gt, acc[nt][4 * g + 3] * gt};
  }
  __syncthreads();
  {
    const int ft = w & 3, fg = w >> 2;
    f32x4 s0 = apart[(ft * 4 + 2 * fg) * 64 + lane], s1 = apart[(ft * 4 + 2 * fg + 1) * 64 + lane];
#pragma unroll
    for (int k = 0; k < 4; ++k) {
      s0 += part[((ft * 4 + k) * 4 + 2 * fg) * 64 + lane];
      s1 += part[((ft * 4 + k) * 4 + 2 * fg + 1) * 64 + lane];
    }
    const u32x4 ov = widen(pack2(s0[0], s0[1]), pack2(s0[2], s0[3]), pack2(s1[0], s1[1]), pack2(s1[2], s1[3]));
    const int col = 32 * (ft >> 1) + 16 * fg + 8 * h;
    if (col < 56) *(u32x4*)(out + (oqb * 128 + oqh * 64 + (ft & 1) * 32 + r) * DM + nb * 56 + col) = ov;
  }
}

__global__ __launch_bounds__(512, 2) void fused_block(const u16* __restrict__ x, const u16* __restrict__ WA, const u16* __restrict__ WB,
                                                      const u16* __restrict__ gate_w, const u16* __restrict__ WoA,
                                                      const u16* __restrict__ WoB, char* __restrict__ ws, u16* __restrict__ out) {
  extern __shared__ __attribute__((aligned(16))) char smem_[];
  char LDSP* smem = (char LDSP*)smem_;
  const rsrc_t rs = __builtin_amdgcn_make_buffer_rsrc(ws, 0, 32u << 20, 0x00020000);
  ST(0, 0);
  u64 tag;
  const int b = blockIdx.x, xcd = b & 7, j = b >> 3;
  if (j < 28) {
    const int mt = 4 * (xcd & 3) + (j & 3);
    const int nt = 7 * (xcd >> 2) + (j >> 2);
    if (__builtin_amdgcn_readfirstlane(threadIdx.x >> 8) == 0) {
      if (nt < 9) p1_body<2, 0>(x, WA, WB, ws, rs, mt, nt, smem, tag);
      else if (nt == 9) p1_body<2, 1>(x, WA, WB, ws, rs, mt, nt, smem, tag);
      else p1_body<2, 2>(x, WA, WB, ws, rs, mt, nt, smem, tag);
    } else {
      if (nt < 9) p1_body<1, 0>(x, WA, WB, ws, rs, mt, nt, smem, tag);
      else p1_body<1, 1>(x, WA, WB, ws, rs, mt, nt, smem, tag);
    }
  } else {
    p1_spare(x, gate_w, WoA, WoB, ws, rs, xcd * 4 + j - 28, tag);
  }
  ST(0, 1);
  if ((b >> 3) & 1) p2_body<1>(ws, out, rs, smem, tag);
  else p2_body<0>(ws, out, rs, smem, tag);
}

extern "C" void kernel_launch(void* const* d_in, const int* in_sizes, int n_in, void* d_out, int out_size, void* d_ws,
                              size_t ws_size, hipStream_t stream) {
  const u16* x = (const u16*)d_in[0];
  const u16* WA = (const u16*)d_in[1];
  const u16* WoA = (const u16*)d_in[2];
  const u16* WB = (const u16*)d_in[4];
  const u16* WoB = (const u16*)d_in[5];
  const u16* gw = (const u16*)d_in[6];
  (void)hipFuncSetAttribute((const void*)fused_block, hipFuncAttributeMaxDynamicSharedMemorySize, LDS_BYTES);
  fused_block<<<256, 512, LDS_BYTES, stream>>>(x, WA, WB, gw, WoA, WoB, (char*)d_ws, (u16*)d_out);
}
```

```cpp
#include <hip/hip_runtime.h>
#include <hip/hip_bf16.h>
#include <stdint.h>
#include <type_traits>

typedef short bf16x8 __attribute__((ext_vector_type(8)));
typedef short s16x4 __attribute__((ext_vector_type(4)));
typedef float f32x16 __attribute__((ext_vector_type(16)));
typedef float f32x4 __attribute__((ext_vector_type(4)));
typedef float f32x2 __attribute__((ext_vector_type(2)));
typedef unsigned u32x4 __attribute__((ext_vector_type(4)));
typedef unsigned u32x2 __attribute__((ext_vector_type(2)));
typedef __bf16 bf2 __attribute__((ext_vector_type(2)));
typedef unsigned short u16;

#define DI __device__ __forceinline__
#define MFMA(a, b, c) __builtin_amdgcn_mfma_f32_32x32x16_bf16((a), (b), (c), 0, 0, 0)
#define LDSP __attribute__((address_space(3)))
#define GLBP __attribute__((address_space(1)))

constexpr int SEQ = 2048, DM = 448, NH = 16, DH = 28, N3 = 1344;
constexpr float QSCALE = 0.18898223650461362f * 1.4426950408889634f;

#define ST(k, i)
constexpr unsigned WS_Q = 0;
constexpr unsigned WS_K = 4u << 20;
constexpr unsigned WS_V = 8u << 20;
constexpr unsigned WS_O = 12u << 20;
constexpr unsigned WS_G = 16u << 20;
constexpr unsigned WS_W = 17u << 20;
constexpr unsigned WS_C = 19u << 20;
constexpr unsigned F1B = WS_C + 4096, F1A = WS_C + 8192, F2A = WS_C + 12288, F2B = WS_C + 16384;
constexpr unsigned QK_BR = 64 * 56 * 512;

DI unsigned pack2(float lo, float hi) {
  f32x2 v = {lo, hi};
  return __builtin_bit_cast(unsigned, __builtin_convertvector(v, bf2));
}
DI bf16x8 as_frag(u32x4 v) { return __builtin_bit_cast(bf16x8, v); }
DI float bf_lo(unsigned v) { return __uint_as_float(v << 16); }
DI float bf_hi(unsigned v) { return __uint_as_float(v & 0xffff0000u); }
DI float swap_max(float v) {
  auto rr = __builtin_amdgcn_permlane32_swap(__float_as_uint(v), __float_as_uint(v), false, false);
  return fmaxf(__uint_as_float(rr[0]), __uint_as_float(rr[1]));
}
DI s16x4 tr_read(const char LDSP* p) {
  return __builtin_amdgcn_ds_read_tr16_b64_v4i16((s16x4 LDSP*)p);
}
DI bf16x8 cat(s16x4 a, s16x4 b) { return __builtin_shufflevector(a, b, 0, 1, 2, 3, 4, 5, 6, 7); }
template <int S>
DI u32x4 pack_step(const f32x16& x, float sc) {
  u32x4 p;
  p[0] = pack2(x[8 * S + 0] * sc, x[8 * S + 1] * sc);
  p[1] = pack2(x[8 * S + 2] * sc, x[8 * S + 3] * sc);
  p[2] = pack2(x[8 * S + 4] * sc, x[8 * S + 5] * sc);
  p[3] = pack2(x[8 * S + 6] * sc, x[8 * S + 7] * sc);
  return p;
}
template <int S>
DI u32x4 pack_step1(const f32x16& x) {
  u32x4 p;
  p[0] = pack2(x[8 * S + 0], x[8 * S + 1]);
  p[1] = pack2(x[8 * S + 2], x[8 * S + 3]);
  p[2] = pack2(x[8 * S + 4], x[8 * S + 5]);
  p[3] = pack2(x[8 * S + 6], x[8 * S + 7]);
  return p;
}

#define RAWBAR() do { asm volatile("" ::: "memory"); __builtin_amdgcn_s_barrier(); asm volatile("" ::: "memory"); } while (0)
#define WAITV(n) ((((n) & 15) | (((n) >> 4) << 14)) | 0x0F70)
#define WAITVL(n) ((((n) & 15) | (((n) >> 4) << 14)) | 0x0070)
DI u32x4 widen(unsigned a0, unsigned a1, unsigned b0, unsigned b1) {
  auto r0 = __builtin_amdgcn_permlane32_swap(a0, b0, false, false);
  auto r1 = __builtin_amdgcn_permlane32_swap(a1, b1, false, false);
  return u32x4{r0[0], r1[0], r0[1], r1[1]};
}

typedef __amdgpu_buffer_rsrc_t rsrc_t;
typedef unsigned v4u_t __attribute__((ext_vector_type(4)));
typedef unsigned long long u64;
typedef GLBP u64 gu64;
DI u32x4 bload(rsrc_t rs, unsigned voff, unsigned soff) {
  return __builtin_bit_cast(u32x4, __builtin_amdgcn_raw_buffer_load_b128(rs, voff, soff, 16));
}
DI void bstore(u32x4 v, rsrc_t rs, unsigned voff, unsigned soff) {
  __builtin_amdgcn_raw_buffer_store_b128(__builtin_bit_cast(v4u_t, v), rs, voff + soff, 0, 16);
}
DI void set_flag(char* ws, unsigned off, u64 tag) {
  __hip_atomic_store((gu64*)(ws + off), tag, __ATOMIC_RELAXED, __HIP_MEMORY_SCOPE_AGENT);
}
DI u64 read_tag(const char* ws) {
  u64 e;
  asm volatile("s_load_dwordx2 %0, %1, 0x0\n\ts_waitcnt lgkmcnt(0)" : "=s"(e) : "s"(ws + WS_C) : "memory");
  return e + 1;
}
DI void wait_flags256(char* ws, unsigned off, u64 tag, int lane) {
  gu64* f = (gu64*)(ws + off) + lane;
  for (unsigned spins = 0; spins < (1u << 20); ++spins) {
    bool ok = true;
#pragma unroll
    for (int k = 0; k < 4; ++k) ok &= __hip_atomic_load(f + 64 * k, __ATOMIC_RELAXED, __HIP_MEMORY_SCOPE_AGENT) == tag;
    if (__all(ok)) break;
    __builtin_amdgcn_s_sleep(2);
  }
  asm volatile("" ::: "memory");
}
DI void wait_flags16(char* ws, unsigned off, u64 tag, int lane) {
  gu64* f = (gu64*)(ws + off) + (lane & 15);
  for (unsigned spins = 0; spins < (1u << 20); ++spins) {
    if (__all(__hip_atomic_load(f, __ATOMIC_RELAXED, __HIP_MEMORY_SCOPE_AGENT) == tag)) break;
    __builtin_amdgcn_s_sleep(1);
  }
  asm volatile("" ::: "memory");
}

constexpr int P1_WSLOT = 64 * 192;
constexpr int P1_WR = 3 * 16384;
constexpr int P1_WA = P1_WR + 3 * P1_WSLOT;
constexpr int P1_CTL = P1_WA + 6 * P1_WSLOT;
constexpr int P1_SINK = P1_CTL + 64;
constexpr int LDS_BYTES = P1_SINK + 256;

constexpr unsigned DROP = 0x80000000u;
template <bool V>
DI void p1_pack(const f32x16& a, int br, int ct, int tile, int lane, u32x4* sv, unsigned* so) {
  const int type = ct / 14, cbase = (ct % 14) * 32;
  if (!V) {
    const float sc = type == 0 ? QSCALE : 1.f;
    const unsigned o = (type == 0 ? WS_Q : WS_K) + br * QK_BR + (tile * 56 + (cbase >> 3)) * 512 + lane * 16;
#pragma unroll
    for (int g = 0; g < 2; ++g) {
      sv[g] = widen(pack2(a[8 * g] * sc, a[8 * g + 1] * sc), pack2(a[8 * g + 2] * sc, a[8 * g + 3] * sc),
                    pack2(a[8 * g + 4] * sc, a[8 * g + 5] * sc), pack2(a[8 * g + 6] * sc, a[8 * g + 7] * sc));
      so[g] = o + g * 1024;
    }
  } else {
    const unsigned f = cbase + (lane & 31), head = f / 28, d = f % 28;
    const unsigned o = WS_V + ((br * 16 * 64 + tile) * 2) * 1024 + (head * 64 * 2 * 64 + d + (lane & 32)) * 16;
    sv[0] = pack_step1<0>(a); so[0] = o;
    sv[1] = pack_step1<1>(a); so[1] = o + 1024;
    const unsigned c = d == 0 ? 0x3F803F80u : 0u;
    sv[2] = u32x4{c, c, c, c}; so[2] = d < 4 ? o + 28 * 16 : DROP;
    sv[3] = sv[2]; so[3] = d < 4 ? o + 28 * 16 + 1024 : DROP;
  }
}

DI void warm_l2(const u16* x, const u16* WA, const u16* WB, char LDSP* smem) {
  const int b = blockIdx.x, xcd = b & 7;
  const unsigned g = (b >> 3) * 512 + threadIdx.x;
  const char* p;
  if (g < 3584) {
    p = (const char*)x + ((xcd & 3) * 512 * 7 + g) * 128;
  } else {
    const unsigned idx = g - 3584, row2 = idx / 12, c = idx % 12;
    unsigned off = (row2 >> 1) * 2688 + (xcd >> 2) * 1280 + c * 128;
    off = off < (unsigned)(DM * N3 * 2 - 4) ? off : 0u;
    p = (const char*)((row2 & 1) ? WA : WB) + off;
  }
  __builtin_amdgcn_global_load_lds((const GLBP void*)p, (LDSP void*)(smem + P1_SINK), 4, 0, 0);
  const u64 pc = __builtin_amdgcn_s_getpc();
  constexpr unsigned len = 60 * 1024;
  const unsigned co = ((g >> 9) * 32 + (g & 31)) * 128;
  if ((threadIdx.x >> 6) == 0)
    __builtin_amdgcn_global_load_lds((const GLBP void*)((const char*)pc + (co + 4 <= len ? co : 0u)), (LDSP void*)(smem + P1_SINK), 4, 0, 0);
}

DI void lds_arrive(char LDSP* smem, int idx, int last, char* ws, unsigned flag_off, u64 tag, int lane) {
  if (lane == 0) {
    if (__hip_atomic_fetch_add((int LDSP*)(smem + P1_CTL) + idx, 1, __ATOMIC_RELAXED, __HIP_MEMORY_SCOPE_WORKGROUP) == last) set_flag(ws, flag_off, tag);
  }
}

template <int NT, int NV>
DI void p1_body(const u16* __restrict__ x, const u16* __restrict__ WA, const u16* __restrict__ WB, char* __restrict__ ws, rsrc_t rs,
                int mt, int nt, char LDSP* smem, u64& tag, u32x4 (&sv)[8], unsigned (&so)[8]) {
  const int tid = threadIdx.x, lane = tid & 63;
  const int w = __builtin_amdgcn_readfirstlane(tid >> 6);
  const int msub = w & 3;
  const int j0 = NT == 2 ? 0 : 2;
  const int r = lane & 31, h = lane >> 5;
  constexpr int NS = 2 * (NT - NV) + 4 * NV;
  constexpr int NW = NT == 2 ? 1 : 2;
  const int wi0 = NT == 2 ? w : 2 * w - 4;
  const u16* xs[2];
  int wso[2];
#pragma unroll
  for (int i = 0; i < 2; ++i) {
    int lr = (w * 2 + i) * 8 + (lane >> 3);
    int logical = (lane & 7) ^ ((lr >> 1) & 7);
    xs[i] = x + (mt * 128 + lr) * DM + logical * 8;
    const int id = (wi0 + i) * 64 + lane;
    wso[i] = (id / 12) * N3 + nt * 96 + (id % 12) * 8;
  }
  auto wbase = [&](int s) { return s < 7 ? P1_WR + (s % 3) * P1_WSLOT : (s < 13 ? P1_WA + (s - 7) * P1_WSLOT : 16384); };
  auto issue_x = [&](int c, int i) {
    __builtin_amdgcn_global_load_lds((const GLBP void*)(xs[i] + c * 64), (LDSP void*)(smem + (c % 3) * 16384 + (w * 2 + i) * 1024), 16, 0, 0);
  };
  auto issue_w = [&](int s) {
#pragma unroll
    for (int i = 0; i < NW; ++i)
      __builtin_amdgcn_global_load_lds((const GLBP void*)((s < 7 ? WB : WA) + wso[i] + (s % 7) * 64 * N3),
                                       (LDSP void*)(smem + wbase(s) + (wi0 + i) * 1024), 16, 0, 0);
  };
  int xoff[4];
  {
    int lr = 32 * msub + r, sw = (lr >> 1) & 7;
#pragma unroll
    for (int t = 0; t < 4; ++t) xoff[t] = lr * 128 + (((2 * t + h) ^ sw) << 4);
  }
  const int q4 = (lane & 15) >> 2, p4 = lane & 3, g16 = (lane >> 4) & 1;
  const int woff = (8 * h + q4) * 192 + 64 * j0 + 32 * g16 + 8 * p4;

  f32x16 acc[NT] = {};
  bf16x8 xf[7][4];
  s16x4 wl[2][4][NT], wh[2][4][NT];
  auto readf = [&](int s, int m) {
    const int t = m / NT, j = m % NT;
    if (j == 0 && s < 7) xf[s][t] = *(const bf16x8 LDSP*)(smem + (s % 3) * 16384 + xoff[t]);
    const char LDSP* wp = smem + woff + wbase(s) + t * 16 * 192 + j * 64;
    wl[s & 1][t][j] = tr_read(wp);
    wh[s & 1][t][j] = tr_read(wp + 4 * 192);
  };
#pragma unroll
  for (int c = 0; c < 3; ++c) { issue_x(c, 0); issue_x(c, 1); issue_w(c); }
  tag = read_tag(ws);
  if (tid < 8) ((int LDSP*)(smem + P1_CTL))[tid] = 0;
  __builtin_amdgcn_s_waitcnt(WAITVL(2 * (NW + 2)));
  RAWBAR();
#pragma unroll
  for (int m = 0; m < 4 * NT; ++m) readf(0, m);
#pragma unroll
  for (int s = 0; s < 14; ++s) {
    if (s < 6 || s == 9) {
      if (s == 0) __builtin_amdgcn_s_waitcnt(WAITVL(NW + 2));
      else if (s == 1) __builtin_amdgcn_s_waitcnt(WAITVL(2 * NW + 2));
      else if (s < 5) __builtin_amdgcn_s_waitcnt(WAITVL(3 * NW + 2));
      else if (s == 5) __builtin_amdgcn_s_waitcnt(WAITVL(2 * NW));
      else __builtin_amdgcn_s_waitcnt(WAITVL(NS));
      RAWBAR();
    }
    if (s == 12) {
      __builtin_amdgcn_s_waitcnt(WAITV(0));
      asm volatile("" ::: "memory");
      lds_arrive(smem, 4, 7, ws, F1B + (mt * 16 + nt) * 8, tag, lane);
    }
    __builtin_amdgcn_sched_barrier(0);
#pragma unroll
    for (int m = 0; m < 4 * NT; ++m) {
      const int t = m / NT, j = m % NT;
      const bf16x8 wfr = cat(wl[s & 1][t][j], wh[s & 1][t][j]);
      if (j < NT - NV) acc[j] = MFMA(wfr, xf[s % 7][t], acc[j]);
      else acc[j] = MFMA(xf[s % 7][t], wfr, acc[j]);
      __builtin_amdgcn_sched_barrier(0);
      if (s < 13) readf(s + 1, m);
      if (j == 0) {
        if (s + 3 < 7) { if (t < 2) issue_x(s + 3, t); else if (t == 2) issue_w(s + 3); }
        if (s < 7 && t == 3) issue_w(s + 7);
      }
      __builtin_amdgcn_sched_barrier(0);
    }
    if (s == 6 || s == 13) {
#pragma unroll
      for (int e = 0; e < 8; ++e) so[e] = DROP;
#pragma unroll
      for (int j = 0; j < NT; ++j) {
        if (j < NT - NV) p1_pack<false>(acc[j], s == 6, nt * 3 + j0 + j, mt * 4 + msub, lane, sv + 4 * j, so + 4 * j);
        else p1_pack<true>(acc[j], s == 6, nt * 3 + j0 + j, mt * 4 + msub, lane, sv + 4 * j, so + 4 * j);
        if (s == 6) {
#pragma unroll
          for (int e = 0; e < (j < NT - NV ? 2 : 4); ++e) bstore(sv[4 * j + e], rs, so[4 * j + e], 0);
        }
        acc[j] = f32x16{};
      }
      __builtin_amdgcn_sched_barrier(0);
    }
  }
}

DI void p1_spare(const u16* __restrict__ x, const u16* __restrict__ gate_w, const u16* __restrict__ WoA, const u16* __restrict__ WoB,
                 char* __restrict__ ws, rsrc_t rs, int sp, char LDSP* smem, u64& tag) {
  const int tid = threadIdx.x, lane = tid & 63, r = lane & 31, h = lane >> 5;
  tag = read_tag(ws);
  if (tid < 8) ((int LDSP*)(smem + P1_CTL))[tid] = 0;
  __syncthreads();
  if (tid == 0) set_flag(ws, F1B + ((sp >> 1) * 16 + 14 + (sp & 1)) * 8, tag);
#pragma unroll
  for (int e = 0; e < 4; ++e) {
    const int fid = sp * 32 + (tid >> 6) * 4 + e;
    const int sfr = fid & 1, vh = (fid >> 1) & 31, nt = (fid >> 6) & 1, nb = fid >> 7;
    const int n = 56 * nb + 32 * nt + r;
    const u16* Wo = (vh >> 4) ? WoB : WoA;
    unsigned v[8];
#pragma unroll
    for (int jj = 0; jj < 8; ++jj) {
      const int d = 16 * sfr + 8 * (jj >> 2) + 4 * h + (jj & 3);
      const bool ok = d < DH && n < DM;
      const unsigned tv = Wo[ok ? ((vh & 15) * DH + d) * DM + n : 0];
      v[jj] = ok ? tv : 0u;
    }
    bstore(u32x4{v[0] | (v[1] << 16), v[2] | (v[3] << 16), v[4] | (v[5] << 16), v[6] | (v[7] << 16)}, rs, WS_W + fid * 1024 + lane * 16, 0);
  }
  const int row = sp * 64 + (tid >> 3), part = tid & 7;
  const u32x4* xp = (const u32x4*)(x + row * DM) + part;
  const u32x4* gp = (const u32x4*)(gate_w) + part;
  float acc = 0.f;
#pragma unroll
  for (int i = 0; i < 7; ++i) {
    u32x4 a = xp[i * 8], bb = gp[i * 8];
#pragma unroll
    for (int e = 0; e < 4; ++e) {
      acc = fmaf(bf_lo(a[e]), bf_lo(bb[e]), acc);
      acc = fmaf(bf_hi(a[e]), bf_hi(bb[e]), acc);
    }
  }
  acc += __shfl_xor(acc, 1);
  acc += __shfl_xor(acc, 2);
  acc += __shfl_xor(acc, 4);
  if (part == 0)
    __hip_atomic_store((GLBP float*)(ws + WS_G) + row, 1.f / (1.f + __expf(-acc)), __ATOMIC_RELAXED, __HIP_MEMORY_SCOPE_AGENT);
}

constexpr unsigned long long lmask(int i, bool prev) {
  unsigned long long m = 0;
  for (int l = 0; l < 64; ++l) {
    int r = l & 31, h = l >> 5;
    int kr = (i & 3) + 8 * (i >> 2) + 4 * h;
    int diff = r - kr + (prev ? 32 : 0);
    if (diff >= 0 && diff <= 14 && (diff & 1) == 0) m |= 1ull << l;
  }
  return m;
}
DI float sel_mask(float val, float other, unsigned long long mask) {
  return __builtin_amdgcn_inverse_ballot_w64(mask) ? val : other;
}
template <int ODD>
DI void set_foreign(u32x4& f0, u32x4& f1, int h, unsigned a, unsigned b) {
  if (ODD) { if (!h) { f0[0] = a; f0[1] = b; } }
  else { if (h) { f1[2] = a; f1[3] = b; } }
}
DI float gload(char* ws, int idx) { return __hip_atomic_load((GLBP float*)(ws + WS_G) + idx, __ATOMIC_RELAXED, __HIP_MEMORY_SCOPE_AGENT); }

template <int ODD>
DI void p2_body(char* __restrict__ ws, u16* __restrict__ out, rsrc_t rs, char LDSP* smem, const u64 tag, const int fidx,
                const u32x4 (&sv)[8], const unsigned (&so)[8]) {
  f32x4 LDSP* part = (f32x4 LDSP*)smem;
  float LDSP* cw = (float LDSP*)(smem + 131072);
  f32x4 LDSP* apart = (f32x4 LDSP*)(smem + 131072 + 4096);
  volatile int LDSP* ctl = (volatile int LDSP*)(smem + P1_CTL);
  const int tid = threadIdx.x, lane = tid & 63;
  const int w = __builtin_amdgcn_readfirstlane(tid >> 6);
  const int r = lane & 31, h = lane >> 5;
  const int b = blockIdx.x;
  const int head = (b & 7) * 2 + ODD, qb = b >> 4;
  const int oqb = (b & 7) * 2 + (b >> 7), oqh = (b >> 3) & 1, nb = (b >> 4) & 7;
  const unsigned p0 = (7 * head) >> 1;
  const unsigned l16 = lane * 16;
  const unsigned qoff = WS_Q + QK_BR + (qb * 4 * 56 + p0) * 512;
  const unsigned koff = WS_K + QK_BR + (w * 8 * 56 + p0) * 512;
  const unsigned voff = WS_V + ((16 + head) * 64 + w * 8) * 2048;
  if (w == 0) {
    wait_flags256(ws, F1B, tag, lane);
    if (b == 0 && lane == 0) set_flag(ws, WS_C, tag);
  }
  __syncthreads();
  ST(1, 0);
  ST(2, 0);
  u32x4 q[4][2];
  u32x4 kf[4][2], vf[4][2];
  auto loadkv = [&](int t) {
    if (t >= 4 && w >= 4) {
      const u32x4 LDSP* sp = (const u32x4 LDSP*)(smem + w * 16384 + (t - 4) * 4096) + lane;
      kf[t & 3][0] = sp[0]; kf[t & 3][1] = sp[64]; vf[t & 3][0] = sp[128]; vf[t & 3][1] = sp[192];
    } else {
      kf[t & 3][0] = bload(rs, l16, koff + t * 28672);
      kf[t & 3][1] = bload(rs, l16 + 1024, koff + t * 28672);
      vf[t & 3][0] = bload(rs, l16, voff + t * 2048);
      vf[t & 3][1] = bload(rs, l16 + 1024, voff + t * 2048);
    }
  };
  auto store_a = [&]() {
#pragma unroll
    for (int e = 0; e < 8; ++e) bstore(sv[e], rs, so[e], 0);
  };
  if (w >= 4) {
    store_a();
    __builtin_amdgcn_s_sleep(32);
    u32x4 st[16];
#pragma unroll
    for (int e = 0; e < 16; ++e) {
      const int t = 4 + (e >> 2), f = e & 3;
      st[e] = bload(rs, l16 + (f & 1) * 1024, f < 2 ? koff + t * 28672 : voff + t * 2048);
    }
#pragma unroll
    for (int e = 0; e < 16; ++e) ((u32x4 LDSP*)(smem + w * 16384))[e * 64 + lane] = st[e];
    asm volatile("s_waitcnt vmcnt(0)" ::: "memory");
    lds_arrive(smem, 2, 7, ws, F1A + fidx * 8, tag, lane);
    __builtin_amdgcn_s_setprio(1);
  }
  kf[0][0] = bload(rs, l16, koff);
  kf[0][1] = bload(rs, l16 + 1024, koff);
#pragma unroll
  for (int i = 0; i < 4; ++i) { q[i][0] = bload(rs, l16, qoff + i * 28672); q[i][1] = bload(rs, l16 + 1024, qoff + i * 28672); }
  vf[0][0] = bload(rs, l16, voff);
  vf[0][1] = bload(rs, l16 + 1024, voff);
  loadkv(1);
  loadkv(2);
  if (w < 4) store_a();
  __builtin_amdgcn_sched_barrier(0);

  auto fold_shift = [&](int i, float m) {
    m = swap_max(m);
    if (!h) cw[(i * 32 + r) * 8 + w] = m;
    const float c = -m;
    unsigned p01 = pack2(c, 0.f);
    float r1 = c - bf_lo(p01);
    unsigned p1 = pack2(r1, 0.f);
    float r2 = r1 - bf_lo(p1);
    unsigned p2 = pack2(r2, 0.f);
    set_foreign<ODD>(q[i][0], q[i][1], h, (p01 & 0xffffu) | (p1 << 16), p2 & 0xffffu);
  };
  const int ri = w >> 1, rs1 = w & 1;
  f32x4 a0, a1;
  float l;
  auto write_part = [&](f32x16 (&o)[4]) {
#pragma unroll
    for (int i = 0; i < 4; ++i)
#pragma unroll
      for (int g = 0; g < 4; ++g)
        part[((w * 4 + i) * 4 + g) * 64 + lane] = f32x4{o[i][4 * g], o[i][4 * g + 1], o[i][4 * g + 2], o[i][4 * g + 3]};
  };
  auto reduce = [&]() {
    const f32x4 c0 = *(f32x4 LDSP*)(cw + (ri * 32 + r) * 8), c1 = *(f32x4 LDSP*)(cw + (ri * 32 + r) * 8 + 4);
    const float cm = fmaxf(fmaxf(fmaxf(c0[0], c0[1]), fmaxf(c0[2], c0[3])), fmaxf(fmaxf(c1[0], c1[1]), fmaxf(c1[2], c1[3])));
    a0 = f32x4{}; a1 = f32x4{}; l = 0.f;
#pragma unroll
    for (int ww = 0; ww < 8; ++ww) {
      const float wt = __builtin_amdgcn_exp2f((ww < 4 ? c0[ww & 3] : c1[ww & 3]) - cm);
      a0 += part[((ww * 4 + ri) * 4 + 2 * rs1) * 64 + lane] * wt;
      a1 += part[((ww * 4 + ri) * 4 + 2 * rs1 + 1) * 64 + lane] * wt;
      l += ((float LDSP*)(part + ((ww * 4 + ri) * 4 + 3) * 64 + 32 + r))[0] * wt;
    }
  };
  const int qt = w & 1, kq = w >> 1;
  u32x4 wfr[2][8];
  auto loadW = [&]() {
#pragma unroll
    for (int nt = 0; nt < 2; ++nt) {
      const unsigned wo = WS_W + (((nb * 2 + nt) * 32 + 16 + 4 * kq) * 2) * 1024;
#pragma unroll
      for (int e = 0; e < 8; ++e) wfr[nt][e] = bload(rs, l16 + (e & 3) * 1024, wo + (e >> 2) * 4096);
    }
  };

#pragma unroll
  for (int i = 0; i < 4; ++i) {
    set_foreign<ODD>(q[i][0], q[i][1], h, 0u, 0u);
    f32x16 s = MFMA(as_frag(kf[0][0]), as_frag(q[i][0]), f32x16{});
    s = MFMA(as_frag(kf[0][1]), as_frag(q[i][1]), s);
    float m = s[0];
#pragma unroll
    for (int j = 1; j < 16; ++j) m = fmaxf(m, s[j]);
    fold_shift(i, m);
  }
  if (w >= 4) {
    ST(2, 4);
    {
      gu64* f = (gu64*)(ws + F1A) + (qb > 0 ? qb - 1 : 0) * 16 + (lane & 31) - ((lane & 15) >= 14 ? 2 : 0);
      for (unsigned spins = 0; spins < (1u << 20); ++spins) {
        if (__all(__hip_atomic_load(f, __ATOMIC_RELAXED, __HIP_MEMORY_SCOPE_AGENT) == tag)) break;
        __builtin_amdgcn_s_sleep(2);
      }
      asm volatile("" ::: "memory");
    const int ltile = qb * 4 + (w & 3);
    const unsigned lqo = WS_Q + (ltile * 56 + p0) * 512, lko = WS_K + (ltile * 56 + p0) * 512, lvo = WS_V + (head * 64 + ltile) * 2048;
    const unsigned pd = ltile > 0 ? 1 : 0;
    u32x4 lq0 = bload(rs, l16, lqo), lq1 = bload(rs, l16 + 1024, lqo);
    u32x4 lk0 = bload(rs, l16, lko), lk1 = bload(rs, l16 + 1024, lko);
    u32x4 pk0 = bload(rs, l16, lko - pd * 28672), pk1 = bload(rs, l16 + 1024, lko - pd * 28672);
    u32x4 lv0 = bload(rs, l16, lvo), lv1 = bload(rs, l16 + 1024, lvo);
    u32x4 pv1 = bload(rs, l16 + 1024, lvo - pd * 2048);
    set_foreign<ODD>(lq0, lq1, h, 0u, 0u);
    f32x16 sp = MFMA(as_frag(pk0), as_frag(lq0), f32x16{});
    sp = MFMA(as_frag(pk1), as_frag(lq1), sp);
    f32x16 sc = MFMA(as_frag(lk0), as_frag(lq0), f32x16{});
    sc = MFMA(as_frag(lk1), as_frag(lq1), sc);
    const bool first = (ltile == 0);
    const float NEG = -3.0e38f;
    float m = NEG;
#define LM_C(i) { sc[i] = sel_mask(sc[i], NEG, lmask(i, false)); m = fmaxf(m, sc[i]); }
#define LM_P(i) { sp[i] = sel_mask(sp[i], NEG, first ? 0ull : lmask(i, true)); m = fmaxf(m, sp[i]); }
    LM_C(0) LM_C(1) LM_C(2) LM_C(3) LM_C(4) LM_C(5) LM_C(6) LM_C(7)
    LM_C(8) LM_C(9) LM_C(10) LM_C(11) LM_C(12) LM_C(13) LM_C(14) LM_C(15)
    LM_P(8) LM_P(9) LM_P(10) LM_P(11) LM_P(12) LM_P(13) LM_P(14) LM_P(15)
#undef LM_C
#undef LM_P
    m = swap_max(m);
#pragma unroll
    for (int i = 0; i < 16; ++i) sc[i] = __builtin_amdgcn_exp2f(sc[i] - m);
#pragma unroll
    for (int i = 8; i < 16; ++i) sp[i] = __builtin_amdgcn_exp2f(sp[i] - m);
    f32x16 o = MFMA(as_frag(pv1), as_frag(pack_step1<1>(sp)), f32x16{});
    o = MFMA(as_frag(lv0), as_frag(pack_step1<0>(sc)), o);
    o = MFMA(as_frag(lv1), as_frag(pack_step1<1>(sc)), o);
    float ll = __shfl(o[12], 32 + r);
    float inv = 1.f / ll;
    u32x4 o0 = pack_step<0>(o, inv), o1 = pack_step<1>(o, inv);
    if (h) { o1[2] = 0; o1[3] = 0; }
    const unsigned oo = WS_O + ((ltile * 32 + head) * 2) * 1024;
    bstore(o0, rs, l16, oo);
    bstore(o1, rs, l16 + 1024, oo);
    asm volatile("s_waitcnt vmcnt(0)" ::: "memory");
    lds_arrive(smem, 1, 3, ws, F2A + (qb * 16 + head) * 8, tag, lane);
    }
    ST(2, 5);
  }
  __builtin_amdgcn_s_setprio(0);
  ST(1, 1);
  {
    f32x16 o[4] = {};
    set_foreign<ODD>(kf[0][0], kf[0][1], h, 0x3F803F80u, 0x00003F80u);
    f32x16 sa = MFMA(as_frag(kf[0][0]), as_frag(q[0][0]), f32x16{});
    sa = MFMA(as_frag(kf[0][1]), as_frag(q[0][1]), sa);
    f32x16 sb;
    u32x4 pa;
    u32x4 pbp;
    float e3[4];
#define SB __builtin_amdgcn_sched_barrier(0)
#define EXPQ(S, g) { S[4 * g] = __builtin_amdgcn_exp2f(S[4 * g]); S[4 * g + 1] = __builtin_amdgcn_exp2f(S[4 * g + 1]); \
                     S[4 * g + 2] = __builtin_amdgcn_exp2f(S[4 * g + 2]); S[4 * g + 3] = __builtin_amdgcn_exp2f(S[4 * g + 3]); }
    auto step = [&](auto nc, f32x16& sc, f32x16& sn) {
      constexpr int n = decltype(nc)::value;
      constexpr int t = n >> 2, i = n & 3;
      constexpr int t1 = ((n + 1) >> 2) & 3, i1 = (n + 1) & 3;
      constexpr int tp = n > 0 ? ((n - 1) >> 2) & 3 : 0, ip = n > 0 ? (n - 1) & 3 : 0;
      if ((n & 7) == 0 && n > 0) ST(2, n >> 3);
      if (n == 8 && w < 4) {
        asm volatile("s_waitcnt vmcnt(8)" ::: "memory");
        lds_arrive(smem, 2, 7, ws, F1A + fidx * 8, tag, lane);
      }
      if (i == 1 && t + 3 < 8) loadkv(t + 3);
      if (i == 3 && n + 1 < 32) set_foreign<ODD>(kf[t1][0], kf[t1][1], h, 0x3F803F80u, 0x00003F80u);
      SB;
      if (n + 1 < 32) sn = MFMA(as_frag(kf[t1][0]), as_frag(q[i1][0]), f32x16{});
      SB;
      if (n > 0) { pbp[2] = pack2(e3[0], e3[1]); pbp[3] = pack2(e3[2], e3[3]); }
      EXPQ(sc, 0);
      SB;
      if (n + 1 < 32) sn = MFMA(as_frag(kf[t1][1]), as_frag(q[i1][1]), sn);
      SB;
      pa[0] = pack2(sc[0], sc[1]); pa[1] = pack2(sc[2], sc[3]);
      EXPQ(sc, 1);
      SB;
      if (n > 0) o[ip] = MFMA(as_frag(vf[tp][1]), as_frag(pbp), o[ip]);
      SB;
      pa[2] = pack2(sc[4], sc[5]); pa[3] = pack2(sc[6], sc[7]);
      EXPQ(sc, 2);
      SB;
      o[i] = MFMA(as_frag(vf[t & 3][0]), as_frag(pa), o[i]);
      SB;
      pbp[0] = pack2(sc[8], sc[9]); pbp[1] = pack2(sc[10], sc[11]);
      EXPQ(sc, 3);
      e3[0] = sc[12]; e3[1] = sc[13]; e3[2] = sc[14]; e3[3] = sc[15];
      SB;
    };
#define STEP2(n) step(std::integral_constant<int, n>{}, sa, sb); step(std::integral_constant<int, n + 1>{}, sb, sa);
    STEP2(0) STEP2(2) STEP2(4) STEP2(6) STEP2(8) STEP2(10) STEP2(12) STEP2(14)
    STEP2(16) STEP2(18) STEP2(20) STEP2(22) STEP2(24) STEP2(26) STEP2(28) STEP2(30)
#undef STEP2
    pbp[2] = pack2(e3[0], e3[1]); pbp[3] = pack2(e3[2], e3[3]);
    o[3] = MFMA(as_frag(vf[3][1]), as_frag(pbp), o[3]);
    ST(1, 2);
    write_part(o);
  }
  SB;
  if (w < 4) {
    ST(2, 4);
    {
      gu64* f = lane < 32 ? (gu64*)(ws + F1A) + (lane >> 1) * 16 + 14 + (lane & 1) : (gu64*)(ws + F2A) + oqb * 16 + (lane & 15);
      for (unsigned spins = 0; spins < (1u << 20); ++spins) {
        if (__all(__hip_atomic_load(f, __ATOMIC_RELAXED, __HIP_MEMORY_SCOPE_AGENT) == tag)) break;
        __builtin_amdgcn_s_sleep(2);
      }
      asm volatile("" ::: "memory");
    }
    ST(2, 5);
    const int aq = oqb * 4 + oqh * 2 + (w & 1), an = w >> 1;
    const unsigned ao = WS_O + (aq * 64) * 1024, aw = WS_W + ((nb * 2 + an) * 64) * 1024;
    f32x16 acc = {};
    u32x4 fo[2][4], fw[2][4];
#pragma unroll
    for (int e = 0; e < 4; ++e) { fo[0][e] = bload(rs, l16 + e * 1024, ao); fw[0][e] = bload(rs, l16 + e * 1024, aw); }
#pragma unroll
    for (int c = 0; c < 8; ++c) {
      if (c < 7) {
#pragma unroll
        for (int e = 0; e < 4; ++e) {
          fo[(c + 1) & 1][e] = bload(rs, l16 + e * 1024, ao + (c + 1) * 4096);
          fw[(c + 1) & 1][e] = bload(rs, l16 + e * 1024, aw + (c + 1) * 4096);
        }
      }
      SB;
#pragma unroll
      for (int e = 0; e < 4; ++e) acc = MFMA(as_frag(fw[c & 1][e]), as_frag(fo[c & 1][e]), acc);
      SB;
    }
    ST(2, 6);
    const float ga = gload(ws, aq * 32 + r);
#pragma unroll
    for (int g = 0; g < 4; ++g)
      apart[(w * 4 + g) * 64 + lane] = f32x4{acc[4 * g] * ga, acc[4 * g + 1] * ga, acc[4 * g + 2] * ga, acc[4 * g + 3] * ga};
  }
  __syncthreads();
  loadW();
  reduce();
  ST(1, 3);
  {
    float am = fmaxf(fmaxf(fmaxf(fabsf(a0[0]), fabsf(a0[1])), fmaxf(fabsf(a0[2]), fabsf(a0[3]))),
                     fmaxf(fmaxf(fabsf(a1[0]), fabsf(a1[1])), fmaxf(fabsf(a1[2]), fabsf(a1[3]))));
    float chk = (a0[0] + a0[1] + a0[2] + a0[3]) * 0.f + (a1[0] + a1[1] + a1[2] + a1[3]) * 0.f;
    bool bad = !(l < 1.0e30f) || !(am < 3.0e38f) || !(chk == 0.f);
    if (bad) ctl[0] = 1;
    __syncthreads();
  }
  if (__builtin_amdgcn_readfirstlane(ctl[0])) {
#pragma unroll
    for (int i = 0; i < 4; ++i) set_foreign<ODD>(q[i][0], q[i][1], h, 0u, 0u);
    float m[4] = {-3.0e38f, -3.0e38f, -3.0e38f, -3.0e38f};
#pragma unroll 1
    for (int t = 0; t < 8; ++t) {
      u32x4 k0 = bload(rs, l16, koff + t * 28672), k1 = bload(rs, l16 + 1024, koff + t * 28672);
#pragma unroll
      for (int i = 0; i < 4; ++i) {
        f32x16 s = MFMA(as_frag(k0), as_frag(q[i][0]), f32x16{});
        s = MFMA(as_frag(k1), as_frag(q[i][1]), s);
#pragma unroll
        for (int j = 0; j < 16; ++j) m[i] = fmaxf(m[i], s[j]);
      }
    }
#pragma unroll
    for (int i = 0; i < 4; ++i) fold_shift(i, m[i]);
    f32x16 o[4] = {};
#pragma unroll 1
    for (int t = 0; t < 8; ++t) {
      u32x4 k0 = bload(rs, l16, koff + t * 28672), k1 = bload(rs, l16 + 1024, koff + t * 28672);
      u32x4 v0 = bload(rs, l16, voff + t * 2048), v1 = bload(rs, l16 + 1024, voff + t * 2048);
      set_foreign<ODD>(k0, k1, h, 0x3F803F80u, 0x00003F80u);
#pragma unroll
      for (int i = 0; i < 4; ++i) {
        f32x16 s = MFMA(as_frag(k0), as_frag(q[i][0]), f32x16{});
        s = MFMA(as_frag(k1), as_frag(q[i][1]), s);
#pragma unroll
        for (int j = 0; j < 16; ++j) s[j] = __builtin_amdgcn_exp2f(s[j]);
        o[i] = MFMA(as_frag(v0), as_frag(pack_step1<0>(s)), o[i]);
        o[i] = MFMA(as_frag(v1), as_frag(pack_step1<1>(s)), o[i]);
      }
    }
    write_part(o);
    __syncthreads();
    reduce();
    loadW();
  }
  {
    const float inv = 1.f / l;
    u32x4 ov;
    ov[0] = pack2(a0[0] * inv, a0[1] * inv);
    ov[1] = pack2(a0[2] * inv, a0[3] * inv);
    ov[2] = pack2(a1[0] * inv, a1[1] * inv);
    ov[3] = pack2(a1[2] * inv, a1[3] * inv);
    if (h && rs1) { ov[2] = 0; ov[3] = 0; }
    bstore(ov, rs, l16, WS_O + (((qb * 4 + ri) * 32 + 16 + head) * 2 + rs1) * 1024);
  }
  const int oqt = oqb * 4 + oqh * 2 + qt;
  const unsigned ofo = WS_O + ((oqt * 32 + 16 + 4 * kq) * 2) * 1024;
  const float gt = 1.f - gload(ws, oqt * 32 + r);
  asm volatile("s_waitcnt vmcnt(0)" ::: "memory");
  __syncthreads();
  ST(1, 4);
  if (w == 0) {
    if (lane == 0) set_flag(ws, F2B + (qb * 16 + head) * 8, tag);
    wait_flags16(ws, F2B + oqb * 128, tag, lane);
  }
  __syncthreads();
  ST(1, 5);
  {
    u32x4 ofr[8];
    f32x16 acc[2] = {};
#pragma unroll
    for (int e = 0; e < 8; ++e) ofr[e] = bload(rs, l16 + (e & 3) * 1024, ofo + (e >> 2) * 4096);
#pragma unroll
    for (int e = 0; e < 8; ++e)
#pragma unroll
      for (int nt = 0; nt < 2; ++nt) acc[nt] = MFMA(as_frag(wfr[nt][e]), as_frag(ofr[e]), acc[nt]);
    ST(1, 6);
#pragma unroll
    for (int nt = 0; nt < 2; ++nt)
#pragma unroll
      for (int g = 0; g < 4; ++g)
        part[(((nt * 2 + qt) * 4 + kq) * 4 + g) * 64 + lane] =
            f32x4{acc[nt][4 * g] * gt, acc[nt][4 * g + 1] * gt, acc[nt][4 * g + 2] * gt, acc[nt][4 * g + 3] * gt};
  }
  __syncthreads();
  {
    const int ft = w & 3, fg = w >> 2;
    f32x4 s0 = apart[(ft * 4 + 2 * fg) * 64 + lane], s1 = apart[(ft * 4 + 2 * fg + 1) * 64 + lane];
#pragma unroll
    for (int k = 0; k < 4; ++k) {
      s0 += part[((ft * 4 + k) * 4 + 2 * fg) * 64 + lane];
      s1 += part[((ft * 4 + k) * 4 + 2 * fg + 1) * 64 + lane];
    }
    const u32x4 ov = widen(pack2(s0[0], s0[1]), pack2(s0[2], s0[3]), pack2(s1[0], s1[1]), pack2(s1[2], s1[3]));
    const int col = 32 * (ft >> 1) + 16 * fg + 8 * h;
    if (col < 56) *(u32x4*)(out + (oqb * 128 + oqh * 64 + (ft & 1) * 32 + r) * DM + nb * 56 + col) = ov;
  }
}

__global__ __launch_bounds__(512, 2) void fused_block(const u16* __restrict__ x, const u16* __restrict__ WA, const u16* __restrict__ WB,
                                                      const u16* __restrict__ gate_w, const u16* __restrict__ WoA,
                                                      const u16* __restrict__ WoB, char* __restrict__ ws, u16* __restrict__ out) {
  extern __shared__ __attribute__((aligned(16))) char smem_[];
  char LDSP* smem = (char LDSP*)smem_;
  const rsrc_t rs = __builtin_amdgcn_make_buffer_rsrc(ws, 0, 32u << 20, 0x00020000);
  ST(0, 0);
  warm_l2(x, WA, WB, smem);
  u64 tag;
  u32x4 sv[8];
  unsigned so[8];
  const int b = blockIdx.x, xcd = b & 7, j = b >> 3;
  if (j < 28) {
    const int mt = 4 * (xcd & 3) + (j & 3);
    const int nt = 7 * (xcd >> 2) + (j >> 2);
    if (__builtin_amdgcn_readfirstlane(threadIdx.x >> 8) == 0) {
      if (nt < 9) p1_body<2, 0>(x, WA, WB, ws, rs, mt, nt, smem, tag, sv, so);
      else if (nt == 9) p1_body<2, 1>(x, WA, WB, ws, rs, mt, nt, smem, tag, sv, so);
      else p1_body<2, 2>(x, WA, WB, ws, rs, mt, nt, smem, tag, sv, so);
    } else {
      if (nt < 9) p1_body<1, 0>(x, WA, WB, ws, rs, mt, nt, smem, tag, sv, so);
      else p1_body<1, 1>(x, WA, WB, ws, rs, mt, nt, smem, tag, sv, so);
    }
  } else {
    p1_spare(x, gate_w, WoA, WoB, ws, rs, xcd * 4 + j - 28, smem, tag);
#pragma unroll
    for (int e = 0; e < 8; ++e) { sv[e] = u32x4{}; so[e] = DROP; }
  }
  ST(0, 1);
  const int sp = xcd * 4 + j - 28;
  const int fidx = j < 28 ? (4 * (xcd & 3) + (j & 3)) * 16 + 7 * (xcd >> 2) + (j >> 2) : (sp >> 1) * 16 + 14 + (sp & 1);
  if ((b >> 3) & 1) p2_body<1>(ws, out, rs, smem, tag, fidx, sv, so);
  else p2_body<0>(ws, out, rs, smem, tag, fidx, sv, so);
}

extern "C" void kernel_launch(void* const* d_in, const int* in_sizes, int n_in, void* d_out, int out_size, void* d_ws,
                              size_t ws_size, hipStream_t stream) {
  const u16* x = (const u16*)d_in[0];
  const u16* WA = (const u16*)d_in[1];
  const u16* WoA = (const u16*)d_in[2];
  const u16* WB = (const u16*)d_in[4];
  const u16* WoB = (const u16*)d_in[5];
  const u16* gw = (const u16*)d_in[6];
  (void)hipFuncSetAttribute((const void*)fused_block, hipFuncAttributeMaxDynamicSharedMemorySize, LDS_BYTES);
  fused_block<<<256, 512, LDS_BYTES, stream>>>(x, WA, WB, gw, WoA, WoB, (char*)d_ws, (u16*)d_out);
}
```

```cpp
#include <hip/hip_runtime.h>
#include <hip/hip_bf16.h>
#include <stdint.h>
#include <type_traits>

typedef short bf16x8 __attribute__((ext_vector_type(8)));
typedef short s16x4 __attribute__((ext_vector_type(4)));
typedef float f32x16 __attribute__((ext_vector_type(16)));
typedef float f32x4 __attribute__((ext_vector_type(4)));
typedef float f32x2 __attribute__((ext_vector_type(2)));
typedef unsigned u32x4 __attribute__((ext_vector_type(4)));
typedef unsigned u32x2 __attribute__((ext_vector_type(2)));
typedef __bf16 bf2 __attribute__((ext_vector_type(2)));
typedef unsigned short u16;

#define DI __device__ __forceinline__
#define MFMA(a, b, c) __builtin_amdgcn_mfma_f32_32x32x16_bf16((a), (b), (c), 0, 0, 0)
#define LDSP __attribute__((address_space(3)))
#define GLBP __attribute__((address_space(1)))

constexpr int SEQ = 2048, DM = 448, NH = 16, DH = 28, N3 = 1344;
constexpr float QSCALE = 0.18898223650461362f * 1.4426950408889634f;

#define ST(k, i)
constexpr unsigned WS_Q = 0;
constexpr unsigned WS_K = 4u << 20;
constexpr unsigned WS_V = 8u << 20;
constexpr unsigned WS_O = 12u << 20;
constexpr unsigned WS_G = 16u << 20;
constexpr unsigned WS_W = 17u << 20;
constexpr unsigned WS_C = 19u << 20;
constexpr unsigned F1B = WS_C + 4096, F1A = WS_C + 8192, F2A = WS_C + 12288, F2B = WS_C + 16384;
constexpr unsigned QK_BR = 64 * 56 * 512;

DI unsigned pack2(float lo, float hi) {
  f32x2 v = {lo, hi};
  return __builtin_bit_cast(unsigned, __builtin_convertvector(v, bf2));
}
DI bf16x8 as_frag(u32x4 v) { return __builtin_bit_cast(bf16x8, v); }
DI float bf_lo(unsigned v) { return __uint_as_float(v << 16); }
DI float bf_hi(unsigned v) { return __uint_as_float(v & 0xffff0000u); }
DI float swap_max(float v) {
  auto rr = __builtin_amdgcn_permlane32_swap(__float_as_uint(v), __float_as_uint(v), false, false);
  return fmaxf(__uint_as_float(rr[0]), __uint_as_float(rr[1]));
}
DI s16x4 tr_read(const char LDSP* p) {
  return __builtin_amdgcn_ds_read_tr16_b64_v4i16((s16x4 LDSP*)p);
}
DI bf16x8 cat(s16x4 a, s16x4 b) { return __builtin_shufflevector(a, b, 0, 1, 2, 3, 4, 5, 6, 7); }
template <int S>
DI u32x4 pack_step(const f32x16& x, float sc) {
  u32x4 p;
  p[0] = pack2(x[8 * S + 0] * sc, x[8 * S + 1] * sc);
  p[1] = pack2(x[8 * S + 2] * sc, x[8 * S + 3] * sc);
  p[2] = pack2(x[8 * S + 4] * sc, x[8 * S + 5] * sc);
  p[3] = pack2(x[8 * S + 6] * sc, x[8 * S + 7] * sc);
  return p;
}
template <int S>
DI u32x4 pack_step1(const f32x16& x) {
  u32x4 p;
  p[0] = pack2(x[8 * S + 0], x[8 * S + 1]);
  p[1] = pack2(x[8 * S + 2], x[8 * S + 3]);
  p[2] = pack2(x[8 * S + 4], x[8 * S + 5]);
  p[3] = pack2(x[8 * S + 6], x[8 * S + 7]);
  return p;
}

#define RAWBAR() do { asm volatile("" ::: "memory"); __builtin_amdgcn_s_barrier(); asm volatile("" ::: "memory"); } while (0)
#define WAITV(n) ((((n) & 15) | (((n) >> 4) << 14)) | 0x0F70)
#define WAITVL(n) ((((n) & 15) | (((n) >> 4) << 14)) | 0x0070)
DI u32x4 widen(unsigned a0, unsigned a1, unsigned b0, unsigned b1) {
  auto r0 = __builtin_amdgcn_permlane32_swap(a0, b0, false, false);
  auto r1 = __builtin_amdgcn_permlane32_swap(a1, b1, false, false);
  return u32x4{r0[0], r1[0], r0[1], r1[1]};
}

typedef __amdgpu_buffer_rsrc_t rsrc_t;
typedef unsigned v4u_t __attribute__((ext_vector_type(4)));
typedef unsigned long long u64;
typedef GLBP u64 gu64;
DI u32x4 bload(rsrc_t rs, unsigned voff, unsigned soff) {
  return __builtin_bit_cast(u32x4, __builtin_amdgcn_raw_buffer_load_b128(rs, voff, soff, 16));
}
DI void bstore(u32x4 v, rsrc_t rs, unsigned voff, unsigned soff) {
  __builtin_amdgcn_raw_buffer_store_b128(__builtin_bit_cast(v4u_t, v), rs, voff + soff, 0, 16);
}
DI void set_flag(char* ws, unsigned off, u64 tag) {
  __hip_atomic_store((gu64*)(ws + off), tag, __ATOMIC_RELAXED, __HIP_MEMORY_SCOPE_AGENT);
}
DI u64 read_tag(const char* ws) {
  u64 e;
  asm volatile("s_load_dwordx2 %0, %1, 0x0\n\ts_waitcnt lgkmcnt(0)" : "=s"(e) : "s"(ws + WS_C) : "memory");
  return e + 1;
}
DI void wait_flags256(char* ws, unsigned off, u64 tag, int lane) {
  gu64* f = (gu64*)(ws + off) + lane;
  for (unsigned spins = 0; spins < (1u << 20); ++spins) {
    bool ok = true;
#pragma unroll
    for (int k = 0; k < 4; ++k) ok &= __hip_atomic_load(f + 64 * k, __ATOMIC_RELAXED, __HIP_MEMORY_SCOPE_AGENT) == tag;
    if (__all(ok)) break;
    __builtin_amdgcn_s_sleep(2);
  }
  asm volatile("" ::: "memory");
}
DI void wait_flags16(char* ws, unsigned off, u64 tag, int lane) {
  gu64* f = (gu64*)(ws + off) + (lane & 15);
  for (unsigned spins = 0; spins < (1u << 20); ++spins) {
    if (__all(__hip_atomic_load(f, __ATOMIC_RELAXED, __HIP_MEMORY_SCOPE_AGENT) == tag)) break;
    __builtin_amdgcn_s_sleep(1);
  }
  asm volatile("" ::: "memory");
}

constexpr int P1_WSLOT = 64 * 192;
constexpr int P1_SLOT = 16384 + P1_WSLOT;
constexpr int P1_CTL = 5 * P1_SLOT + P1_WSLOT;
constexpr int P1_SINK = P1_CTL + 64;
constexpr int LDS_BYTES = P1_SINK + 256;

constexpr unsigned DROP = 0x80000000u;
template <bool V>
DI void p1_pack(const f32x16& a, int br, int ct, int tile, int lane, u32x4* sv, unsigned* so) {
  const int type = ct / 14, cbase = (ct % 14) * 32;
  if (!V) {
    const float sc = type == 0 ? QSCALE : 1.f;
    const unsigned o = (type == 0 ? WS_Q : WS_K) + br * QK_BR + (tile * 56 + (cbase >> 3)) * 512 + lane * 16;
#pragma unroll
    for (int g = 0; g < 2; ++g) {
      sv[g] = widen(pack2(a[8 * g] * sc, a[8 * g + 1] * sc), pack2(a[8 * g + 2] * sc, a[8 * g + 3] * sc),
                    pack2(a[8 * g + 4] * sc, a[8 * g + 5] * sc), pack2(a[8 * g + 6] * sc, a[8 * g + 7] * sc));
      so[g] = o + g * 1024;
    }
  } else {
    const unsigned f = cbase + (lane & 31), head = f / 28, d = f % 28;
    const unsigned o = WS_V + ((br * 16 * 64 + tile) * 2) * 1024 + (head * 64 * 2 * 64 + d + (lane & 32)) * 16;
    sv[0] = pack_step1<0>(a); so[0] = o;
    sv[1] = pack_step1<1>(a); so[1] = o + 1024;
    const unsigned c = d == 0 ? 0x3F803F80u : 0u;
    sv[2] = u32x4{c, c, c, c}; so[2] = d < 4 ? o + 28 * 16 : DROP;
    sv[3] = sv[2]; so[3] = d < 4 ? o + 28 * 16 + 1024 : DROP;
  }
}

DI void warm_l2(const u16* x, const u16* WA, const u16* WB, char LDSP* smem, const u64 pc) {
  const int b = blockIdx.x, xcd = b & 7;
  const unsigned g = ((b >> 3) - 28) * 512 + threadIdx.x;
  constexpr unsigned len = 60 * 1024;
  __builtin_amdgcn_global_load_lds((const GLBP void*)((const char*)pc + (g * 128 + 4 <= len ? g * 128 : 0u)), (LDSP void*)(smem + P1_SINK), 4, 0, 0);
#pragma unroll
  for (int e = 0; e < 4; ++e) {
    const unsigned i = e * 2048 + g;
    const char* p;
    if (i < 1024) {
      p = (const char*)x + ((xcd & 3) * 512 + (i >> 1)) * 896 + 640 + (i & 1) * 128;
    } else {
      const unsigned idx = i - 1024, row = idx / 12, c = idx % 12;
      unsigned off = (row < 128 ? row + 320 : row - 128) * 2688 + (xcd >> 2) * 1280 + c * 128;
      off = off < (unsigned)(DM * N3 * 2 - 4) ? off : 0u;
      p = (const char*)(row < 128 ? WB : WA) + off;
    }
    __builtin_amdgcn_global_load_lds((const GLBP void*)p, (LDSP void*)(smem + P1_SINK), 4, 0, 0);
  }
}

DI void lds_arrive(char LDSP* smem, int idx, int last, char* ws, unsigned flag_off, u64 tag, int lane) {
  if (lane == 0) {
    if (__hip_atomic_fetch_add((int LDSP*)(smem + P1_CTL) + idx, 1, __ATOMIC_RELAXED, __HIP_MEMORY_SCOPE_WORKGROUP) == last) set_flag(ws, flag_off, tag);
  }
}

template <int NT, int NV>
DI void p1_body(const u16* __restrict__ x, const u16* __restrict__ WA, const u16* __restrict__ WB, char* __restrict__ ws, rsrc_t rs,
                int mt, int nt, char LDSP* smem, u64& tag, u32x4 (&sv)[8], unsigned (&so)[8]) {
  const int tid = threadIdx.x, lane = tid & 63;
  const int w = __builtin_amdgcn_readfirstlane(tid >> 6);
  const int msub = w & 3;
  const int j0 = NT == 2 ? 0 : 2;
  const int r = lane & 31, h = lane >> 5;
  constexpr int NS = 2 * (NT - NV) + 4 * NV;
  constexpr int NW = NT == 2 ? 1 : 2;
  const int wi0 = NT == 2 ? w : 2 * w - 4;
  const u16* xs[2];
  int wso[2];
#pragma unroll
  for (int i = 0; i < 2; ++i) {
    int lr = (w * 2 + i) * 8 + (lane >> 3);
    int logical = (lane & 7) ^ ((lr >> 1) & 7);
    xs[i] = x + (mt * 128 + lr) * DM + logical * 8;
    const int id = (wi0 + i) * 64 + lane;
    wso[i] = (id / 12) * N3 + nt * 96 + (id % 12) * 8;
  }
  auto wbase = [&](int s) {
    return s < 7 ? (s % 5) * P1_SLOT + 16384 : (s < 13 ? (2 + (s - 7) / 2) * P1_SLOT + ((s - 7) & 1) * P1_WSLOT : 5 * P1_SLOT);
  };
  auto issue_x = [&](int c, int i) {
    __builtin_amdgcn_global_load_lds((const GLBP void*)(xs[i] + c * 64), (LDSP void*)(smem + (c % 5) * P1_SLOT + (w * 2 + i) * 1024), 16, 0, 0);
  };
  auto issue_w = [&](int s) {
#pragma unroll
    for (int i = 0; i < NW; ++i)
      __builtin_amdgcn_global_load_lds((const GLBP void*)((s < 7 ? WB : WA) + wso[i] + (s % 7) * 64 * N3),
                                       (LDSP void*)(smem + wbase(s) + (wi0 + i) * 1024), 16, 0, 0);
  };
  int xoff[4];
  {
    int lr = 32 * msub + r, sw = (lr >> 1) & 7;
#pragma unroll
    for (int t = 0; t < 4; ++t) xoff[t] = lr * 128 + (((2 * t + h) ^ sw) << 4);
  }
  const int q4 = (lane & 15) >> 2, p4 = lane & 3, g16 = (lane >> 4) & 1;
  const int woff = (8 * h + q4) * 192 + 64 * j0 + 32 * g16 + 8 * p4;

  f32x16 acc[NT] = {};
  bf16x8 xf[7][4];
  s16x4 wl[2][4][NT], wh[2][4][NT];
  auto readf = [&](int s, int m) {
    const int t = m / NT, j = m % NT;
    if (j == 0 && s < 7) xf[s][t] = *(const bf16x8 LDSP*)(smem + (s % 5) * P1_SLOT + xoff[t]);
    const char LDSP* wp = smem + woff + wbase(s) + t * 16 * 192 + j * 64;
    wl[s & 1][t][j] = tr_read(wp);
    wh[s & 1][t][j] = tr_read(wp + 4 * 192);
  };
#pragma unroll
  for (int c = 0; c < 5; ++c) { issue_x(c, 0); issue_x(c, 1); issue_w(c); }
  issue_w(13);
  tag = read_tag(ws);
  if (tid < 8) ((int LDSP*)(smem + P1_CTL))[tid] = 0;
  __builtin_amdgcn_s_waitcnt(WAITVL(4 * (NW + 2) + NW));
  RAWBAR();
#pragma unroll
  for (int m = 0; m < 4 * NT; ++m) readf(0, m);
#pragma unroll
  for (int s = 0; s < 14; ++s) {
    if (s < 7 || s == 8) {
      if (s < 3) __builtin_amdgcn_s_waitcnt(WAITVL(3 * (NW + 2) + NW));
      else if (s == 3) __builtin_amdgcn_s_waitcnt(WAITVL(2 * (NW + 2) + 3 * NW));
      else if (s == 4) __builtin_amdgcn_s_waitcnt(WAITVL(NW + 2 + 4 * NW));
      else if (s == 5) __builtin_amdgcn_s_waitcnt(WAITVL(6 * NW));
      else if (s == 6) __builtin_amdgcn_s_waitcnt(WAITVL(4 * NW));
      else __builtin_amdgcn_s_waitcnt(WAITVL(NS));
      RAWBAR();
    }
    if (s == 12) {
      __builtin_amdgcn_s_waitcnt(WAITV(0));
      asm volatile("" ::: "memory");
      lds_arrive(smem, 4, 7, ws, F1B + (nt * 16 + mt) * 8, tag, lane);
    }
    __builtin_amdgcn_sched_barrier(0);
#pragma unroll
    for (int m = 0; m < 4 * NT; ++m) {
      const int t = m / NT, j = m % NT;
      const bf16x8 wfr = cat(wl[s & 1][t][j], wh[s & 1][t][j]);
      if (j < NT - NV) acc[j] = MFMA(wfr, xf[s % 7][t], acc[j]);
      else acc[j] = MFMA(xf[s % 7][t], wfr, acc[j]);
      __builtin_amdgcn_sched_barrier(0);
      if (s < 13) readf(s + 1, m);
      if (j == 0) {
        if (s < 2) { if (t < 2) issue_x(s + 5, t); else if (t == 2) issue_w(s + 5); }
        else if (s < 5 && t < 2) issue_w(7 + 2 * (s - 2) + t);
      }
      __builtin_amdgcn_sched_barrier(0);
    }
    if (s == 6 || s == 13) {
#pragma unroll
      for (int e = 0; e < 8; ++e) so[e] = DROP;
#pragma unroll
      for (int j = 0; j < NT; ++j) {
        if (j < NT - NV) p1_pack<false>(acc[j], s == 6, nt * 3 + j0 + j, mt * 4 + msub, lane, sv + 4 * j, so + 4 * j);
        else p1_pack<true>(acc[j], s == 6, nt * 3 + j0 + j, mt * 4 + msub, lane, sv + 4 * j, so + 4 * j);
        if (s == 6) {
#pragma unroll
          for (int e = 0; e < (j < NT - NV ? 2 : 4); ++e) bstore(sv[4 * j + e], rs, so[4 * j + e], 0);
        }
        acc[j] = f32x16{};
      }
      __builtin_amdgcn_sched_barrier(0);
    }
  }
}

DI void p1_spare(const u16* __restrict__ x, const u16* __restrict__ gate_w, const u16* __restrict__ WoA, const u16* __restrict__ WoB,
                 char* __restrict__ ws, rsrc_t rs, int sp, char LDSP* smem, u64& tag) {
  const int tid = threadIdx.x, lane = tid & 63, r = lane & 31, h = lane >> 5;
  tag = read_tag(ws);
  if (tid < 8) ((int LDSP*)(smem + P1_CTL))[tid] = 0;
  __syncthreads();
  if (tid == 0) set_flag(ws, F1B + ((14 + (sp & 1)) * 16 + (sp >> 1)) * 8, tag);
#pragma unroll
  for (int e = 0; e < 4; ++e) {
    const int fid = sp * 32 + (tid >> 6) * 4 + e;
    const int sfr = fid & 1, vh = (fid >> 1) & 31, nt = (fid >> 6) & 1, nb = fid >> 7;
    const int n = 56 * nb + 32 * nt + r;
    const u16* Wo = (vh >> 4) ? WoB : WoA;
    unsigned v[8];
#pragma unroll
    for (int jj = 0; jj < 8; ++jj) {
      const int d = 16 * sfr + 8 * (jj >> 2) + 4 * h + (jj & 3);
      const bool ok = d < DH && n < DM;
      const unsigned tv = Wo[ok ? ((vh & 15) * DH + d) * DM + n : 0];
      v[jj] = ok ? tv : 0u;
    }
    bstore(u32x4{v[0] | (v[1] << 16), v[2] | (v[3] << 16), v[4] | (v[5] << 16), v[6] | (v[7] << 16)}, rs, WS_W + fid * 1024 + lane * 16, 0);
  }
  const int row = sp * 64 + (tid >> 3), part = tid & 7;
  const u32x4* xp = (const u32x4*)(x + row * DM) + part;
  const u32x4* gp = (const u32x4*)(gate_w) + part;
  float acc = 0.f;
#pragma unroll
  for (int i = 0; i < 7; ++i) {
    u32x4 a = xp[i * 8], bb = gp[i * 8];
#pragma unroll
    for (int e = 0; e < 4; ++e) {
      acc = fmaf(bf_lo(a[e]), bf_lo(bb[e]), acc);
      acc = fmaf(bf_hi(a[e]), bf_hi(bb[e]), acc);
    }
  }
  acc += __shfl_xor(acc, 1);
  acc += __shfl_xor(acc, 2);
  acc += __shfl_xor(acc, 4);
  if (part == 0)
    __hip_atomic_store((GLBP float*)(ws + WS_G) + row, 1.f / (1.f + __expf(-acc)), __ATOMIC_RELAXED, __HIP_MEMORY_SCOPE_AGENT);
}

constexpr unsigned long long lmask(int i, bool prev) {
  unsigned long long m = 0;
  for (int l = 0; l < 64; ++l) {
    int r = l & 31, h = l >> 5;
    int kr = (i & 3) + 8 * (i >> 2) + 4 * h;
    int diff = r - kr + (prev ? 32 : 0);
    if (diff >= 0 && diff <= 14 && (diff & 1) == 0) m |= 1ull << l;
  }
  return m;
}
DI float sel_mask(float val, float other, unsigned long long mask) {
  return __builtin_amdgcn_inverse_ballot_w64(mask) ? val : other;
}
template <int ODD>
DI void set_foreign(u32x4& f0, u32x4& f1, int h, unsigned a, unsigned b) {
  if (ODD) { if (!h) { f0[0] = a; f0[1] = b; } }
  else { if (h) { f1[2] = a; f1[3] = b; } }
}
DI float gload(char* ws, int idx) { return __hip_atomic_load((GLBP float*)(ws + WS_G) + idx, __ATOMIC_RELAXED, __HIP_MEMORY_SCOPE_AGENT); }

template <int ODD>
DI void p2_body(char* __restrict__ ws, u16* __restrict__ out, rsrc_t rs, char LDSP* smem, const u64 tag, const int fidx,
                const u32x4 (&sv)[8], const unsigned (&so)[8]) {
  f32x4 LDSP* part = (f32x4 LDSP*)smem;
  float LDSP* cw = (float LDSP*)(smem + 131072);
  f32x4 LDSP* apart = (f32x4 LDSP*)(smem + 131072 + 4096);
  volatile int LDSP* ctl = (volatile int LDSP*)(smem + P1_CTL);
  const int tid = threadIdx.x;
  int lane = tid & 63;
  const int w = __builtin_amdgcn_readfirstlane(tid >> 6);
  int r = lane & 31, h = lane >> 5;
  const int b = blockIdx.x;
  const int head = (b & 7) * 2 + ODD, qb = b >> 4;
  const int oqb = (b & 7) * 2 + (b >> 7), oqh = (b >> 3) & 1, nb = (b >> 4) & 7;
  const unsigned p0 = (7 * head) >> 1;
  unsigned l16 = lane * 16;
  const unsigned qoff = WS_Q + QK_BR + (qb * 4 * 56 + p0) * 512;
  const unsigned koff = WS_K + QK_BR + (w * 8 * 56 + p0) * 512;
  const unsigned voff = WS_V + ((16 + head) * 64 + w * 8) * 2048;
  if (w == 0) {
    const int sel = lane >> 4;
    const int c0 = sel < 2 ? 448 + 8 * (int)p0 + (sel & 1) * 31 : 896 + 28 * head + (sel & 1) * 27;
    gu64* f = (gu64*)(ws + F1B) + (c0 / 96) * 16 + (lane & 15);
    gu64* fq = (gu64*)(ws + F1B) + ((8 * (int)p0 + (lane & 1) * 31) / 96) * 16 + qb;
    for (unsigned spins = 0; spins < (1u << 20); ++spins) {
      bool ok = __hip_atomic_load(f, __ATOMIC_RELAXED, __HIP_MEMORY_SCOPE_AGENT) == tag;
      ok &= __hip_atomic_load(fq, __ATOMIC_RELAXED, __HIP_MEMORY_SCOPE_AGENT) == tag;
      if (__all(ok)) break;
      __builtin_amdgcn_s_sleep(1);
    }
    asm volatile("" ::: "memory");
  }
  __syncthreads();
  ST(1, 0);
  ST(2, 0);
  u32x4 q[4][2];
  u32x4 kf[4][2], vf[4][2];
  auto loadkv = [&](int t) {
    if (t >= 4 && w >= 4) {
      const u32x4 LDSP* sp = (const u32x4 LDSP*)(smem + w * 16384 + (t - 4) * 4096) + lane;
      kf[t & 3][0] = sp[0]; kf[t & 3][1] = sp[64]; vf[t & 3][0] = sp[128]; vf[t & 3][1] = sp[192];
    } else {
      kf[t & 3][0] = bload(rs, l16, koff + t * 28672);
      kf[t & 3][1] = bload(rs, l16 + 1024, koff + t * 28672);
      vf[t & 3][0] = bload(rs, l16, voff + t * 2048);
      vf[t & 3][1] = bload(rs, l16 + 1024, voff + t * 2048);
    }
  };
  auto store_a = [&]() {
#pragma unroll
    for (int e = 0; e < 8; ++e) bstore(sv[e], rs, so[e], 0);
  };
  if (w >= 4) {
    store_a();
    __builtin_amdgcn_s_sleep(32);
    u32x4 st[16];
#pragma unroll
    for (int e = 0; e < 16; ++e) {
      const int t = 4 + (e >> 2), f = e & 3;
      st[e] = bload(rs, l16 + (f & 1) * 1024, f < 2 ? koff + t * 28672 : voff + t * 2048);
    }
#pragma unroll
    for (int e = 0; e < 16; ++e) ((u32x4 LDSP*)(smem + w * 16384))[e * 64 + lane] = st[e];
    asm volatile("s_waitcnt vmcnt(0)" ::: "memory");
    lds_arrive(smem, 2, 7, ws, F1A + fidx * 8, tag, lane);
    if (b == 0 && w == 5) {
      wait_flags256(ws, F1B, tag, lane);
      if (lane == 0) set_flag(ws, WS_C, tag);
    }
    __builtin_amdgcn_s_setprio(1);
  }
  kf[0][0] = bload(rs, l16, koff);
  kf[0][1] = bload(rs, l16 + 1024, koff);
#pragma unroll
  for (int i = 0; i < 4; ++i) { q[i][0] = bload(rs, l16, qoff + i * 28672); q[i][1] = bload(rs, l16 + 1024, qoff + i * 28672); }
  vf[0][0] = bload(rs, l16, voff);
  vf[0][1] = bload(rs, l16 + 1024, voff);
  loadkv(1);
  loadkv(2);
  if (w < 4) store_a();
  __builtin_amdgcn_sched_barrier(0);

  auto fold_shift = [&](int i, float m) {
    m = swap_max(m);
    if (!h) cw[(i * 32 + r) * 8 + w] = m;
    const float c = -m;
    unsigned p01 = pack2(c, 0.f);
    float r1 = c - bf_lo(p01);
    unsigned p1 = pack2(r1, 0.f);
    float r2 = r1 - bf_lo(p1);
    unsigned p2 = pack2(r2, 0.f);
    set_foreign<ODD>(q[i][0], q[i][1], h, (p01 & 0xffffu) | (p1 << 16), p2 & 0xffffu);
  };
  const int ri = w >> 1, rs1 = w & 1;
  f32x4 a0, a1;
  float l;
  auto write_part = [&](f32x16 (&o)[4]) {
#pragma unroll
    for (int i = 0; i < 4; ++i)
#pragma unroll
      for (int g = 0; g < 4; ++g)
        part[((w * 4 + i) * 4 + g) * 64 + lane] = f32x4{o[i][4 * g], o[i][4 * g + 1], o[i][4 * g + 2], o[i][4 * g + 3]};
  };
  auto reduce = [&]() {
    const f32x4 c0 = *(f32x4 LDSP*)(cw + (ri * 32 + r) * 8), c1 = *(f32x4 LDSP*)(cw + (ri * 32 + r) * 8 + 4);
    const float cm = fmaxf(fmaxf(fmaxf(c0[0], c0[1]), fmaxf(c0[2], c0[3])), fmaxf(fmaxf(c1[0], c1[1]), fmaxf(c1[2], c1[3])));
    a0 = f32x4{}; a1 = f32x4{}; l = 0.f;
#pragma unroll
    for (int ww = 0; ww < 8; ++ww) {
      const float wt = __builtin_amdgcn_exp2f((ww < 4 ? c0[ww & 3] : c1[ww & 3]) - cm);
      a0 += part[((ww * 4 + ri) * 4 + 2 * rs1) * 64 + lane] * wt;
      a1 += part[((ww * 4 + ri) * 4 + 2 * rs1 + 1) * 64 + lane] * wt;
      l += ((float LDSP*)(part + ((ww * 4 + ri) * 4 + 3) * 64 + 32 + r))[0] * wt;
    }
  };
  const int qt = w & 1, kq = w >> 1;
  u32x4 wfr[2][8];
  auto loadW = [&]() {
#pragma unroll
    for (int nt = 0; nt < 2; ++nt) {
      const unsigned wo = WS_W + (((nb * 2 + nt) * 32 + 16 + 4 * kq) * 2) * 1024;
#pragma unroll
      for (int e = 0; e < 8; ++e) wfr[nt][e] = bload(rs, l16 + (e & 3) * 1024, wo + (e >> 2) * 4096);
    }
  };

#pragma unroll
  for (int i = 0; i < 4; ++i) {
    set_foreign<ODD>(q[i][0], q[i][1], h, 0u, 0u);
    f32x16 s = MFMA(as_frag(kf[0][0]), as_frag(q[i][0]), f32x16{});
    s = MFMA(as_frag(kf[0][1]), as_frag(q[i][1]), s);
    fold_shift(i, fmaxf(fmaxf(s[0], s[1]), s[2]));
  }
  if (w >= 4) {
    ST(2, 4);
    {
      gu64* f = (gu64*)(ws + F1A) + (qb > 0 ? qb - 1 : 0) * 16 + (lane & 31) - ((lane & 15) >= 14 ? 2 : 0);
      for (unsigned spins = 0; spins < (1u << 20); ++spins) {
        if (__all(__hip_atomic_load(f, __ATOMIC_RELAXED, __HIP_MEMORY_SCOPE_AGENT) == tag)) break;
        __builtin_amdgcn_s_sleep(2);
      }
      asm volatile("" ::: "memory");
    const int ltile = qb * 4 + (w & 3);
    const unsigned lqo = WS_Q + (ltile * 56 + p0) * 512, lko = WS_K + (ltile * 56 + p0) * 512, lvo = WS_V + (head * 64 + ltile) * 2048;
    const unsigned pd = ltile > 0 ? 1 : 0;
    u32x4 lq0 = bload(rs, l16, lqo), lq1 = bload(rs, l16 + 1024, lqo);
    u32x4 lk0 = bload(rs, l16, lko), lk1 = bload(rs, l16 + 1024, lko);
    u32x4 pk0 = bload(rs, l16, lko - pd * 28672), pk1 = bload(rs, l16 + 1024, lko - pd * 28672);
    u32x4 lv0 = bload(rs, l16, lvo), lv1 = bload(rs, l16 + 1024, lvo);
    u32x4 pv1 = bload(rs, l16 + 1024, lvo - pd * 2048);
    set_foreign<ODD>(lq0, lq1, h, 0u, 0u);
    f32x16 sp = MFMA(as_frag(pk0), as_frag(lq0), f32x16{});
    sp = MFMA(as_frag(pk1), as_frag(lq1), sp);
    f32x16 sc = MFMA(as_frag(lk0), as_frag(lq0), f32x16{});
    sc = MFMA(as_frag(lk1), as_frag(lq1), sc);
    const bool first = (ltile == 0);
    const float NEG = -3.0e38f;
    float m = NEG;
#define LM_C(i) { sc[i] = sel_mask(sc[i], NEG, lmask(i, false)); m = fmaxf(m, sc[i]); }
#define LM_P(i) { sp[i] = sel_mask(sp[i], NEG, first ? 0ull : lmask(i, true)); m = fmaxf(m, sp[i]); }
    LM_C(0) LM_C(1) LM_C(2) LM_C(3) LM_C(4) LM_C(5) LM_C(6) LM_C(7)
    LM_C(8) LM_C(9) LM_C(10) LM_C(11) LM_C(12) LM_C(13) LM_C(14) LM_C(15)
    LM_P(8) LM_P(9) LM_P(10) LM_P(11) LM_P(12) LM_P(13) LM_P(14) LM_P(15)
#undef LM_C
#undef LM_P
    m = swap_max(m);
#pragma unroll
    for (int i = 0; i < 16; ++i) sc[i] = __builtin_amdgcn_exp2f(sc[i] - m);
#pragma unroll
    for (int i = 8; i < 16; ++i) sp[i] = __builtin_amdgcn_exp2f(sp[i] - m);
    f32x16 o = MFMA(as_frag(pv1), as_frag(pack_step1<1>(sp)), f32x16{});
    o = MFMA(as_frag(lv0), as_frag(pack_step1<0>(sc)), o);
    o = MFMA(as_frag(lv1), as_frag(pack_step1<1>(sc)), o);
    float ll = __shfl(o[12], 32 + r);
    float inv = 1.f / ll;
    u32x4 o0 = pack_step<0>(o, inv), o1 = pack_step<1>(o, inv);
    if (h) { o1[2] = 0; o1[3] = 0; }
    const unsigned oo = WS_O + ((ltile * 32 + head) * 2) * 1024;
    bstore(o0, rs, l16, oo);
    bstore(o1, rs, l16 + 1024, oo);
    asm volatile("s_waitcnt vmcnt(0)" ::: "memory");
    lds_arrive(smem, 1, 3, ws, F2A + (qb * 16 + head) * 8, tag, lane);
    }
    ST(2, 5);
  }
  __builtin_amdgcn_s_setprio(0);
  ST(1, 1);
  {
    f32x16 o[4] = {};
    set_foreign<ODD>(kf[0][0], kf[0][1], h, 0x3F803F80u, 0x00003F80u);
    f32x16 sa = MFMA(as_frag(kf[0][0]), as_frag(q[0][0]), f32x16{});
    sa = MFMA(as_frag(kf[0][1]), as_frag(q[0][1]), sa);
    f32x16 sb;
    u32x4 pa;
    u32x4 pbp;
    float e3[4];
#define SB __builtin_amdgcn_sched_barrier(0)
#define EXPQ(S, g) { S[4 * g] = __builtin_amdgcn_exp2f(S[4 * g]); S[4 * g + 1] = __builtin_amdgcn_exp2f(S[4 * g + 1]); \
                     S[4 * g + 2] = __builtin_amdgcn_exp2f(S[4 * g + 2]); S[4 * g + 3] = __builtin_amdgcn_exp2f(S[4 * g + 3]); }
    auto step = [&](auto nc, f32x16& sc, f32x16& sn) {
      constexpr int n = decltype(nc)::value;
      constexpr int t = n >> 2, i = n & 3;
      constexpr int t1 = ((n + 1) >> 2) & 3, i1 = (n + 1) & 3;
      constexpr int tp = n > 0 ? ((n - 1) >> 2) & 3 : 0, ip = n > 0 ? (n - 1) & 3 : 0;
      if ((n & 7) == 0 && n > 0) ST(2, n >> 3);
      if (n == 8) asm volatile("" :: "v"(e3[0]), "v"(e3[1]), "v"(e3[2]), "v"(e3[3]), "v"(pbp[0]), "v"(pbp[1]));
      if (n == 8 && w < 4) {
        asm volatile("s_waitcnt vmcnt(8)" ::: "memory");
        lds_arrive(smem, 2, 7, ws, F1A + fidx * 8, tag, lane);
      }
      if (i == 1 && t + 3 < 8) {
        asm volatile("" :: "v"(e3[0]), "v"(e3[1]), "v"(e3[2]), "v"(e3[3]), "v"(pbp[0]), "v"(pbp[1]));
        loadkv(t + 3);
      }
      if (i == 3 && n + 1 < 32) set_foreign<ODD>(kf[t1][0], kf[t1][1], h, 0x3F803F80u, 0x00003F80u);
      SB;
      if (n + 1 < 32) sn = MFMA(as_frag(kf[t1][0]), as_frag(q[i1][0]), f32x16{});
      SB;
      if (n > 0) { pbp[2] = pack2(e3[0], e3[1]); pbp[3] = pack2(e3[2], e3[3]); }
      SB;
      EXPQ(sc, 0);
      SB;
      if (n + 1 < 32) sn = MFMA(as_frag(kf[t1][1]), as_frag(q[i1][1]), sn);
      SB;
      pa[0] = pack2(sc[0], sc[1]); pa[1] = pack2(sc[2], sc[3]);
      SB;
      EXPQ(sc, 1);
      SB;
      if (n > 0) o[ip] = MFMA(as_frag(vf[tp][1]), as_frag(pbp), o[ip]);
      SB;
      pa[2] = pack2(sc[4], sc[5]); pa[3] = pack2(sc[6], sc[7]);
      SB;
      EXPQ(sc, 2);
      SB;
      o[i] = MFMA(as_frag(vf[t & 3][0]), as_frag(pa), o[i]);
      SB;
      pbp[0] = pack2(sc[8], sc[9]); pbp[1] = pack2(sc[10], sc[11]);
      SB;
      EXPQ(sc, 3);
      e3[0] = sc[12]; e3[1] = sc[13]; e3[2] = sc[14]; e3[3] = sc[15];
      SB;
    };
#define STEP2(n) step(std::integral_constant<int, n>{}, sa, sb); step(std::integral_constant<int, n + 1>{}, sb, sa);
    STEP2(0) STEP2(2) STEP2(4) STEP2(6) STEP2(8) STEP2(10) STEP2(12) STEP2(14)
    STEP2(16) STEP2(18) STEP2(20) STEP2(22) STEP2(24) STEP2(26) STEP2(28) STEP2(30)
#undef STEP2
    pbp[2] = pack2(e3[0], e3[1]); pbp[3] = pack2(e3[2], e3[3]);
    o[3] = MFMA(as_frag(vf[3][1]), as_frag(pbp), o[3]);
    ST(1, 2);
    asm volatile("" : "+v"(lane));
    r = lane & 31; h = lane >> 5; l16 = lane * 16;
    write_part(o);
  }
  SB;
  if (w < 4) {
    ST(2, 4);
    {
      gu64* f = lane < 32 ? (gu64*)(ws + F1A) + (lane >> 1) * 16 + 14 + (lane & 1) : (gu64*)(ws + F2A) + oqb * 16 + (lane & 15);
      for (unsigned spins = 0; spins < (1u << 20); ++spins) {
        if (__all(__hip_atomic_load(f, __ATOMIC_RELAXED, __HIP_MEMORY_SCOPE_AGENT) == tag)) break;
        __builtin_amdgcn_s_sleep(2);
      }
      asm volatile("" ::: "memory");
    }
    ST(2, 5);
    const int aq = oqb * 4 + oqh * 2 + (w & 1), an = w >> 1;
    const unsigned ao = WS_O + (aq * 64) * 1024, aw = WS_W + ((nb * 2 + an) * 64) * 1024;
    f32x16 acc = {};
    u32x4 fo[2][4], fw[2][4];
#pragma unroll
    for (int e = 0; e < 4; ++e) { fo[0][e] = bload(rs, l16 + e * 1024, ao); fw[0][e] = bload(rs, l16 + e * 1024, aw); }
#pragma unroll
    for (int c = 0; c < 8; ++c) {
      if (c < 7) {
#pragma unroll
        for (int e = 0; e < 4; ++e) {
          fo[(c + 1) & 1][e] = bload(rs, l16 + e * 1024, ao + (c + 1) * 4096);
          fw[(c + 1) & 1][e] = bload(rs, l16 + e * 1024, aw + (c + 1) * 4096);
        }
      }
      SB;
#pragma unroll
      for (int e = 0; e < 4; ++e) acc = MFMA(as_frag(fw[c & 1][e]), as_frag(fo[c & 1][e]), acc);
      SB;
    }
    ST(2, 6);
    const float ga = gload(ws, aq * 32 + r);
#pragma unroll
    for (int g = 0; g < 4; ++g)
      apart[(w * 4 + g) * 64 + lane] = f32x4{acc[4 * g] * ga, acc[4 * g + 1] * ga, acc[4 * g + 2] * ga, acc[4 * g + 3] * ga};
  }
  __syncthreads();
  loadW();
  reduce();
  ST(1, 3);
  {
    float am = fmaxf(fmaxf(fmaxf(fabsf(a0[0]), fabsf(a0[1])), fmaxf(fabsf(a0[2]), fabsf(a0[3]))),
                     fmaxf(fmaxf(fabsf(a1[0]), fabsf(a1[1])), fmaxf(fabsf(a1[2]), fabsf(a1[3]))));
    float chk = (a0[0] + a0[1] + a0[2] + a0[3]) * 0.f + (a1[0] + a1[1] + a1[2] + a1[3]) * 0.f;
    bool bad = !(l < 1.0e30f) || !(am < 3.0e38f) || !(chk == 0.f);
    if (bad) ctl[0] = 1;
    __syncthreads();
  }
  if (__builtin_amdgcn_readfirstlane(ctl[0])) {
#pragma unroll
    for (int i = 0; i < 4; ++i) set_foreign<ODD>(q[i][0], q[i][1], h, 0u, 0u);
    float m[4] = {-3.0e38f, -3.0e38f, -3.0e38f, -3.0e38f};
#pragma unroll 1
    for (int t = 0; t < 8; ++t) {
      u32x4 k0 = bload(rs, l16, koff + t * 28672), k1 = bload(rs, l16 + 1024, koff + t * 28672);
#pragma unroll
      for (int i = 0; i < 4; ++i) {
        f32x16 s = MFMA(as_frag(k0), as_frag(q[i][0]), f32x16{});
        s = MFMA(as_frag(k1), as_frag(q[i][1]), s);
#pragma unroll
        for (int j = 0; j < 16; ++j) m[i] = fmaxf(m[i], s[j]);
      }
    }
#pragma unroll
    for (int i = 0; i < 4; ++i) fold_shift(i, m[i]);
    f32x16 o[4] = {};
#pragma unroll 1
    for (int t = 0; t < 8; ++t) {
      u32x4 k0 = bload(rs, l16, koff + t * 28672), k1 = bload(rs, l16 + 1024, koff + t * 28672);
      u32x4 v0 = bload(rs, l16, voff + t * 2048), v1 = bload(rs, l16 + 1024, voff + t * 2048);
      set_foreign<ODD>(k0, k1, h, 0x3F803F80u, 0x00003F80u);
#pragma unroll
      for (int i = 0; i < 4; ++i) {
        f32x16 s = MFMA(as_frag(k0), as_frag(q[i][0]), f32x16{});
        s = MFMA(as_frag(k1), as_frag(q[i][1]), s);
#pragma unroll
        for (int j = 0; j < 16; ++j) s[j] = __builtin_amdgcn_exp2f(s[j]);
        o[i] = MFMA(as_frag(v0), as_frag(pack_step1<0>(s)), o[i]);
        o[i] = MFMA(as_frag(v1), as_frag(pack_step1<1>(s)), o[i]);
      }
    }
    write_part(o);
    __syncthreads();
    reduce();
    loadW();
  }
  {
    const float inv = 1.f / l;
    u32x4 ov;
    ov[0] = pack2(a0[0] * inv, a0[1] * inv);
    ov[1] = pack2(a0[2] * inv, a0[3] * inv);
    ov[2] = pack2(a1[0] * inv, a1[1] * inv);
    ov[3] = pack2(a1[2] * inv, a1[3] * inv);
    if (h && rs1) { ov[2] = 0; ov[3] = 0; }
    bstore(ov, rs, l16, WS_O + (((qb * 4 + ri) * 32 + 16 + head) * 2 + rs1) * 1024);
  }
  const int oqt = oqb * 4 + oqh * 2 + qt;
  const unsigned ofo = WS_O + ((oqt * 32 + 16 + 4 * kq) * 2) * 1024;
  const float gt = 1.f - gload(ws, oqt * 32 + r);
  asm volatile("s_waitcnt vmcnt(0)" ::: "memory");
  __syncthreads();
  ST(1, 4);
  if (w == 0) {
    if (lane == 0) set_flag(ws, F2B + (qb * 16 + head) * 8, tag);
    wait_flags16(ws, F2B + oqb * 128, tag, lane);
  }
  __syncthreads();
  ST(1, 5);
  {
    u32x4 ofr[8];
    f32x16 acc[2] = {};
#pragma unroll
    for (int e = 0; e < 8; ++e) ofr[e] = bload(rs, l16 + (e & 3) * 1024, ofo + (e >> 2) * 4096);
#pragma unroll
    for (int e = 0; e < 8; ++e)
#pragma unroll
      for (int nt = 0; nt < 2; ++nt) acc[nt] = MFMA(as_frag(wfr[nt][e]), as_frag(ofr[e]), acc[nt]);
    ST(1, 6);
#pragma unroll
    for (int nt = 0; nt < 2; ++nt)
#pragma unroll
      for (int g = 0; g < 4; ++g)
        part[(((nt * 2 + qt) * 4 + kq) * 4 + g) * 64 + lane] =
            f32x4{acc[nt][4 * g] * gt, acc[nt][4 * g + 1] * gt, acc[nt][4 * g + 2] * gt, acc[nt][4 * g + 3] * gt};
  }
  __syncthreads();
  {
    const int ft = w & 3, fg = w >> 2;
    f32x4 s0 = apart[(ft * 4 + 2 * fg) * 64 + lane], s1 = apart[(ft * 4 + 2 * fg + 1) * 64 + lane];
#pragma unroll
    for (int k = 0; k < 4; ++k) {
      s0 += part[((ft * 4 + k) * 4 + 2 * fg) * 64 + lane];
      s1 += part[((ft * 4 + k) * 4 + 2 * fg + 1) * 64 + lane];
    }
    const u32x4 ov = widen(pack2(s0[0], s0[1]), pack2(s0[2], s0[3]), pack2(s1[0], s1[1]), pack2(s1[2], s1[3]));
    const int col = 32 * (ft >> 1) + 16 * fg + 8 * h;
    if (col < 56) *(u32x4*)(out + (oqb * 128 + oqh * 64 + (ft & 1) * 32 + r) * DM + nb * 56 + col) = ov;
  }
}

__global__ __launch_bounds__(512, 2) void fused_block(const u16* __restrict__ x, const u16* __restrict__ WA, const u16* __restrict__ WB,
                                                      const u16* __restrict__ gate_w, const u16* __restrict__ WoA,
                                                      const u16* __restrict__ WoB, char* __restrict__ ws, u16* __restrict__ out) {
  extern __shared__ __attribute__((aligned(16))) char smem_[];
  char LDSP* smem = (char LDSP*)smem_;
  const rsrc_t rs = __builtin_amdgcn_make_buffer_rsrc(ws, 0, 32u << 20, 0x00020000);
  u64 tag = __builtin_amdgcn_s_getpc();
  ST(0, 0);
  u32x4 sv[8];
  unsigned so[8];
  const int b = blockIdx.x, xcd = b & 7, j = b >> 3;
  if (j < 28) {
    const int mt = 4 * (xcd & 3) + (j & 3);
    const int nt = 7 * (xcd >> 2) + (j >> 2);
    if (__builtin_amdgcn_readfirstlane(threadIdx.x >> 8) == 0) {
      if (nt < 9) p1_body<2, 0>(x, WA, WB, ws, rs, mt, nt, smem, tag, sv, so);
      else if (nt == 9) p1_body<2, 1>(x, WA, WB, ws, rs, mt, nt, smem, tag, sv, so);
      else p1_body<2, 2>(x, WA, WB, ws, rs, mt, nt, smem, tag, sv, so);
    } else {
      if (nt < 9) p1_body<1, 0>(x, WA, WB, ws, rs, mt, nt, smem, tag, sv, so);
      else p1_body<1, 1>(x, WA, WB, ws, rs, mt, nt, smem, tag, sv, so);
    }
  } else {
    warm_l2(x, WA, WB, smem, tag);
    p1_spare(x, gate_w, WoA, WoB, ws, rs, xcd * 4 + j - 28, smem, tag);
#pragma unroll
    for (int e = 0; e < 8; ++e) { sv[e] = u32x4{}; so[e] = DROP; }
  }
  ST(0, 1);
  const int sp = xcd * 4 + j - 28;
  const int fidx = j < 28 ? (4 * (xcd & 3) + (j & 3)) * 16 + 7 * (xcd >> 2) + (j >> 2) : (sp >> 1) * 16 + 14 + (sp & 1);
  if ((b >> 3) & 1) p2_body<1>(ws, out, rs, smem, tag, fidx, sv, so);
  else p2_body<0>(ws, out, rs, smem, tag, fidx, sv, so);
}

extern "C" void kernel_launch(void* const* d_in, const int* in_sizes, int n_in, void* d_out, int out_size, void* d_ws,
                              size_t ws_size, hipStream_t stream) {
  const u16* x = (const u16*)d_in[0];
  const u16* WA = (const u16*)d_in[1];
  const u16* WoA = (const u16*)d_in[2];
  const u16* WB = (const u16*)d_in[4];
  const u16* WoB = (const u16*)d_in[5];
  const u16* gw = (const u16*)d_in[6];
  (void)hipFuncSetAttribute((const void*)fused_block, hipFuncAttributeMaxDynamicSharedMemorySize, LDS_BYTES);
  fused_block<<<256, 512, LDS_BYTES, stream>>>(x, WA, WB, gw, WoA, WoB, (char*)d_ws, (u16*)d_out);
}
```

```cpp
#include <hip/hip_runtime.h>
#include <hip/hip_bf16.h>
#include <stdint.h>
#include <type_traits>

typedef short bf16x8 __attribute__((ext_vector_type(8)));
typedef short s16x4 __attribute__((ext_vector_type(4)));
typedef float f32x16 __attribute__((ext_vector_type(16)));
typedef float f32x4 __attribute__((ext_vector_type(4)));
typedef float f32x2 __attribute__((ext_vector_type(2)));
typedef unsigned u32x4 __attribute__((ext_vector_type(4)));
typedef unsigned u32x2 __attribute__((ext_vector_type(2)));
typedef __bf16 bf2 __attribute__((ext_vector_type(2)));
typedef unsigned short u16;

#define DI __device__ __forceinline__
#define MFMA(a, b, c) __builtin_amdgcn_mfma_f32_32x32x16_bf16((a), (b), (c), 0, 0, 0)
#define LDSP __attribute__((address_space(3)))
#define GLBP __attribute__((address_space(1)))

constexpr int SEQ = 2048, DM = 448, NH = 16, DH = 28, N3 = 1344;
constexpr float QSCALE = 0.18898223650461362f * 1.4426950408889634f;

#define ST(k, i)
constexpr unsigned WS_Q = 0;
constexpr unsigned WS_K = 4u << 20;
constexpr unsigned WS_V = 8u << 20;
constexpr unsigned WS_O = 12u << 20;
constexpr unsigned WS_G = 16u << 20;
constexpr unsigned WS_W = 17u << 20;
constexpr unsigned WS_C = 19u << 20;
constexpr unsigned F1B = WS_C + 4096, F1A = WS_C + 8192, F2A = WS_C + 12288, F2B = WS_C + 16384;
constexpr unsigned QK_BR = 64 * 56 * 512;

DI unsigned pack2(float lo, float hi) {
  f32x2 v = {lo, hi};
  return __builtin_bit_cast(unsigned, __builtin_convertvector(v, bf2));
}
DI bf16x8 as_frag(u32x4 v) { return __builtin_bit_cast(bf16x8, v); }
DI float bf_lo(unsigned v) { return __uint_as_float(v << 16); }
DI float bf_hi(unsigned v) { return __uint_as_float(v & 0xffff0000u); }
DI float swap_max(float v) {
  auto rr = __builtin_amdgcn_permlane32_swap(__float_as_uint(v), __float_as_uint(v), false, false);
  return fmaxf(__uint_as_float(rr[0]), __uint_as_float(rr[1]));
}
DI s16x4 tr_read(const char LDSP* p) {
  return __builtin_amdgcn_ds_read_tr16_b64_v4i16((s16x4 LDSP*)p);
}
DI bf16x8 cat(s16x4 a, s16x4 b) { return __builtin_shufflevector(a, b, 0, 1, 2, 3, 4, 5, 6, 7); }
template <int S>
DI u32x4 pack_step(const f32x16& x, float sc) {
  u32x4 p;
  p[0] = pack2(x[8 * S + 0] * sc, x[8 * S + 1] * sc);
  p[1] = pack2(x[8 * S + 2] * sc, x[8 * S + 3] * sc);
  p[2] = pack2(x[8 * S + 4] * sc, x[8 * S + 5] * sc);
  p[3] = pack2(x[8 * S + 6] * sc, x[8 * S + 7] * sc);
  return p;
}
template <int S>
DI u32x4 pack_step1(const f32x16& x) {
  u32x4 p;
  p[0] = pack2(x[8 * S + 0], x[8 * S + 1]);
  p[1] = pack2(x[8 * S + 2], x[8 * S + 3]);
  p[2] = pack2(x[8 * S + 4], x[8 * S + 5]);
  p[3] = pack2(x[8 * S + 6], x[8 * S + 7]);
  return p;
}

#define RAWBAR() do { asm volatile("" ::: "memory"); __builtin_amdgcn_s_barrier(); asm volatile("" ::: "memory"); } while (0)
#define WAITV(n) ((((n) & 15) | (((n) >> 4) << 14)) | 0x0F70)
#define WAITVL(n) ((((n) & 15) | (((n) >> 4) << 14)) | 0x0070)
DI u32x4 widen(unsigned a0, unsigned a1, unsigned b0, unsigned b1) {
  auto r0 = __builtin_amdgcn_permlane32_swap(a0, b0, false, false);
  auto r1 = __builtin_amdgcn_permlane32_swap(a1, b1, false, false);
  return u32x4{r0[0], r1[0], r0[1], r1[1]};
}

typedef __amdgpu_buffer_rsrc_t rsrc_t;
typedef unsigned v4u_t __attribute__((ext_vector_type(4)));
typedef unsigned long long u64;
typedef GLBP u64 gu64;
DI u32x4 bload(rsrc_t rs, unsigned voff, unsigned soff) {
  return __builtin_bit_cast(u32x4, __builtin_amdgcn_raw_buffer_load_b128(rs, voff, soff, 16));
}
DI void bstore(u32x4 v, rsrc_t rs, unsigned voff, unsigned soff) {
  __builtin_amdgcn_raw_buffer_store_b128(__builtin_bit_cast(v4u_t, v), rs, voff + soff, 0, 16);
}
DI void set_flag(char* ws, unsigned off, u64 tag) {
  __hip_atomic_store((gu64*)(ws + off), tag, __ATOMIC_RELAXED, __HIP_MEMORY_SCOPE_AGENT);
}
DI u64 read_tag(const char* ws) {
  u64 e;
  asm volatile("s_load_dwordx2 %0, %1, 0x0\n\ts_waitcnt lgkmcnt(0)" : "=s"(e) : "s"(ws + WS_C) : "memory");
  return e + 1;
}
DI void wait_flags256(char* ws, unsigned off, u64 tag, int lane) {
  gu64* f = (gu64*)(ws + off) + lane;
  for (unsigned spins = 0; spins < (1u << 20); ++spins) {
    bool ok = true;
#pragma unroll
    for (int k = 0; k < 4; ++k) ok &= __hip_atomic_load(f + 64 * k, __ATOMIC_RELAXED, __HIP_MEMORY_SCOPE_AGENT) == tag;
    if (__all(ok)) break;
    __builtin_amdgcn_s_sleep(2);
  }
  asm volatile("" ::: "memory");
}
DI void wait_flags16(char* ws, unsigned off, u64 tag, int lane) {
  gu64* f = (gu64*)(ws + off) + (lane & 15);
  for (unsigned spins = 0; spins < (1u << 20); ++spins) {
    if (__all(__hip_atomic_load(f, __ATOMIC_RELAXED, __HIP_MEMORY_SCOPE_AGENT) == tag)) break;
    __builtin_amdgcn_s_sleep(1);
  }
  asm volatile("" ::: "memory");
}

constexpr int P1_WSLOT = 64 * 192;
constexpr int P1_SLOT = 16384 + P1_WSLOT;
constexpr int P1_CTL = 5 * P1_SLOT + P1_WSLOT;
constexpr int P1_SINK = P1_CTL + 64;
constexpr int LDS_BYTES = P1_SINK + 256;

constexpr unsigned DROP = 0x80000000u;
template <bool V>
DI void p1_pack(const f32x16& a, int br, int ct, int tile, int lane, u32x4* sv, unsigned* so) {
  const int type = ct / 14, cbase = (ct % 14) * 32;
  if (!V) {
    const float sc = type == 0 ? QSCALE : 1.f;
    const unsigned o = (type == 0 ? WS_Q : WS_K) + br * QK_BR + (tile * 56 + (cbase >> 3)) * 512 + lane * 16;
#pragma unroll
    for (int g = 0; g < 2; ++g) {
      sv[g] = widen(pack2(a[8 * g] * sc, a[8 * g + 1] * sc), pack2(a[8 * g + 2] * sc, a[8 * g + 3] * sc),
                    pack2(a[8 * g + 4] * sc, a[8 * g + 5] * sc), pack2(a[8 * g + 6] * sc, a[8 * g + 7] * sc));
      so[g] = o + g * 1024;
    }
  } else {
    const unsigned f = cbase + (lane & 31), head = f / 28, d = f % 28;
    const unsigned o = WS_V + ((br * 16 * 64 + tile) * 2) * 1024 + (head * 64 * 2 * 64 + d + (lane & 32)) * 16;
    sv[0] = pack_step1<0>(a); so[0] = o;
    sv[1] = pack_step1<1>(a); so[1] = o + 1024;
    const unsigned c = d == 0 ? 0x3F803F80u : 0u;
    sv[2] = u32x4{c, c, c, c}; so[2] = d < 4 ? o + 28 * 16 : DROP;
    sv[3] = sv[2]; so[3] = d < 4 ? o + 28 * 16 + 1024 : DROP;
  }
}

DI void warm_l2(const u16* x, const u16* WA, const u16* WB, char LDSP* smem, const u64 pc) {
  const int b = blockIdx.x, xcd = b & 7;
  const unsigned g = ((b >> 3) - 28) * 512 + threadIdx.x;
  constexpr unsigned len = 60 * 1024;
  __builtin_amdgcn_global_load_lds((const GLBP void*)((const char*)pc + (g * 128 + 4 <= len ? g * 128 : 0u)), (LDSP void*)(smem + P1_SINK), 4, 0, 0);
#pragma unroll
  for (int e = 0; e < 4; ++e) {
    const unsigned i = e * 2048 + g;
    const char* p;
    if (i < 1024) {
      p = (const char*)x + ((xcd & 3) * 512 + (i >> 1)) * 896 + 640 + (i & 1) * 128;
    } else {
      const unsigned idx = i - 1024, row = idx / 12, c = idx % 12;
      unsigned off = (row < 128 ? row + 320 : row - 128) * 2688 + (xcd >> 2) * 1280 + c * 128;
      off = off < (unsigned)(DM * N3 * 2 - 4) ? off : 0u;
      p = (const char*)(row < 128 ? WB : WA) + off;
    }
    __builtin_amdgcn_global_load_lds((const GLBP void*)p, (LDSP void*)(smem + P1_SINK), 4, 0, 0);
  }
}

DI void lds_arrive(char LDSP* smem, int idx, int last, char* ws, unsigned flag_off, u64 tag, int lane) {
  if (lane == 0) {
    if (__hip_atomic_fetch_add((int LDSP*)(smem + P1_CTL) + idx, 1, __ATOMIC_RELAXED, __HIP_MEMORY_SCOPE_WORKGROUP) == last) set_flag(ws, flag_off, tag);
  }
}

template <int NT, int NV>
DI void p1_body(const u16* __restrict__ x, const u16* __restrict__ WA, const u16* __restrict__ WB, char* __restrict__ ws, rsrc_t rs,
                int mt, int nt, char LDSP* smem, u64& tag, u32x4 (&sv)[8], unsigned (&so)[8]) {
  const int tid = threadIdx.x, lane = tid & 63;
  const int w = __builtin_amdgcn_readfirstlane(tid >> 6);
  const int msub = w & 3;
  const int j0 = NT == 2 ? 0 : 2;
  const int r = lane & 31, h = lane >> 5;
  constexpr int NS = 2 * (NT - NV) + 4 * NV;
  constexpr int NW = NT == 2 ? 1 : 2;
  const int wi0 = NT == 2 ? w : 2 * w - 4;
  const u16* xs[2];
  int wso[2];
#pragma unroll
  for (int i = 0; i < 2; ++i) {
    int lr = (w * 2 + i) * 8 + (lane >> 3);
    int logical = (lane & 7) ^ ((lr >> 1) & 7);
    xs[i] = x + (mt * 128 + lr) * DM + logical * 8;
    const int id = (wi0 + i) * 64 + lane;
    wso[i] = (id / 12) * N3 + nt * 96 + (id % 12) * 8;
  }
  auto wbase = [&](int s) {
    return s < 7 ? (s % 5) * P1_SLOT + 16384 : (s < 13 ? (2 + (s - 7) / 2) * P1_SLOT + ((s - 7) & 1) * P1_WSLOT : 5 * P1_SLOT);
  };
  auto issue_x = [&](int c, int i) {
    __builtin_amdgcn_global_load_lds((const GLBP void*)(xs[i] + c * 64), (LDSP void*)(smem + (c % 5) * P1_SLOT + (w * 2 + i) * 1024), 16, 0, 0);
  };
  auto issue_w = [&](int s) {
#pragma unroll
    for (int i = 0; i < NW; ++i)
      __builtin_amdgcn_global_load_lds((const GLBP void*)((s < 7 ? WB : WA) + wso[i] + (s % 7) * 64 * N3),
                                       (LDSP void*)(smem + wbase(s) + (wi0 + i) * 1024), 16, 0, 0);
  };
  int xoff[4];
  {
    int lr = 32 * msub + r, sw = (lr >> 1) & 7;
#pragma unroll
    for (int t = 0; t < 4; ++t) xoff[t] = lr * 128 + (((2 * t + h) ^ sw) << 4);
  }
  const int q4 = (lane & 15) >> 2, p4 = lane & 3, g16 = (lane >> 4) & 1;
  const int woff = (8 * h + q4) * 192 + 64 * j0 + 32 * g16 + 8 * p4;

  f32x16 acc[NT] = {};
  bf16x8 xf[7][4];
  s16x4 wl[2][4][NT], wh[2][4][NT];
  auto readf = [&](int s, int m) {
    const int t = m / NT, j = m % NT;
    if (j == 0 && s < 7) xf[s][t] = *(const bf16x8 LDSP*)(smem + (s % 5) * P1_SLOT + xoff[t]);
    const char LDSP* wp = smem + woff + wbase(s) + t * 16 * 192 + j * 64;
    wl[s & 1][t][j] = tr_read(wp);
    wh[s & 1][t][j] = tr_read(wp + 4 * 192);
  };
#pragma unroll
  for (int c = 0; c < 5; ++c) { issue_x(c, 0); issue_x(c, 1); issue_w(c); }
  issue_w(13);
  tag = read_tag(ws);
  if (tid < 8) ((int LDSP*)(smem + P1_CTL))[tid] = 0;
  __builtin_amdgcn_s_waitcnt(WAITVL(4 * (NW + 2) + NW));
  RAWBAR();
#pragma unroll
  for (int m = 0; m < 4 * NT; ++m) readf(0, m);
#pragma unroll
  for (int s = 0; s < 14; ++s) {
    if (s < 7 || s == 8) {
      if (s < 3) __builtin_amdgcn_s_waitcnt(WAITVL(3 * (NW + 2) + NW));
      else if (s == 3) __builtin_amdgcn_s_waitcnt(WAITVL(2 * (NW + 2) + 3 * NW));
      else if (s == 4) __builtin_amdgcn_s_waitcnt(WAITVL(NW + 2 + 4 * NW));
      else if (s == 5) __builtin_amdgcn_s_waitcnt(WAITVL(6 * NW));
      else if (s == 6) __builtin_amdgcn_s_waitcnt(WAITVL(4 * NW));
      else __builtin_amdgcn_s_waitcnt(WAITVL(NS));
      RAWBAR();
    }
    if (s == 12) {
      __builtin_amdgcn_s_waitcnt(WAITV(0));
      asm volatile("" ::: "memory");
      lds_arrive(smem, 4, 7, ws, F1B + (nt * 16 + mt) * 8, tag, lane);
    }
    __builtin_amdgcn_sched_barrier(0);
#pragma unroll
    for (int m = 0; m < 4 * NT; ++m) {
      const int t = m / NT, j = m % NT;
      const bf16x8 wfr = cat(wl[s & 1][t][j], wh[s & 1][t][j]);
      if (j < NT - NV) acc[j] = MFMA(wfr, xf[s % 7][t], acc[j]);
      else acc[j] = MFMA(xf[s % 7][t], wfr, acc[j]);
      __builtin_amdgcn_sched_barrier(0);
      if (s < 13) readf(s + 1, m);
      if (j == 0) {
        if (s < 2) { if (t < 2) issue_x(s + 5, t); else if (t == 2) issue_w(s + 5); }
        else if (s < 5 && t < 2) issue_w(7 + 2 * (s - 2) + t);
      }
      __builtin_amdgcn_sched_barrier(0);
    }
    if (s == 6 || s == 13) {
#pragma unroll
      for (int e = 0; e < 8; ++e) so[e] = DROP;
#pragma unroll
      for (int j = 0; j < NT; ++j) {
        if (j < NT - NV) p1_pack<false>(acc[j], s == 6, nt * 3 + j0 + j, mt * 4 + msub, lane, sv + 4 * j, so + 4 * j);
        else p1_pack<true>(acc[j], s == 6, nt * 3 + j0 + j, mt * 4 + msub, lane, sv + 4 * j, so + 4 * j);
        if (s == 6) {
#pragma unroll
          for (int e = 0; e < (j < NT - NV ? 2 : 4); ++e) bstore(sv[4 * j + e], rs, so[4 * j + e], 0);
        }
        acc[j] = f32x16{};
      }
      __builtin_amdgcn_sched_barrier(0);
    }
  }
}

DI void p1_spare(const u16* __restrict__ x, const u16* __restrict__ gate_w, const u16* __restrict__ WoA, const u16* __restrict__ WoB,
                 char* __restrict__ ws, rsrc_t rs, int sp, char LDSP* smem, u64& tag) {
  const int tid = threadIdx.x, lane = tid & 63, r = lane & 31, h = lane >> 5;
  tag = read_tag(ws);
  if (tid < 8) ((int LDSP*)(smem + P1_CTL))[tid] = 0;
  __syncthreads();
  if (tid == 0) set_flag(ws, F1B + ((14 + (sp & 1)) * 16 + (sp >> 1)) * 8, tag);
#pragma unroll
  for (int e = 0; e < 4; ++e) {
    const int fid = sp * 32 + (tid >> 6) * 4 + e;
    const int sfr = fid & 1, vh = (fid >> 1) & 31, nt = (fid >> 6) & 1, nb = fid >> 7;
    const int n = 56 * nb + 32 * nt + r;
    const u16* Wo = (vh >> 4) ? WoB : WoA;
    unsigned v[8];
#pragma unroll
    for (int jj = 0; jj < 8; ++jj) {
      const int d = 16 * sfr + 8 * (jj >> 2) + 4 * h + (jj & 3);
      const bool ok = d < DH && n < DM;
      const unsigned tv = Wo[ok ? ((vh & 15) * DH + d) * DM + n : 0];
      v[jj] = ok ? tv : 0u;
    }
    bstore(u32x4{v[0] | (v[1] << 16), v[2] | (v[3] << 16), v[4] | (v[5] << 16), v[6] | (v[7] << 16)}, rs, WS_W + fid * 1024 + lane * 16, 0);
  }
  const int row = sp * 64 + (tid >> 3), part = tid & 7;
  const u32x4* xp = (const u32x4*)(x + row * DM) + part;
  const u32x4* gp = (const u32x4*)(gate_w) + part;
  float acc = 0.f;
#pragma unroll
  for (int i = 0; i < 7; ++i) {
    u32x4 a = xp[i * 8], bb = gp[i * 8];
#pragma unroll
    for (int e = 0; e < 4; ++e) {
      acc = fmaf(bf_lo(a[e]), bf_lo(bb[e]), acc);
      acc = fmaf(bf_hi(a[e]), bf_hi(bb[e]), acc);
    }
  }
  acc += __shfl_xor(acc, 1);
  acc += __shfl_xor(acc, 2);
  acc += __shfl_xor(acc, 4);
  if (part == 0)
    __hip_atomic_store((GLBP float*)(ws + WS_G) + row, 1.f / (1.f + __expf(-acc)), __ATOMIC_RELAXED, __HIP_MEMORY_SCOPE_AGENT);
}

constexpr unsigned long long lmask(int i, bool prev) {
  unsigned long long m = 0;
  for (int l = 0; l < 64; ++l) {
    int r = l & 31, h = l >> 5;
    int kr = (i & 3) + 8 * (i >> 2) + 4 * h;
    int diff = r - kr + (prev ? 32 : 0);
    if (diff >= 0 && diff <= 14 && (diff & 1) == 0) m |= 1ull << l;
  }
  return m;
}
DI float sel_mask(float val, float other, unsigned long long mask) {
  return __builtin_amdgcn_inverse_ballot_w64(mask) ? val : other;
}
template <int ODD>
DI void set_foreign(u32x4& f0, u32x4& f1, int h, unsigned a, unsigned b) {
  if (ODD) { if (!h) { f0[0] = a; f0[1] = b; } }
  else { if (h) { f1[2] = a; f1[3] = b; } }
}
DI float gload(char* ws, int idx) { return __hip_atomic_load((GLBP float*)(ws + WS_G) + idx, __ATOMIC_RELAXED, __HIP_MEMORY_SCOPE_AGENT); }

template <int ODD>
DI void p2_body(char* __restrict__ ws, u16* __restrict__ out, rsrc_t rs, char LDSP* smem, const u64 tag, const int fidx,
                const u32x4 (&sv)[8], const unsigned (&so)[8]) {
  f32x4 LDSP* part = (f32x4 LDSP*)smem;
  float LDSP* cw = (float LDSP*)(smem + 131072);
  volatile int LDSP* ctl = (volatile int LDSP*)(smem + P1_CTL);
  const int tid = threadIdx.x;
  int lane = tid & 63;
  const int w = __builtin_amdgcn_readfirstlane(tid >> 6);
  int r = lane & 31, h = lane >> 5;
  const int b = blockIdx.x;
  const int head = (b & 7) * 2 + ODD, qb = b >> 4;
  const int oqb = (b & 7) * 2 + (b >> 7), oqh = (b >> 3) & 1, nb = (b >> 4) & 7;
  const unsigned p0 = (7 * head) >> 1;
  unsigned l16 = lane * 16;
  const unsigned qoff = WS_Q + QK_BR + (qb * 4 * 56 + p0) * 512;
  const unsigned koff = WS_K + QK_BR + (w * 8 * 56 + p0) * 512;
  const unsigned voff = WS_V + ((16 + head) * 64 + w * 8) * 2048;
  if (w == 0) {
    const int sel = lane >> 4;
    const int c0 = sel < 2 ? 448 + 8 * (int)p0 + (sel & 1) * 31 : 896 + 28 * head + (sel & 1) * 27;
    gu64* f = (gu64*)(ws + F1B) + (c0 / 96) * 16 + (lane & 15);
    gu64* fq = (gu64*)(ws + F1B) + ((8 * (int)p0 + (lane & 1) * 31) / 96) * 16 + qb;
    for (unsigned spins = 0; spins < (1u << 20); ++spins) {
      bool ok = __hip_atomic_load(f, __ATOMIC_RELAXED, __HIP_MEMORY_SCOPE_AGENT) == tag;
      ok &= __hip_atomic_load(fq, __ATOMIC_RELAXED, __HIP_MEMORY_SCOPE_AGENT) == tag;
      if (__all(ok)) break;
      __builtin_amdgcn_s_sleep(1);
    }
    asm volatile("" ::: "memory");
  }
  __syncthreads();
  ST(1, 0);
  ST(2, 0);
  u32x4 q[4][2];
  u32x4 kf[4][2], vf[4][2];
  auto loadkv = [&](int t) {
    kf[t & 3][0] = bload(rs, l16, koff + t * 28672);
    kf[t & 3][1] = bload(rs, l16 + 1024, koff + t * 28672);
    vf[t & 3][0] = bload(rs, l16, voff + t * 2048);
    vf[t & 3][1] = bload(rs, l16 + 1024, voff + t * 2048);
  };
  auto store_a = [&]() {
#pragma unroll
    for (int e = 0; e < 8; ++e) bstore(sv[e], rs, so[e], 0);
  };
  if (w >= 4) {
    store_a();
    asm volatile("s_waitcnt vmcnt(0)" ::: "memory");
    lds_arrive(smem, 2, 7, ws, F1A + fidx * 8, tag, lane);
    if (b == 0 && w == 5) {
      wait_flags256(ws, F1B, tag, lane);
      if (lane == 0) set_flag(ws, WS_C, tag);
    }
    __builtin_amdgcn_s_setprio(1);
  }
  kf[0][0] = bload(rs, l16, koff);
  kf[0][1] = bload(rs, l16 + 1024, koff);
#pragma unroll
  for (int i = 0; i < 4; ++i) { q[i][0] = bload(rs, l16, qoff + i * 28672); q[i][1] = bload(rs, l16 + 1024, qoff + i * 28672); }
  vf[0][0] = bload(rs, l16, voff);
  vf[0][1] = bload(rs, l16 + 1024, voff);
  loadkv(1);
  loadkv(2);
  if (w < 4) store_a();
  __builtin_amdgcn_sched_barrier(0);

  auto fold_shift = [&](int i, float m) {
    m = swap_max(m);
    if (!h) cw[(i * 32 + r) * 8 + w] = m;
    const float c = -m;
    unsigned p01 = pack2(c, 0.f);
    float r1 = c - bf_lo(p01);
    unsigned p1 = pack2(r1, 0.f);
    float r2 = r1 - bf_lo(p1);
    unsigned p2 = pack2(r2, 0.f);
    set_foreign<ODD>(q[i][0], q[i][1], h, (p01 & 0xffffu) | (p1 << 16), p2 & 0xffffu);
  };
  const int ri = w >> 1, rs1 = w & 1;
  f32x4 a0, a1;
  float l;
  auto write_part = [&](f32x16 (&o)[4]) {
#pragma unroll
    for (int i = 0; i < 4; ++i)
#pragma unroll
      for (int g = 0; g < 4; ++g)
        part[((w * 4 + i) * 4 + g) * 64 + lane] = f32x4{o[i][4 * g], o[i][4 * g + 1], o[i][4 * g + 2], o[i][4 * g + 3]};
  };
  auto reduce = [&]() {
    const f32x4 c0 = *(f32x4 LDSP*)(cw + (ri * 32 + r) * 8), c1 = *(f32x4 LDSP*)(cw + (ri * 32 + r) * 8 + 4);
    const float cm = fmaxf(fmaxf(fmaxf(c0[0], c0[1]), fmaxf(c0[2], c0[3])), fmaxf(fmaxf(c1[0], c1[1]), fmaxf(c1[2], c1[3])));
    a0 = f32x4{}; a1 = f32x4{}; l = 0.f;
#pragma unroll
    for (int ww = 0; ww < 8; ++ww) {
      const float wt = __builtin_amdgcn_exp2f((ww < 4 ? c0[ww & 3] : c1[ww & 3]) - cm);
      a0 += part[((ww * 4 + ri) * 4 + 2 * rs1) * 64 + lane] * wt;
      a1 += part[((ww * 4 + ri) * 4 + 2 * rs1 + 1) * 64 + lane] * wt;
      l += ((float LDSP*)(part + ((ww * 4 + ri) * 4 + 3) * 64 + 32 + r))[0] * wt;
    }
  };
  const int qt = w & 1, kq = w >> 1;
  u32x4 wfr[2][2][8];
  auto loadW = [&]() {
#pragma unroll
    for (int br = 0; br < 2; ++br)
#pragma unroll
      for (int nt = 0; nt < 2; ++nt) {
        const unsigned wo = WS_W + (((nb * 2 + nt) * 32 + br * 16 + 4 * kq) * 2) * 1024;
#pragma unroll
        for (int e = 0; e < 8; ++e) wfr[br][nt][e] = bload(rs, l16 + (e & 3) * 1024, wo + (e >> 2) * 4096);
      }
  };

#pragma unroll
  for (int i = 0; i < 4; ++i) {
    set_foreign<ODD>(q[i][0], q[i][1], h, 0u, 0u);
    f32x16 s = MFMA(as_frag(kf[0][0]), as_frag(q[i][0]), f32x16{});
    s = MFMA(as_frag(kf[0][1]), as_frag(q[i][1]), s);
    fold_shift(i, fmaxf(fmaxf(s[0], s[1]), s[2]));
  }
  __builtin_amdgcn_s_setprio(0);
  ST(1, 1);
  {
    f32x16 o[4] = {};
    set_foreign<ODD>(kf[0][0], kf[0][1], h, 0x3F803F80u, 0x00003F80u);
    f32x16 sa = MFMA(as_frag(kf[0][0]), as_frag(q[0][0]), f32x16{});
    sa = MFMA(as_frag(kf[0][1]), as_frag(q[0][1]), sa);
    f32x16 sb;
    u32x4 pa;
    u32x4 pbp;
    float e3[4];
#define SB __builtin_amdgcn_sched_barrier(0)
#define EXPQ(S, g) { S[4 * g] = __builtin_amdgcn_exp2f(S[4 * g]); S[4 * g + 1] = __builtin_amdgcn_exp2f(S[4 * g + 1]); \
                     S[4 * g + 2] = __builtin_amdgcn_exp2f(S[4 * g + 2]); S[4 * g + 3] = __builtin_amdgcn_exp2f(S[4 * g + 3]); }
    auto step = [&](auto nc, f32x16& sc, f32x16& sn) {
      constexpr int n = decltype(nc)::value;
      constexpr int t = n >> 2, i = n & 3;
      constexpr int t1 = ((n + 1) >> 2) & 3, i1 = (n + 1) & 3;
      constexpr int tp = n > 0 ? ((n - 1) >> 2) & 3 : 0, ip = n > 0 ? (n - 1) & 3 : 0;
      if ((n & 7) == 0 && n > 0) ST(2, n >> 3);
      if (n == 8) asm volatile("" :: "v"(e3[0]), "v"(e3[1]), "v"(e3[2]), "v"(e3[3]), "v"(pbp[0]), "v"(pbp[1]));
      if (n == 8 && w < 4) {
        asm volatile("s_waitcnt vmcnt(8)" ::: "memory");
        lds_arrive(smem, 2, 7, ws, F1A + fidx * 8, tag, lane);
      }
      if (i == 1 && t + 3 < 8) loadkv(t + 3);
      if (i == 3 && n + 1 < 32) set_foreign<ODD>(kf[t1][0], kf[t1][1], h, 0x3F803F80u, 0x00003F80u);
      SB;
      if (n + 1 < 32) sn = MFMA(as_frag(kf[t1][0]), as_frag(q[i1][0]), f32x16{});
      SB;
      if (n > 0) { pbp[2] = pack2(e3[0], e3[1]); pbp[3] = pack2(e3[2], e3[3]); }
      SB;
      EXPQ(sc, 0);
      SB;
      if (n + 1 < 32) sn = MFMA(as_frag(kf[t1][1]), as_frag(q[i1][1]), sn);
      SB;
      pa[0] = pack2(sc[0], sc[1]); pa[1] = pack2(sc[2], sc[3]);
      SB;
      EXPQ(sc, 1);
      SB;
      if (n > 0) o[ip] = MFMA(as_frag(vf[tp][1]), as_frag(pbp), o[ip]);
      SB;
      pa[2] = pack2(sc[4], sc[5]); pa[3] = pack2(sc[6], sc[7]);
      SB;
      EXPQ(sc, 2);
      SB;
      o[i] = MFMA(as_frag(vf[t & 3][0]), as_frag(pa), o[i]);
      SB;
      pbp[0] = pack2(sc[8], sc[9]); pbp[1] = pack2(sc[10], sc[11]);
      SB;
      EXPQ(sc, 3);
      e3[0] = sc[12]; e3[1] = sc[13]; e3[2] = sc[14]; e3[3] = sc[15];
      SB;
    };
#define STEP2(n) step(std::integral_constant<int, n>{}, sa, sb); step(std::integral_constant<int, n + 1>{}, sb, sa);
    STEP2(0) STEP2(2) STEP2(4) STEP2(6) STEP2(8) STEP2(10) STEP2(12) STEP2(14)
    STEP2(16) STEP2(18) STEP2(20) STEP2(22) STEP2(24) STEP2(26) STEP2(28) STEP2(30)
#undef STEP2
    pbp[2] = pack2(e3[0], e3[1]); pbp[3] = pack2(e3[2], e3[3]);
    o[3] = MFMA(as_frag(vf[3][1]), as_frag(pbp), o[3]);
    ST(1, 2);
    asm volatile("" : "+v"(lane));
    r = lane & 31; h = lane >> 5; l16 = lane * 16;
    write_part(o);
  }
  SB;
  if (w < 4) {
    ST(2, 4);
    {
      gu64* f = (gu64*)(ws + F1A) + (qb > 0 ? qb - 1 : 0) * 16 + (lane & 31) - ((lane & 15) >= 14 ? 2 : 0);
      for (unsigned spins = 0; spins < (1u << 20); ++spins) {
        if (__all(__hip_atomic_load(f, __ATOMIC_RELAXED, __HIP_MEMORY_SCOPE_AGENT) == tag)) break;
        __builtin_amdgcn_s_sleep(2);
      }
      asm volatile("" ::: "memory");
    const int ltile = qb * 4 + (w & 3);
    const unsigned lqo = WS_Q + (ltile * 56 + p0) * 512, lko = WS_K + (ltile * 56 + p0) * 512, lvo = WS_V + (head * 64 + ltile) * 2048;
    const unsigned pd = ltile > 0 ? 1 : 0;
    u32x4 lq0 = bload(rs, l16, lqo), lq1 = bload(rs, l16 + 1024, lqo);
    u32x4 lk0 = bload(rs, l16, lko), lk1 = bload(rs, l16 + 1024, lko);
    u32x4 pk0 = bload(rs, l16, lko - pd * 28672), pk1 = bload(rs, l16 + 1024, lko - pd * 28672);
    u32x4 lv0 = bload(rs, l16, lvo), lv1 = bload(rs, l16 + 1024, lvo);
    u32x4 pv1 = bload(rs, l16 + 1024, lvo - pd * 2048);
    set_foreign<ODD>(lq0, lq1, h, 0u, 0u);
    f32x16 sp = MFMA(as_frag(pk0), as_frag(lq0), f32x16{});
    sp = MFMA(as_frag(pk1), as_frag(lq1), sp);
    f32x16 sc = MFMA(as_frag(lk0), as_frag(lq0), f32x16{});
    sc = MFMA(as_frag(lk1), as_frag(lq1), sc);
    const bool first = (ltile == 0);
    const float NEG = -3.0e38f;
    float m = NEG;
#define LM_C(i) { sc[i] = sel_mask(sc[i], NEG, lmask(i, false)); m = fmaxf(m, sc[i]); }
#define LM_P(i) { sp[i] = sel_mask(sp[i], NEG, first ? 0ull : lmask(i, true)); m = fmaxf(m, sp[i]); }
    LM_C(0) LM_C(1) LM_C(2) LM_C(3) LM_C(4) LM_C(5) LM_C(6) LM_C(7)
    LM_C(8) LM_C(9) LM_C(10) LM_C(11) LM_C(12) LM_C(13) LM_C(14) LM_C(15)
    LM_P(8) LM_P(9) LM_P(10) LM_P(11) LM_P(12) LM_P(13) LM_P(14) LM_P(15)
#undef LM_C
#undef LM_P
    m = swap_max(m);
#pragma unroll
    for (int i = 0; i < 16; ++i) sc[i] = __builtin_amdgcn_exp2f(sc[i] - m);
#pragma unroll
    for (int i = 8; i < 16; ++i) sp[i] = __builtin_amdgcn_exp2f(sp[i] - m);
    f32x16 o = MFMA(as_frag(pv1), as_frag(pack_step1<1>(sp)), f32x16{});
    o = MFMA(as_frag(lv0), as_frag(pack_step1<0>(sc)), o);
    o = MFMA(as_frag(lv1), as_frag(pack_step1<1>(sc)), o);
    float ll = __shfl(o[12], 32 + r);
    float inv = 1.f / ll;
    u32x4 o0 = pack_step<0>(o, inv), o1 = pack_step<1>(o, inv);
    if (h) { o1[2] = 0; o1[3] = 0; }
    const unsigned oo = WS_O + ((ltile * 32 + head) * 2) * 1024;
    bstore(o0, rs, l16, oo);
    bstore(o1, rs, l16 + 1024, oo);
    asm volatile("s_waitcnt vmcnt(0)" ::: "memory");
    lds_arrive(smem, 1, 3, ws, F2A + (qb * 16 + head) * 8, tag, lane);
    }
    ST(2, 5);
    if (w == 0) {
      gu64* f = (gu64*)(ws + F1A) + ((lane & 31) >> 1) * 16 + 14 + (lane & 1);
      for (unsigned spins = 0; spins < (1u << 20); ++spins) {
        if (__all(__hip_atomic_load(f, __ATOMIC_RELAXED, __HIP_MEMORY_SCOPE_AGENT) == tag)) break;
        __builtin_amdgcn_s_sleep(2);
      }
      asm volatile("" ::: "memory");
    }
  }
  __syncthreads();
  loadW();
  reduce();
  ST(1, 3);
  {
    float am = fmaxf(fmaxf(fmaxf(fabsf(a0[0]), fabsf(a0[1])), fmaxf(fabsf(a0[2]), fabsf(a0[3]))),
                     fmaxf(fmaxf(fabsf(a1[0]), fabsf(a1[1])), fmaxf(fabsf(a1[2]), fabsf(a1[3]))));
    float chk = (a0[0] + a0[1] + a0[2] + a0[3]) * 0.f + (a1[0] + a1[1] + a1[2] + a1[3]) * 0.f;
    bool bad = !(l < 1.0e30f) || !(am < 3.0e38f) || !(chk == 0.f);
    if (bad) ctl[0] = 1;
    __syncthreads();
  }
  if (__builtin_amdgcn_readfirstlane(ctl[0])) {
#pragma unroll
    for (int i = 0; i < 4; ++i) set_foreign<ODD>(q[i][0], q[i][1], h, 0u, 0u);
    float m[4] = {-3.0e38f, -3.0e38f, -3.0e38f, -3.0e38f};
#pragma unroll 1
    for (int t = 0; t < 8; ++t) {
      u32x4 k0 = bload(rs, l16, koff + t * 28672), k1 = bload(rs, l16 + 1024, koff + t * 28672);
#pragma unroll
      for (int i = 0; i < 4; ++i) {
        f32x16 s = MFMA(as_frag(k0), as_frag(q[i][0]), f32x16{});
        s = MFMA(as_frag(k1), as_frag(q[i][1]), s);
#pragma unroll
        for (int j = 0; j < 16; ++j) m[i] = fmaxf(m[i], s[j]);
      }
    }
#pragma unroll
    for (int i = 0; i < 4; ++i) fold_shift(i, m[i]);
    f32x16 o[4] = {};
#pragma unroll 1
    for (int t = 0; t < 8; ++t) {
      u32x4 k0 = bload(rs, l16, koff + t * 28672), k1 = bload(rs, l16 + 1024, koff + t * 28672);
      u32x4 v0 = bload(rs, l16, voff + t * 2048), v1 = bload(rs, l16 + 1024, voff + t * 2048);
      set_foreign<ODD>(k0, k1, h, 0x3F803F80u, 0x00003F80u);
#pragma unroll
      for (int i = 0; i < 4; ++i) {
        f32x16 s = MFMA(as_frag(k0), as_frag(q[i][0]), f32x16{});
        s = MFMA(as_frag(k1), as_frag(q[i][1]), s);
#pragma unroll
        for (int j = 0; j < 16; ++j) s[j] = __builtin_amdgcn_exp2f(s[j]);
        o[i] = MFMA(as_frag(v0), as_frag(pack_step1<0>(s)), o[i]);
        o[i] = MFMA(as_frag(v1), as_frag(pack_step1<1>(s)), o[i]);
      }
    }
    write_part(o);
    __syncthreads();
    reduce();
    loadW();
  }
  {
    const float inv = 1.f / l;
    u32x4 ov;
    ov[0] = pack2(a0[0] * inv, a0[1] * inv);
    ov[1] = pack2(a0[2] * inv, a0[3] * inv);
    ov[2] = pack2(a1[0] * inv, a1[1] * inv);
    ov[3] = pack2(a1[2] * inv, a1[3] * inv);
    if (h && rs1) { ov[2] = 0; ov[3] = 0; }
    bstore(ov, rs, l16, WS_O + (((qb * 4 + ri) * 32 + 16 + head) * 2 + rs1) * 1024);
  }
  const int oqt = oqb * 4 + oqh * 2 + qt;
  const unsigned ofo = WS_O + ((oqt * 32 + 4 * kq) * 2) * 1024;
  const float gt = gload(ws, oqt * 32 + r);
  if (w == 1) wait_flags16(ws, F2A + oqb * 128, tag, lane);
  asm volatile("s_waitcnt vmcnt(0)" ::: "memory");
  __syncthreads();
  ST(1, 4);
  if (tid == 0) set_flag(ws, F2B + (qb * 16 + head) * 8, tag);
  {
    u32x4 ofr[8];
    f32x16 acc[2][2] = {};
#pragma unroll
    for (int e = 0; e < 8; ++e) ofr[e] = bload(rs, l16 + (e & 3) * 1024, ofo + (e >> 2) * 4096);
#pragma unroll
    for (int e = 0; e < 8; ++e)
#pragma unroll
      for (int nt = 0; nt < 2; ++nt) acc[0][nt] = MFMA(as_frag(wfr[0][nt][e]), as_frag(ofr[e]), acc[0][nt]);
    if (w == 0) wait_flags16(ws, F2B + oqb * 128, tag, lane);
    __syncthreads();
    ST(1, 5);
#pragma unroll
    for (int e = 0; e < 8; ++e) ofr[e] = bload(rs, l16 + (e & 3) * 1024, ofo + 32768 + (e >> 2) * 4096);
#pragma unroll
    for (int e = 0; e < 8; ++e)
#pragma unroll
      for (int nt = 0; nt < 2; ++nt) acc[1][nt] = MFMA(as_frag(wfr[1][nt][e]), as_frag(ofr[e]), acc[1][nt]);
    ST(1, 6);
#pragma unroll
    for (int nt = 0; nt < 2; ++nt)
#pragma unroll
      for (int g = 0; g < 4; ++g) {
        f32x4 v;
#pragma unroll
        for (int e = 0; e < 4; ++e) v[e] = acc[0][nt][4 * g + e] * gt + acc[1][nt][4 * g + e] * (1.f - gt);
        part[(((nt * 2 + qt) * 4 + kq) * 4 + g) * 64 + lane] = v;
      }
  }
  __syncthreads();
  {
    const int ft = w & 3, fg = w >> 2;
    f32x4 s0 = {}, s1 = {};
#pragma unroll
    for (int k = 0; k < 4; ++k) {
      s0 += part[((ft * 4 + k) * 4 + 2 * fg) * 64 + lane];
      s1 += part[((ft * 4 + k) * 4 + 2 * fg + 1) * 64 + lane];
    }
    const u32x4 ov = widen(pack2(s0[0], s0[1]), pack2(s0[2], s0[3]), pack2(s1[0], s1[1]), pack2(s1[2], s1[3]));
    const int col = 32 * (ft >> 1) + 16 * fg + 8 * h;
    if (col < 56) *(u32x4*)(out + (oqb * 128 + oqh * 64 + (ft & 1) * 32 + r) * DM + nb * 56 + col) = ov;
  }
}

__global__ __launch_bounds__(512, 2) void fused_block(const u16* __restrict__ x, const u16* __restrict__ WA, const u16* __restrict__ WB,
                                                      const u16* __restrict__ gate_w, const u16* __restrict__ WoA,
                                                      const u16* __restrict__ WoB, char* __restrict__ ws, u16* __restrict__ out) {
  extern __shared__ __attribute__((aligned(16))) char smem_[];
  char LDSP* smem = (char LDSP*)smem_;
  const rsrc_t rs = __builtin_amdgcn_make_buffer_rsrc(ws, 0, 32u << 20, 0x00020000);
  u64 tag = __builtin_amdgcn_s_getpc();
  ST(0, 0);
  u32x4 sv[8];
  unsigned so[8];
  const int b = blockIdx.x, xcd = b & 7, j = b >> 3;
  if (j < 28) {
    const int mt = 4 * (xcd & 3) + (j & 3);
    const int nt = 7 * (xcd >> 2) + (j >> 2);
    if (__builtin_amdgcn_readfirstlane(threadIdx.x >> 8) == 0) {
      if (nt < 9) p1_body<2, 0>(x, WA, WB, ws, rs, mt, nt, smem, tag, sv, so);
      else if (nt == 9) p1_body<2, 1>(x, WA, WB, ws, rs, mt, nt, smem, tag, sv, so);
      else p1_body<2, 2>(x, WA, WB, ws, rs, mt, nt, smem, tag, sv, so);
    } else {
      if (nt < 9) p1_body<1, 0>(x, WA, WB, ws, rs, mt, nt, smem, tag, sv, so);
      else p1_body<1, 1>(x, WA, WB, ws, rs, mt, nt, smem, tag, sv, so);
    }
  } else {
    warm_l2(x, WA, WB, smem, tag);
    p1_spare(x, gate_w, WoA, WoB, ws, rs, xcd * 4 + j - 28, smem, tag);
#pragma unroll
    for (int e = 0; e < 8; ++e) { sv[e] = u32x4{}; so[e] = DROP; }
  }
  ST(0, 1);
  const int sp = xcd * 4 + j - 28;
  const int fidx = j < 28 ? (4 * (xcd & 3) + (j & 3)) * 16 + 7 * (xcd >> 2) + (j >> 2) : (sp >> 1) * 16 + 14 + (sp & 1);
  if ((b >> 3) & 1) p2_body<1>(ws, out, rs, smem, tag, fidx, sv, so);
  else p2_body<0>(ws, out, rs, smem, tag, fidx, sv, so);
}

extern "C" void kernel_launch(void* const* d_in, const int* in_sizes, int n_in, void* d_out, int out_size, void* d_ws,
                              size_t ws_size, hipStream_t stream) {
  const u16* x = (const u16*)d_in[0];
  const u16* WA = (const u16*)d_in[1];
  const u16* WoA = (const u16*)d_in[2];
  const u16* WB = (const u16*)d_in[4];
  const u16* WoB = (const u16*)d_in[5];
  const u16* gw = (const u16*)d_in[6];
  (void)hipFuncSetAttribute((const void*)fused_block, hipFuncAttributeMaxDynamicSharedMemorySize, LDS_BYTES);
  fused_block<<<256, 512, LDS_BYTES, stream>>>(x, WA, WB, gw, WoA, WoB, (char*)d_ws, (u16*)d_out);
}
```

```cpp
#include <hip/hip_runtime.h>
#include <hip/hip_bf16.h>
#include <stdint.h>
#include <type_traits>

typedef short bf16x8 __attribute__((ext_vector_type(8)));
typedef short s16x4 __attribute__((ext_vector_type(4)));
typedef float f32x16 __attribute__((ext_vector_type(16)));
typedef float f32x4 __attribute__((ext_vector_type(4)));
typedef float f32x2 __attribute__((ext_vector_type(2)));
typedef unsigned u32x4 __attribute__((ext_vector_type(4)));
typedef unsigned u32x2 __attribute__((ext_vector_type(2)));
typedef __bf16 bf2 __attribute__((ext_vector_type(2)));
typedef unsigned short u16;

#define DI __device__ __forceinline__
#define MFMA(a, b, c) __builtin_amdgcn_mfma_f32_32x32x16_bf16((a), (b), (c), 0, 0, 0)
#define LDSP __attribute__((address_space(3)))
#define GLBP __attribute__((address_space(1)))

constexpr int SEQ = 2048, DM = 448, NH = 16, DH = 28, N3 = 1344;
constexpr float QSCALE = 0.18898223650461362f * 1.4426950408889634f;

#define ST(k, i)
constexpr unsigned WS_Q = 0;
constexpr unsigned WS_K = 4u << 20;
constexpr unsigned WS_V = 8u << 20;
constexpr unsigned WS_O = 12u << 20;
constexpr unsigned WS_G = 16u << 20;
constexpr unsigned WS_W = 17u << 20;
constexpr unsigned WS_C = 19u << 20;
constexpr unsigned F1B = WS_C + 4096, F1A = WS_C + 8192, F2A = WS_C + 12288, F2B = WS_C + 16384;
constexpr unsigned QK_BR = 64 * 56 * 512;

DI unsigned pack2(float lo, float hi) {
  f32x2 v = {lo, hi};
  return __builtin_bit_cast(unsigned, __builtin_convertvector(v, bf2));
}
DI bf16x8 as_frag(u32x4 v) { return __builtin_bit_cast(bf16x8, v); }
DI float bf_lo(unsigned v) { return __uint_as_float(v << 16); }
DI float bf_hi(unsigned v) { return __uint_as_float(v & 0xffff0000u); }
DI float swap_max(float v) {
  auto rr = __builtin_amdgcn_permlane32_swap(__float_as_uint(v), __float_as_uint(v), false, false);
  return fmaxf(__uint_as_float(rr[0]), __uint_as_float(rr[1]));
}
DI s16x4 tr_read(const char LDSP* p) {
  return __builtin_amdgcn_ds_read_tr16_b64_v4i16((s16x4 LDSP*)p);
}
DI bf16x8 cat(s16x4 a, s16x4 b) { return __builtin_shufflevector(a, b, 0, 1, 2, 3, 4, 5, 6, 7); }
template <int S>
DI u32x4 pack_step(const f32x16& x, float sc) {
  u32x4 p;
  p[0] = pack2(x[8 * S + 0] * sc, x[8 * S + 1] * sc);
  p[1] = pack2(x[8 * S + 2] * sc, x[8 * S + 3] * sc);
  p[2] = pack2(x[8 * S + 4] * sc, x[8 * S + 5] * sc);
  p[3] = pack2(x[8 * S + 6] * sc, x[8 * S + 7] * sc);
  return p;
}
template <int S>
DI u32x4 pack_step1(const f32x16& x) {
  u32x4 p;
  p[0] = pack2(x[8 * S + 0], x[8 * S + 1]);
  p[1] = pack2(x[8 * S + 2], x[8 * S + 3]);
  p[2] = pack2(x[8 * S + 4], x[8 * S + 5]);
  p[3] = pack2(x[8 * S + 6], x[8 * S + 7]);
  return p;
}

#define RAWBAR() do { asm volatile("" ::: "memory"); __builtin_amdgcn_s_barrier(); asm volatile("" ::: "memory"); } while (0)
#define WAITV(n) ((((n) & 15) | (((n) >> 4) << 14)) | 0x0F70)
#define WAITVL(n) ((((n) & 15) | (((n) >> 4) << 14)) | 0x0070)
DI u32x4 widen(unsigned a0, unsigned a1, unsigned b0, unsigned b1) {
  auto r0 = __builtin_amdgcn_permlane32_swap(a0, b0, false, false);
  auto r1 = __builtin_amdgcn_permlane32_swap(a1, b1, false, false);
  return u32x4{r0[0], r1[0], r0[1], r1[1]};
}

typedef __amdgpu_buffer_rsrc_t rsrc_t;
typedef unsigned v4u_t __attribute__((ext_vector_type(4)));
typedef unsigned long long u64;
typedef GLBP u64 gu64;
DI u32x4 bload(rsrc_t rs, unsigned voff, unsigned soff) {
  return __builtin_bit_cast(u32x4, __builtin_amdgcn_raw_buffer_load_b128(rs, voff, soff, 16));
}
DI void bstore(u32x4 v, rsrc_t rs, unsigned voff, unsigned soff) {
  __builtin_amdgcn_raw_buffer_store_b128(__builtin_bit_cast(v4u_t, v), rs, voff + soff, 0, 16);
}
DI void set_flag(char* ws, unsigned off, u64 tag) {
  __hip_atomic_store((gu64*)(ws + off), tag, __ATOMIC_RELAXED, __HIP_MEMORY_SCOPE_AGENT);
}
DI u64 read_tag(const char* ws) {
  u64 e;
  asm volatile("s_load_dwordx2 %0, %1, 0x0\n\ts_waitcnt lgkmcnt(0)" : "=s"(e) : "s"(ws + WS_C) : "memory");
  return e + 1;
}
DI void wait_flags256(char* ws, unsigned off, u64 tag, int lane) {
  gu64* f = (gu64*)(ws + off) + lane;
  for (unsigned spins = 0; spins < (1u << 20); ++spins) {
    bool ok = true;
#pragma unroll
    for (int k = 0; k < 4; ++k) ok &= __hip_atomic_load(f + 64 * k, __ATOMIC_RELAXED, __HIP_MEMORY_SCOPE_AGENT) == tag;
    if (__all(ok)) break;
    __builtin_amdgcn_s_sleep(2);
  }
  asm volatile("" ::: "memory");
}
DI void wait_flags16(char* ws, unsigned off, u64 tag, int lane) {
  gu64* f = (gu64*)(ws + off) + (lane & 15);
  for (unsigned spins = 0; spins < (1u << 20); ++spins) {
    if (__all(__hip_atomic_load(f, __ATOMIC_RELAXED, __HIP_MEMORY_SCOPE_AGENT) == tag)) break;
    __builtin_amdgcn_s_sleep(1);
  }
  asm volatile("" ::: "memory");
}

constexpr int P1_WSLOT = 64 * 192;
constexpr int P1_SLOT = 16384 + P1_WSLOT;
constexpr int P1_CTL = 5 * P1_SLOT + P1_WSLOT;
constexpr int P1_SINK = P1_CTL + 64;
constexpr int LDS_BYTES = P1_SINK + 256;

constexpr unsigned DROP = 0x80000000u;
template <bool V>
DI void p1_pack(const f32x16& a, int br, int ct, int tile, int lane, u32x4* sv, unsigned* so) {
  const int type = ct / 14, cbase = (ct % 14) * 32;
  if (!V) {
    const float sc = type == 0 ? QSCALE : 1.f;
    const unsigned o = (type == 0 ? WS_Q : WS_K) + br * QK_BR + (tile * 56 + (cbase >> 3)) * 512 + lane * 16;
#pragma unroll
    for (int g = 0; g < 2; ++g) {
      sv[g] = widen(pack2(a[8 * g] * sc, a[8 * g + 1] * sc), pack2(a[8 * g + 2] * sc, a[8 * g + 3] * sc),
                    pack2(a[8 * g + 4] * sc, a[8 * g + 5] * sc), pack2(a[8 * g + 6] * sc, a[8 * g + 7] * sc));
      so[g] = o + g * 1024;
    }
  } else {
    const unsigned f = cbase + (lane & 31), head = f / 28, d = f % 28;
    const unsigned o = WS_V + ((br * 16 * 64 + tile) * 2) * 1024 + (head * 64 * 2 * 64 + d + (lane & 32)) * 16;
    sv[0] = pack_step1<0>(a); so[0] = o;
    sv[1] = pack_step1<1>(a); so[1] = o + 1024;
    const unsigned c = d == 0 ? 0x3F803F80u : 0u;
    sv[2] = u32x4{c, c, c, c}; so[2] = d < 4 ? o + 28 * 16 : DROP;
    sv[3] = sv[2]; so[3] = d < 4 ? o + 28 * 16 + 1024 : DROP;
  }
}

DI void warm_code(char LDSP* smem, const u64 pc) {
  const unsigned g = ((blockIdx.x >> 3) - 28) * 512 + threadIdx.x;
  constexpr unsigned len = 60 * 1024;
  if (g * 128 + 4 <= len)
    __builtin_amdgcn_global_load_lds((const GLBP void*)((const char*)pc + g * 128), (LDSP void*)(smem + P1_SINK), 4, 0, 0);
}
DI void warm_data(const u16* x, const u16* WA, const u16* WB, char LDSP* smem) {
  const int xcd = blockIdx.x & 7;
  const unsigned i = ((blockIdx.x >> 3) * 8 + (threadIdx.x >> 6)) * 36 + (threadIdx.x & 63) % 36;
  const char* p;
  if (i < 1024) {
    p = (const char*)x + ((xcd & 3) * 512 + (i >> 1)) * 896 + 640 + (i & 1) * 128;
  } else {
    const unsigned idx = i - 1024, row = idx / 12, c = idx % 12;
    unsigned off = (row < 128 ? row + 320 : row - 128) * 2688 + (xcd >> 2) * 1280 + c * 128;
    off = off < (unsigned)(DM * N3 * 2 - 4) ? off : 0u;
    p = (const char*)(row < 128 ? WB : WA) + off;
  }
  __builtin_amdgcn_global_load_lds((const GLBP void*)p, (LDSP void*)(smem + P1_SINK), 4, 0, 0);
}

DI void lds_arrive(char LDSP* smem, int idx, int last, char* ws, unsigned flag_off, u64 tag, int lane) {
  if (lane == 0) {
    if (__hip_atomic_fetch_add((int LDSP*)(smem + P1_CTL) + idx, 1, __ATOMIC_RELAXED, __HIP_MEMORY_SCOPE_WORKGROUP) == last) set_flag(ws, flag_off, tag);
  }
}

template <int NT, int NV>
DI void p1_body(const u16* __restrict__ x, const u16* __restrict__ WA, const u16* __restrict__ WB, char* __restrict__ ws, rsrc_t rs,
                int mt, int nt, char LDSP* smem, u64& tag, u32x4 (&sv)[8], unsigned (&so)[8]) {
  const int tid = threadIdx.x, lane = tid & 63;
  const int w = __builtin_amdgcn_readfirstlane(tid >> 6);
  const int msub = w & 3;
  const int j0 = NT == 2 ? 0 : 2;
  const int r = lane & 31, h = lane >> 5;
  constexpr int NS = 2 * (NT - NV) + 4 * NV;
  constexpr int NW = NT == 2 ? 1 : 2;
  const int wi0 = NT == 2 ? w : 2 * w - 4;
  const u16* xs[2];
  int wso[2];
#pragma unroll
  for (int i = 0; i < 2; ++i) {
    int lr = (w * 2 + i) * 8 + (lane >> 3);
    int logical = (lane & 7) ^ ((lr >> 1) & 7);
    xs[i] = x + (mt * 128 + lr) * DM + logical * 8;
    const int id = (wi0 + i) * 64 + lane;
    wso[i] = (id / 12) * N3 + nt * 96 + (id % 12) * 8;
  }
  auto wbase = [&](int s) {
    return s < 7 ? (s % 5) * P1_SLOT + 16384 : (s < 13 ? (2 + (s - 7) / 2) * P1_SLOT + ((s - 7) & 1) * P1_WSLOT : 5 * P1_SLOT);
  };
  auto issue_x = [&](int c, int i) {
    __builtin_amdgcn_global_load_lds((const GLBP void*)(xs[i] + c * 64), (LDSP void*)(smem + (c % 5) * P1_SLOT + (w * 2 + i) * 1024), 16, 0, 0);
  };
  auto issue_w = [&](int s) {
#pragma unroll
    for (int i = 0; i < NW; ++i)
      __builtin_amdgcn_global_load_lds((const GLBP void*)((s < 7 ? WB : WA) + wso[i] + (s % 7) * 64 * N3),
                                       (LDSP void*)(smem + wbase(s) + (wi0 + i) * 1024), 16, 0, 0);
  };
  int xoff[4];
  {
    int lr = 32 * msub + r, sw = (lr >> 1) & 7;
#pragma unroll
    for (int t = 0; t < 4; ++t) xoff[t] = lr * 128 + (((2 * t + h) ^ sw) << 4);
  }
  const int q4 = (lane & 15) >> 2, p4 = lane & 3, g16 = (lane >> 4) & 1;
  const int woff = (8 * h + q4) * 192 + 64 * j0 + 32 * g16 + 8 * p4;

  f32x16 acc[NT] = {};
  bf16x8 xf[7][4];
  s16x4 wl[2][4][NT], wh[2][4][NT];
  auto readf = [&](int s, int m) {
    const int t = m / NT, j = m % NT;
    if (j == 0 && s < 7) xf[s][t] = *(const bf16x8 LDSP*)(smem + (s % 5) * P1_SLOT + xoff[t]);
    const char LDSP* wp = smem + woff + wbase(s) + t * 16 * 192 + j * 64;
    wl[s & 1][t][j] = tr_read(wp);
    wh[s & 1][t][j] = tr_read(wp + 4 * 192);
  };
#pragma unroll
  for (int c = 0; c < 5; ++c) { issue_x(c, 0); issue_x(c, 1); issue_w(c); }
  issue_w(13);
  warm_data(x, WA, WB, smem);
  tag = read_tag(ws);
  if (tid < 8) ((int LDSP*)(smem + P1_CTL))[tid] = 0;
  __builtin_amdgcn_s_waitcnt(WAITVL(4 * (NW + 2) + NW + 1));
  RAWBAR();
#pragma unroll
  for (int m = 0; m < 4 * NT; ++m) readf(0, m);
#pragma unroll
  for (int s = 0; s < 14; ++s) {
    if (s < 7 || s == 8) {
      if (s < 3) __builtin_amdgcn_s_waitcnt(WAITVL(3 * (NW + 2) + NW + 1));
      else if (s == 3) __builtin_amdgcn_s_waitcnt(WAITVL(2 * (NW + 2) + 3 * NW + 1));
      else if (s == 4) __builtin_amdgcn_s_waitcnt(WAITVL(NW + 2 + 4 * NW));
      else if (s == 5) __builtin_amdgcn_s_waitcnt(WAITVL(6 * NW));
      else if (s == 6) __builtin_amdgcn_s_waitcnt(WAITVL(4 * NW));
      else __builtin_amdgcn_s_waitcnt(WAITVL(NS));
      RAWBAR();
    }
    if (s == 12) {
      __builtin_amdgcn_s_waitcnt(WAITV(0));
      asm volatile("" ::: "memory");
      lds_arrive(smem, 4, 7, ws, F1B + (nt * 16 + mt) * 8, tag, lane);
    }
    __builtin_amdgcn_sched_barrier(0);
#pragma unroll
    for (int m = 0; m < 4 * NT; ++m) {
      const int t = m / NT, j = m % NT;
      const bf16x8 wfr = cat(wl[s & 1][t][j], wh[s & 1][t][j]);
      if (j < NT - NV) acc[j] = MFMA(wfr, xf[s % 7][t], acc[j]);
      else acc[j] = MFMA(xf[s % 7][t], wfr, acc[j]);
      __builtin_amdgcn_sched_barrier(0);
      if (s < 13) readf(s + 1, m);
      if (j == 0) {
        if (s < 2) { if (t < 2) issue_x(s + 5, t); else if (t == 2) issue_w(s + 5); }
        else if (s < 5 && t < 2) issue_w(7 + 2 * (s - 2) + t);
      }
      __builtin_amdgcn_sched_barrier(0);
    }
    if (s == 6 || s == 13) {
#pragma unroll
      for (int e = 0; e < 8; ++e) so[e] = DROP;
#pragma unroll
      for (int j = 0; j < NT; ++j) {
        if (j < NT - NV) p1_pack<false>(acc[j], s == 6, nt * 3 + j0 + j, mt * 4 + msub, lane, sv + 4 * j, so + 4 * j);
        else p1_pack<true>(acc[j], s == 6, nt * 3 + j0 + j, mt * 4 + msub, lane, sv + 4 * j, so + 4 * j);
        if (s == 6) {
#pragma unroll
          for (int e = 0; e < (j < NT - NV ? 2 : 4); ++e) bstore(sv[4 * j + e], rs, so[4 * j + e], 0);
        }
        acc[j] = f32x16{};
      }
      __builtin_amdgcn_sched_barrier(0);
    }
  }
}

DI void p1_spare(const u16* __restrict__ x, const u16* __restrict__ gate_w, const u16* __restrict__ WoA, const u16* __restrict__ WoB,
                 char* __restrict__ ws, rsrc_t rs, int sp, char LDSP* smem, u64& tag) {
  const int tid = threadIdx.x, lane = tid & 63, r = lane & 31, h = lane >> 5;
  tag = read_tag(ws);
  if (tid < 8) ((int LDSP*)(smem + P1_CTL))[tid] = 0;
  __syncthreads();
  if (tid == 0) set_flag(ws, F1B + ((14 + (sp & 1)) * 16 + (sp >> 1)) * 8, tag);
#pragma unroll
  for (int e = 0; e < 4; ++e) {
    const int fid = sp * 32 + (tid >> 6) * 4 + e;
    const int sfr = fid & 1, vh = (fid >> 1) & 31, nt = (fid >> 6) & 1, nb = fid >> 7;
    const int n = 56 * nb + 32 * nt + r;
    const u16* Wo = (vh >> 4) ? WoB : WoA;
    unsigned v[8];
#pragma unroll
    for (int jj = 0; jj < 8; ++jj) {
      const int d = 16 * sfr + 8 * (jj >> 2) + 4 * h + (jj & 3);
      const bool ok = d < DH && n < DM;
      const unsigned tv = Wo[ok ? ((vh & 15) * DH + d) * DM + n : 0];
      v[jj] = ok ? tv : 0u;
    }
    bstore(u32x4{v[0] | (v[1] << 16), v[2] | (v[3] << 16), v[4] | (v[5] << 16), v[6] | (v[7] << 16)}, rs, WS_W + fid * 1024 + lane * 16, 0);
  }
  const int row = sp * 64 + (tid >> 3), part = tid & 7;
  const u32x4* xp = (const u32x4*)(x + row * DM) + part;
  const u32x4* gp = (const u32x4*)(gate_w) + part;
  float acc = 0.f;
#pragma unroll
  for (int i = 0; i < 7; ++i) {
    u32x4 a = xp[i * 8], bb = gp[i * 8];
#pragma unroll
    for (int e = 0; e < 4; ++e) {
      acc = fmaf(bf_lo(a[e]), bf_lo(bb[e]), acc);
      acc = fmaf(bf_hi(a[e]), bf_hi(bb[e]), acc);
    }
  }
  acc += __shfl_xor(acc, 1);
  acc += __shfl_xor(acc, 2);
  acc += __shfl_xor(acc, 4);
  if (part == 0)
    __hip_atomic_store((GLBP float*)(ws + WS_G) + row, 1.f / (1.f + __expf(-acc)), __ATOMIC_RELAXED, __HIP_MEMORY_SCOPE_AGENT);
}

constexpr unsigned long long lmask(int i, bool prev) {
  unsigned long long m = 0;
  for (int l = 0; l < 64; ++l) {
    int r = l & 31, h = l >> 5;
    int kr = (i & 3) + 8 * (i >> 2) + 4 * h;
    int diff = r - kr + (prev ? 32 : 0);
    if (diff >= 0 && diff <= 14 && (diff & 1) == 0) m |= 1ull << l;
  }
  return m;
}
DI float sel_mask(float val, float other, unsigned long long mask) {
  return __builtin_amdgcn_inverse_ballot_w64(mask) ? val : other;
}
template <int ODD>
DI void set_foreign(u32x4& f0, u32x4& f1, int h, unsigned a, unsigned b) {
  if (ODD) { if (!h) { f0[0] = a; f0[1] = b; } }
  else { if (h) { f1[2] = a; f1[3] = b; } }
}
DI float gload(char* ws, int idx) { return __hip_atomic_load((GLBP float*)(ws + WS_G) + idx, __ATOMIC_RELAXED, __HIP_MEMORY_SCOPE_AGENT); }

template <int ODD>
DI void p2_body(char* __restrict__ ws, u16* __restrict__ out, rsrc_t rs, char LDSP* smem, const u64 tag, const int fidx,
                const u32x4 (&sv)[8], const unsigned (&so)[8]) {
  f32x4 LDSP* part = (f32x4 LDSP*)smem;
  float LDSP* cw = (float LDSP*)(smem + 131072);
  volatile int LDSP* ctl = (volatile int LDSP*)(smem + P1_CTL);
  const int tid = threadIdx.x;
  int lane = tid & 63;
  const int w = __builtin_amdgcn_readfirstlane(tid >> 6);
  int r = lane & 31, h = lane >> 5;
  const int b = blockIdx.x;
  const int head = (b & 7) * 2 + ODD, qb = b >> 4;
  const int oqb = (b & 7) * 2 + (b >> 7), oqh = (b >> 3) & 1, nb = (b >> 4) & 7;
  const unsigned p0 = (7 * head) >> 1;
  unsigned l16 = lane * 16;
  const unsigned qoff = WS_Q + QK_BR + (qb * 4 * 56 + p0) * 512;
  const unsigned koff = WS_K + QK_BR + (w * 8 * 56 + p0) * 512;
  const unsigned voff = WS_V + ((16 + head) * 64 + w * 8) * 2048;
  if (w == 0) {
    const int sel = lane >> 4;
    const int c0 = sel < 2 ? 448 + 8 * (int)p0 + (sel & 1) * 31 : 896 + 28 * head + (sel & 1) * 27;
    gu64* f = (gu64*)(ws + F1B) + (c0 / 96) * 16 + (lane & 15);
    gu64* fq = (gu64*)(ws + F1B) + ((8 * (int)p0 + (lane & 1) * 31) / 96) * 16 + qb;
    for (unsigned spins = 0; spins < (1u << 20); ++spins) {
      bool ok = __hip_atomic_load(f, __ATOMIC_RELAXED, __HIP_MEMORY_SCOPE_AGENT) == tag;
      ok &= __hip_atomic_load(fq, __ATOMIC_RELAXED, __HIP_MEMORY_SCOPE_AGENT) == tag;
      if (__all(ok)) break;
      __builtin_amdgcn_s_sleep(1);
    }
    asm volatile("" ::: "memory");
  }
  __syncthreads();
  ST(1, 0);
  ST(2, 0);
  u32x4 q[4][2];
  u32x4 kf[4][2], vf[4][2];
  auto loadkv = [&](int t) {
    kf[t & 3][0] = bload(rs, l16, koff + t * 28672);
    kf[t & 3][1] = bload(rs, l16 + 1024, koff + t * 28672);
    vf[t & 3][0] = bload(rs, l16, voff + t * 2048);
    vf[t & 3][1] = bload(rs, l16 + 1024, voff + t * 2048);
  };
  auto store_a = [&]() {
#pragma unroll
    for (int e = 0; e < 8; ++e) bstore(sv[e], rs, so[e], 0);
  };
  if (w >= 4) {
    store_a();
    asm volatile("s_waitcnt vmcnt(0)" ::: "memory");
    lds_arrive(smem, 2, 7, ws, F1A + fidx * 8, tag, lane);
    if (b == 0 && w == 5) {
      wait_flags256(ws, F1B, tag, lane);
      if (lane == 0) set_flag(ws, WS_C, tag);
    }
    __builtin_amdgcn_s_setprio(1);
  }
  kf[0][0] = bload(rs, l16, koff);
  kf[0][1] = bload(rs, l16 + 1024, koff);
#pragma unroll
  for (int i = 0; i < 4; ++i) { q[i][0] = bload(rs, l16, qoff + i * 28672); q[i][1] = bload(rs, l16 + 1024, qoff + i * 28672); }
  vf[0][0] = bload(rs, l16, voff);
  vf[0][1] = bload(rs, l16 + 1024, voff);
  loadkv(1);
  loadkv(2);
  if (w < 4) store_a();
  __builtin_amdgcn_sched_barrier(0);

  auto fold_shift = [&](int i, float m) {
    m = swap_max(m);
    if (!h) cw[(i * 32 + r) * 8 + w] = m;
    const float c = -m;
    unsigned p01 = pack2(c, 0.f);
    float r1 = c - bf_lo(p01);
    unsigned p1 = pack2(r1, 0.f);
    float r2 = r1 - bf_lo(p1);
    unsigned p2 = pack2(r2, 0.f);
    set_foreign<ODD>(q[i][0], q[i][1], h, (p01 & 0xffffu) | (p1 << 16), p2 & 0xffffu);
  };
  const int ri = w >> 1, rs1 = w & 1;
  f32x4 a0, a1;
  float l;
  auto write_part = [&](f32x16 (&o)[4]) {
#pragma unroll
    for (int i = 0; i < 4; ++i)
#pragma unroll
      for (int g = 0; g < 4; ++g)
        part[((w * 4 + i) * 4 + g) * 64 + lane] = f32x4{o[i][4 * g], o[i][4 * g + 1], o[i][4 * g + 2], o[i][4 * g + 3]};
  };
  auto reduce = [&]() {
    const f32x4 c0 = *(f32x4 LDSP*)(cw + (ri * 32 + r) * 8), c1 = *(f32x4 LDSP*)(cw + (ri * 32 + r) * 8 + 4);
    const float cm = fmaxf(fmaxf(fmaxf(c0[0], c0[1]), fmaxf(c0[2], c0[3])), fmaxf(fmaxf(c1[0], c1[1]), fmaxf(c1[2], c1[3])));
    a0 = f32x4{}; a1 = f32x4{}; l = 0.f;
#pragma unroll
    for (int ww = 0; ww < 8; ++ww) {
      const float wt = __builtin_amdgcn_exp2f((ww < 4 ? c0[ww & 3] : c1[ww & 3]) - cm);
      a0 += part[((ww * 4 + ri) * 4 + 2 * rs1) * 64 + lane] * wt;
      a1 += part[((ww * 4 + ri) * 4 + 2 * rs1 + 1) * 64 + lane] * wt;
      l += ((float LDSP*)(part + ((ww * 4 + ri) * 4 + 3) * 64 + 32 + r))[0] * wt;
    }
  };
  const int qt = w & 1, kq = w >> 1;
  u32x4 wfr[2][2][8];
  auto loadW = [&]() {
#pragma unroll
    for (int br = 0; br < 2; ++br)
#pragma unroll
      for (int nt = 0; nt < 2; ++nt) {
        const unsigned wo = WS_W + (((nb * 2 + nt) * 32 + br * 16 + 4 * kq) * 2) * 1024;
#pragma unroll
        for (int e = 0; e < 8; ++e) wfr[br][nt][e] = bload(rs, l16 + (e & 3) * 1024, wo + (e >> 2) * 4096);
      }
  };

#pragma unroll
  for (int i = 0; i < 4; ++i) {
    set_foreign<ODD>(q[i][0], q[i][1], h, 0u, 0u);
    f32x16 s = MFMA(as_frag(kf[0][0]), as_frag(q[i][0]), f32x16{});
    s = MFMA(as_frag(kf[0][1]), as_frag(q[i][1]), s);
    fold_shift(i, fmaxf(fmaxf(s[0], s[1]), s[2]));
  }
  __builtin_amdgcn_s_setprio(0);
  ST(1, 1);
  {
    f32x16 o[4] = {};
    set_foreign<ODD>(kf[0][0], kf[0][1], h, 0x3F803F80u, 0x00003F80u);
    f32x16 sa = MFMA(as_frag(kf[0][0]), as_frag(q[0][0]), f32x16{});
    sa = MFMA(as_frag(kf[0][1]), as_frag(q[0][1]), sa);
    f32x16 sb;
    u32x4 pa;
    u32x4 pbp;
    float e3[4];
#define SB __builtin_amdgcn_sched_barrier(0)
#define EXPQ(S, g) { S[4 * g] = __builtin_amdgcn_exp2f(S[4 * g]); S[4 * g + 1] = __builtin_amdgcn_exp2f(S[4 * g + 1]); \
                     S[4 * g + 2] = __builtin_amdgcn_exp2f(S[4 * g + 2]); S[4 * g + 3] = __builtin_amdgcn_exp2f(S[4 * g + 3]); }
    auto step = [&](auto nc, f32x16& sc, f32x16& sn) {
      constexpr int n = decltype(nc)::value;
      constexpr int t = n >> 2, i = n & 3;
      constexpr int t1 = ((n + 1) >> 2) & 3, i1 = (n + 1) & 3;
      constexpr int tp = n > 0 ? ((n - 1) >> 2) & 3 : 0, ip = n > 0 ? (n - 1) & 3 : 0;
      if ((n & 7) == 0 && n > 0) ST(2, n >> 3);
      if (n == 8) asm volatile("" :: "v"(e3[0]), "v"(e3[1]), "v"(e3[2]), "v"(e3[3]), "v"(pbp[0]), "v"(pbp[1]));
      if (n == 8 && w < 4) {
        asm volatile("s_waitcnt vmcnt(8)" ::: "memory");
        lds_arrive(smem, 2, 7, ws, F1A + fidx * 8, tag, lane);
      }
      if (i == 1 && t + 3 < 8) loadkv(t + 3);
      if (i == 3 && n + 1 < 32) set_foreign<ODD>(kf[t1][0], kf[t1][1], h, 0x3F803F80u, 0x00003F80u);
      SB;
      if (n + 1 < 32) sn = MFMA(as_frag(kf[t1][0]), as_frag(q[i1][0]), f32x16{});
      SB;
      if (n > 0) { pbp[2] = pack2(e3[0], e3[1]); pbp[3] = pack2(e3[2], e3[3]); }
      SB;
      EXPQ(sc, 0);
      SB;
      if (n + 1 < 32) sn = MFMA(as_frag(kf[t1][1]), as_frag(q[i1][1]), sn);
      SB;
      pa[0] = pack2(sc[0], sc[1]); pa[1] = pack2(sc[2], sc[3]);
      SB;
      EXPQ(sc, 1);
      SB;
      if (n > 0) o[ip] = MFMA(as_frag(vf[tp][1]), as_frag(pbp), o[ip]);
      SB;
      pa[2] = pack2(sc[4], sc[5]); pa[3] = pack2(sc[6], sc[7]);
      SB;
      EXPQ(sc, 2);
      SB;
      o[i] = MFMA(as_frag(vf[t & 3][0]), as_frag(pa), o[i]);
      SB;
      pbp[0] = pack2(sc[8], sc[9]); pbp[1] = pack2(sc[10], sc[11]);
      SB;
      EXPQ(sc, 3);
      e3[0] = sc[12]; e3[1] = sc[13]; e3[2] = sc[14]; e3[3] = sc[15];
      SB;
    };
#define STEP2(n) step(std::integral_constant<int, n>{}, sa, sb); step(std::integral_constant<int, n + 1>{}, sb, sa);
    STEP2(0) STEP2(2) STEP2(4) STEP2(6) STEP2(8) STEP2(10) STEP2(12) STEP2(14)
    STEP2(16) STEP2(18) STEP2(20) STEP2(22) STEP2(24) STEP2(26) STEP2(28) STEP2(30)
#undef STEP2
    pbp[2] = pack2(e3[0], e3[1]); pbp[3] = pack2(e3[2], e3[3]);
    o[3] = MFMA(as_frag(vf[3][1]), as_frag(pbp), o[3]);
    ST(1, 2);
    asm volatile("" : "+v"(lane));
    r = lane & 31; h = lane >> 5; l16 = lane * 16;
    write_part(o);
  }
  SB;
  if (w < 4) {
    ST(2, 4);
    {
      gu64* f = (gu64*)(ws + F1A) + (qb > 0 ? qb - 1 : 0) * 16 + (lane & 31) - ((lane & 15) >= 14 ? 2 : 0);
      for (unsigned spins = 0; spins < (1u << 20); ++spins) {
        if (__all(__hip_atomic_load(f, __ATOMIC_RELAXED, __HIP_MEMORY_SCOPE_AGENT) == tag)) break;
        __builtin_amdgcn_s_sleep(2);
      }
      asm volatile("" ::: "memory");
    const int ltile = qb * 4 + (w & 3);
    const unsigned lqo = WS_Q + (ltile * 56 + p0) * 512, lko = WS_K + (ltile * 56 + p0) * 512, lvo = WS_V + (head * 64 + ltile) * 2048;
    const unsigned pd = ltile > 0 ? 1 : 0;
    u32x4 lq0 = bload(rs, l16, lqo), lq1 = bload(rs, l16 + 1024, lqo);
    u32x4 lk0 = bload(rs, l16, lko), lk1 = bload(rs, l16 + 1024, lko);
    u32x4 pk0 = bload(rs, l16, lko - pd * 28672), pk1 = bload(rs, l16 + 1024, lko - pd * 28672);
    u32x4 lv0 = bload(rs, l16, lvo), lv1 = bload(rs, l16 + 1024, lvo);
    u32x4 pv1 = bload(rs, l16 + 1024, lvo - pd * 2048);
    set_foreign<ODD>(lq0, lq1, h, 0u, 0u);
    f32x16 sp = MFMA(as_frag(pk0), as_frag(lq0), f32x16{});
    sp = MFMA(as_frag(pk1), as_frag(lq1), sp);
    f32x16 sc = MFMA(as_frag(lk0), as_frag(lq0), f32x16{});
    sc = MFMA(as_frag(lk1), as_frag(lq1), sc);
    const bool first = (ltile == 0);
    const float NEG = -3.0e38f;
    float m = NEG;
#define LM_C(i) { sc[i] = sel_mask(sc[i], NEG, lmask(i, false)); m = fmaxf(m, sc[i]); }
#define LM_P(i) { sp[i] = sel_mask(sp[i], NEG, first ? 0ull : lmask(i, true)); m = fmaxf(m, sp[i]); }
    LM_C(0) LM_C(1) LM_C(2) LM_C(3) LM_C(4) LM_C(5) LM_C(6) LM_C(7)
    LM_C(8) LM_C(9) LM_C(10) LM_C(11) LM_C(12) LM_C(13) LM_C(14) LM_C(15)
    LM_P(8) LM_P(9) LM_P(10) LM_P(11) LM_P(12) LM_P(13) LM_P(14) LM_P(15)
#undef LM_C
#undef LM_P
    m = swap_max(m);
#pragma unroll
    for (int i = 0; i < 16; ++i) sc[i] = __builtin_amdgcn_exp2f(sc[i] - m);
#pragma unroll
    for (int i = 8; i < 16; ++i) sp[i] = __builtin_amdgcn_exp2f(sp[i] - m);
    f32x16 o = MFMA(as_frag(pv1), as_frag(pack_step1<1>(sp)), f32x16{});
    o = MFMA(as_frag(lv0), as_frag(pack_step1<0>(sc)), o);
    o = MFMA(as_frag(lv1), as_frag(pack_step1<1>(sc)), o);
    float ll = __shfl(o[12], 32 + r);
    float inv = 1.f / ll;
    u32x4 o0 = pack_step<0>(o, inv), o1 = pack_step<1>(o, inv);
    if (h) { o1[2] = 0; o1[3] = 0; }
    const unsigned oo = WS_O + ((ltile * 32 + head) * 2) * 1024;
    bstore(o0, rs, l16, oo);
    bstore(o1, rs, l16 + 1024, oo);
    asm volatile("s_waitcnt vmcnt(0)" ::: "memory");
    lds_arrive(smem, 1, 3, ws, F2A + (qb * 16 + head) * 8, tag, lane);
    }
    ST(2, 5);
    if (w == 0) {
      gu64* f = (gu64*)(ws + F1A) + ((lane & 31) >> 1) * 16 + 14 + (lane & 1);
      for (unsigned spins = 0; spins < (1u << 20); ++spins) {
        if (__all(__hip_atomic_load(f, __ATOMIC_RELAXED, __HIP_MEMORY_SCOPE_AGENT) == tag)) break;
        __builtin_amdgcn_s_sleep(2);
      }
      asm volatile("" ::: "memory");
    }
  }
  __syncthreads();
  loadW();
  reduce();
  ST(1, 3);
  {
    float am = fmaxf(fmaxf(fmaxf(fabsf(a0[0]), fabsf(a0[1])), fmaxf(fabsf(a0[2]), fabsf(a0[3]))),
                     fmaxf(fmaxf(fabsf(a1[0]), fabsf(a1[1])), fmaxf(fabsf(a1[2]), fabsf(a1[3]))));
    float chk = (a0[0] + a0[1] + a0[2] + a0[3]) * 0.f + (a1[0] + a1[1] + a1[2] + a1[3]) * 0.f;
    bool bad = !(l < 1.0e30f) || !(am < 3.0e38f) || !(chk == 0.f);
    if (bad) ctl[0] = 1;
    __syncthreads();
  }
  if (__builtin_amdgcn_readfirstlane(ctl[0])) {
#pragma unroll
    for (int i = 0; i < 4; ++i) set_foreign<ODD>(q[i][0], q[i][1], h, 0u, 0u);
    float m[4] = {-3.0e38f, -3.0e38f, -3.0e38f, -3.0e38f};
#pragma unroll 1
    for (int t = 0; t < 8; ++t) {
      u32x4 k0 = bload(rs, l16, koff + t * 28672), k1 = bload(rs, l16 + 1024, koff + t * 28672);
#pragma unroll
      for (int i = 0; i < 4; ++i) {
        f32x16 s = MFMA(as_frag(k0), as_frag(q[i][0]), f32x16{});
        s = MFMA(as_frag(k1), as_frag(q[i][1]), s);
#pragma unroll
        for (int j = 0; j < 16; ++j) m[i] = fmaxf(m[i], s[j]);
      }
    }
#pragma unroll
    for (int i = 0; i < 4; ++i) fold_shift(i, m[i]);
    f32x16 o[4] = {};
#pragma unroll 1
    for (int t = 0; t < 8; ++t) {
      u32x4 k0 = bload(rs, l16, koff + t * 28672), k1 = bload(rs, l16 + 1024, koff + t * 28672);
      u32x4 v0 = bload(rs, l16, voff + t * 2048), v1 = bload(rs, l16 + 1024, voff + t * 2048);
      set_foreign<ODD>(k0, k1, h, 0x3F803F80u, 0x00003F80u);
#pragma unroll
      for (int i = 0; i < 4; ++i) {
        f32x16 s = MFMA(as_frag(k0), as_frag(q[i][0]), f32x16{});
        s = MFMA(as_frag(k1), as_frag(q[i][1]), s);
#pragma unroll
        for (int j = 0; j < 16; ++j) s[j] = __builtin_amdgcn_exp2f(s[j]);
        o[i] = MFMA(as_frag(v0), as_frag(pack_step1<0>(s)), o[i]);
        o[i] = MFMA(as_frag(v1), as_frag(pack_step1<1>(s)), o[i]);
      }
    }
    write_part(o);
    __syncthreads();
    reduce();
    loadW();
  }
  {
    const float inv = 1.f / l;
    u32x4 ov;
    ov[0] = pack2(a0[0] * inv, a0[1] * inv);
    ov[1] = pack2(a0[2] * inv, a0[3] * inv);
    ov[2] = pack2(a1[0] * inv, a1[1] * inv);
    ov[3] = pack2(a1[2] * inv, a1[3] * inv);
    if (h && rs1) { ov[2] = 0; ov[3] = 0; }
    bstore(ov, rs, l16, WS_O + (((qb * 4 + ri) * 32 + 16 + head) * 2 + rs1) * 1024);
  }
  const int oqt = oqb * 4 + oqh * 2 + qt;
  const unsigned ofo = WS_O + ((oqt * 32 + 4 * kq) * 2) * 1024;
  const float gt = gload(ws, oqt * 32 + r);
  if (w == 1) wait_flags16(ws, F2A + oqb * 128, tag, lane);
  asm volatile("s_waitcnt vmcnt(0)" ::: "memory");
  __syncthreads();
  ST(1, 4);
  if (tid == 0) set_flag(ws, F2B + (qb * 16 + head) * 8, tag);
  {
    u32x4 ofr[8];
    f32x16 acc[2][2] = {};
#pragma unroll
    for (int e = 0; e < 8; ++e) ofr[e] = bload(rs, l16 + (e & 3) * 1024, ofo + (e >> 2) * 4096);
#pragma unroll
    for (int e = 0; e < 8; ++e)
#pragma unroll
      for (int nt = 0; nt < 2; ++nt) acc[0][nt] = MFMA(as_frag(wfr[0][nt][e]), as_frag(ofr[e]), acc[0][nt]);
    if (w == 0) wait_flags16(ws, F2B + oqb * 128, tag, lane);
    __syncthreads();
    ST(1, 5);
#pragma unroll
    for (int e = 0; e < 8; ++e) ofr[e] = bload(rs, l16 + (e & 3) * 1024, ofo + 32768 + (e >> 2) * 4096);
#pragma unroll
    for (int e = 0; e < 8; ++e)
#pragma unroll
      for (int nt = 0; nt < 2; ++nt) acc[1][nt] = MFMA(as_frag(wfr[1][nt][e]), as_frag(ofr[e]), acc[1][nt]);
    ST(1, 6);
#pragma unroll
    for (int nt = 0; nt < 2; ++nt)
#pragma unroll
      for (int g = 0; g < 4; ++g) {
        f32x4 v;
#pragma unroll
        for (int e = 0; e < 4; ++e) v[e] = acc[0][nt][4 * g + e] * gt + acc[1][nt][4 * g + e] * (1.f - gt);
        part[(((nt * 2 + qt) * 4 + kq) * 4 + g) * 64 + lane] = v;
      }
  }
  __syncthreads();
  {
    const int ft = w & 3, fg = w >> 2;
    f32x4 s0 = {}, s1 = {};
#pragma unroll
    for (int k = 0; k < 4; ++k) {
      s0 += part[((ft * 4 + k) * 4 + 2 * fg) * 64 + lane];
      s1 += part[((ft * 4 + k) * 4 + 2 * fg + 1) * 64 + lane];
    }
    const u32x4 ov = widen(pack2(s0[0], s0[1]), pack2(s0[2], s0[3]), pack2(s1[0], s1[1]), pack2(s1[2], s1[3]));
    const int col = 32 * (ft >> 1) + 16 * fg + 8 * h;
    if (col < 56) *(u32x4*)(out + (oqb * 128 + oqh * 64 + (ft & 1) * 32 + r) * DM + nb * 56 + col) = ov;
  }
}

__global__ __launch_bounds__(512, 2) void fused_block(const u16* __restrict__ x, const u16* __restrict__ WA, const u16* __restrict__ WB,
                                                      const u16* __restrict__ gate_w, const u16* __restrict__ WoA,
                                                      const u16* __restrict__ WoB, char* __restrict__ ws, u16* __restrict__ out) {
  extern __shared__ __attribute__((aligned(16))) char smem_[];
  char LDSP* smem = (char LDSP*)smem_;
  const rsrc_t rs = __builtin_amdgcn_make_buffer_rsrc(ws, 0, 32u << 20, 0x00020000);
  u64 tag = __builtin_amdgcn_s_getpc();
  ST(0, 0);
  u32x4 sv[8];
  unsigned so[8];
  const int b = blockIdx.x, xcd = b & 7, j = b >> 3;
  if (j < 28) {
    const int mt = 4 * (xcd & 3) + (j & 3);
    const int nt = 7 * (xcd >> 2) + (j >> 2);
    if (__builtin_amdgcn_readfirstlane(threadIdx.x >> 8) == 0) {
      if (nt < 9) p1_body<2, 0>(x, WA, WB, ws, rs, mt, nt, smem, tag, sv, so);
      else if (nt == 9) p1_body<2, 1>(x, WA, WB, ws, rs, mt, nt, smem, tag, sv, so);
      else p1_body<2, 2>(x, WA, WB, ws, rs, mt, nt, smem, tag, sv, so);
    } else {
      if (nt < 9) p1_body<1, 0>(x, WA, WB, ws, rs, mt, nt, smem, tag, sv, so);
      else p1_body<1, 1>(x, WA, WB, ws, rs, mt, nt, smem, tag, sv, so);
    }
  } else {
    warm_code(smem, tag);
    p1_spare(x, gate_w, WoA, WoB, ws, rs, xcd * 4 + j - 28, smem, tag);
#pragma unroll
    for (int e = 0; e < 8; ++e) { sv[e] = u32x4{}; so[e] = DROP; }
  }
  ST(0, 1);
  const int sp = xcd * 4 + j - 28;
  const int fidx = j < 28 ? (4 * (xcd & 3) + (j & 3)) * 16 + 7 * (xcd >> 2) + (j >> 2) : (sp >> 1) * 16 + 14 + (sp & 1);
  if ((b >> 3) & 1) p2_body<1>(ws, out, rs, smem, tag, fidx, sv, so);
  else p2_body<0>(ws, out, rs, smem, tag, fidx, sv, so);
}

extern "C" void kernel_launch(void* const* d_in, const int* in_sizes, int n_in, void* d_out, int out_size, void* d_ws,
                              size_t ws_size, hipStream_t stream) {
  const u16* x = (const u16*)d_in[0];
  const u16* WA = (const u16*)d_in[1];
  const u16* WoA = (const u16*)d_in[2];
  const u16* WB = (const u16*)d_in[4];
  const u16* WoB = (const u16*)d_in[5];
  const u16* gw = (const u16*)d_in[6];
  (void)hipFuncSetAttribute((const void*)fused_block, hipFuncAttributeMaxDynamicSharedMemorySize, LDS_BYTES);
  fused_block<<<256, 512, LDS_BYTES, stream>>>(x, WA, WB, gw, WoA, WoB, (char*)d_ws, (u16*)d_out);
}
```

```cpp
#include <hip/hip_runtime.h>
#include <hip/hip_bf16.h>
#include <stdint.h>
#include <type_traits>

typedef short bf16x8 __attribute__((ext_vector_type(8)));
typedef short s16x4 __attribute__((ext_vector_type(4)));
typedef float f32x16 __attribute__((ext_vector_type(16)));
typedef float f32x4 __attribute__((ext_vector_type(4)));
typedef float f32x2 __attribute__((ext_vector_type(2)));
typedef unsigned u32x4 __attribute__((ext_vector_type(4)));
typedef unsigned u32x2 __attribute__((ext_vector_type(2)));
typedef __bf16 bf2 __attribute__((ext_vector_type(2)));
typedef unsigned short u16;

#define DI __device__ __forceinline__
#define MFMA(a, b, c) __builtin_amdgcn_mfma_f32_32x32x16_bf16((a), (b), (c), 0, 0, 0)
#define LDSP __attribute__((address_space(3)))
#define GLBP __attribute__((address_space(1)))

constexpr int SEQ = 2048, DM = 448, NH = 16, DH = 28, N3 = 1344;
constexpr float QSCALE = 0.18898223650461362f * 1.4426950408889634f;

#define ST(k, i)
constexpr unsigned WS_Q = 0;
constexpr unsigned WS_K = 4u << 20;
constexpr unsigned WS_V = 8u << 20;
constexpr unsigned WS_O = 12u << 20;
constexpr unsigned WS_G = 16u << 20;
constexpr unsigned WS_W = 17u << 20;
constexpr unsigned WS_C = 19u << 20;
constexpr unsigned F1B = WS_C + 4096, F1A = WS_C + 8192, F2A = WS_C + 12288, F2B = WS_C + 16384;
constexpr unsigned QK_BR = 64 * 56 * 512;

DI unsigned pack2(float lo, float hi) {
  f32x2 v = {lo, hi};
  return __builtin_bit_cast(unsigned, __builtin_convertvector(v, bf2));
}
DI bf16x8 as_frag(u32x4 v) { return __builtin_bit_cast(bf16x8, v); }
DI float bf_lo(unsigned v) { return __uint_as_float(v << 16); }
DI float bf_hi(unsigned v) { return __uint_as_float(v & 0xffff0000u); }
DI float swap_max(float v) {
  auto rr = __builtin_amdgcn_permlane32_swap(__float_as_uint(v), __float_as_uint(v), false, false);
  return fmaxf(__uint_as_float(rr[0]), __uint_as_float(rr[1]));
}
DI s16x4 tr_read(const char LDSP* p) {
  return __builtin_amdgcn_ds_read_tr16_b64_v4i16((s16x4 LDSP*)p);
}
DI bf16x8 cat(s16x4 a, s16x4 b) { return __builtin_shufflevector(a, b, 0, 1, 2, 3, 4, 5, 6, 7); }
template <int S>
DI u32x4 pack_step(const f32x16& x, float sc) {
  u32x4 p;
  p[0] = pack2(x[8 * S + 0] * sc, x[8 * S + 1] * sc);
  p[1] = pack2(x[8 * S + 2] * sc, x[8 * S + 3] * sc);
  p[2] = pack2(x[8 * S + 4] * sc, x[8 * S + 5] * sc);
  p[3] = pack2(x[8 * S + 6] * sc, x[8 * S + 7] * sc);
  return p;
}
template <int S>
DI u32x4 pack_step1(const f32x16& x) {
  u32x4 p;
  p[0] = pack2(x[8 * S + 0], x[8 * S + 1]);
  p[1] = pack2(x[8 * S + 2], x[8 * S + 3]);
  p[2] = pack2(x[8 * S + 4], x[8 * S + 5]);
  p[3] = pack2(x[8 * S + 6], x[8 * S + 7]);
  return p;
}

#define RAWBAR() do { asm volatile("" ::: "memory"); __builtin_amdgcn_s_barrier(); asm volatile("" ::: "memory"); } while (0)
#define WAITV(n) ((((n) & 15) | (((n) >> 4) << 14)) | 0x0F70)
#define WAITVL(n) ((((n) & 15) | (((n) >> 4) << 14)) | 0x0070)
DI u32x4 widen(unsigned a0, unsigned a1, unsigned b0, unsigned b1) {
  auto r0 = __builtin_amdgcn_permlane32_swap(a0, b0, false, false);
  auto r1 = __builtin_amdgcn_permlane32_swap(a1, b1, false, false);
  return u32x4{r0[0], r1[0], r0[1], r1[1]};
}

typedef __amdgpu_buffer_rsrc_t rsrc_t;
typedef unsigned v4u_t __attribute__((ext_vector_type(4)));
typedef unsigned long long u64;
typedef GLBP u64 gu64;
DI u32x4 bload(rsrc_t rs, unsigned voff, unsigned soff) {
  return __builtin_bit_cast(u32x4, __builtin_amdgcn_raw_buffer_load_b128(rs, voff, soff, 16));
}
DI void bstore(u32x4 v, rsrc_t rs, unsigned voff, unsigned soff) {
  __builtin_amdgcn_raw_buffer_store_b128(__builtin_bit_cast(v4u_t, v), rs, voff + soff, 0, 16);
}
DI void set_flag(char* ws, unsigned off, u64 tag) {
  __hip_atomic_store((gu64*)(ws + off), tag, __ATOMIC_RELAXED, __HIP_MEMORY_SCOPE_AGENT);
}
DI u64 read_tag(const char* ws) {
  u64 e;
  asm volatile("s_load_dwordx2 %0, %1, 0x0\n\ts_waitcnt lgkmcnt(0)" : "=s"(e) : "s"(ws + WS_C) : "memory");
  return e + 1;
}
DI void wait_flags256(char* ws, unsigned off, u64 tag, int lane) {
  gu64* f = (gu64*)(ws + off) + lane;
  for (unsigned spins = 0; spins < (1u << 20); ++spins) {
    bool ok = true;
#pragma unroll
    for (int k = 0; k < 4; ++k) ok &= __hip_atomic_load(f + 64 * k, __ATOMIC_RELAXED, __HIP_MEMORY_SCOPE_AGENT) == tag;
    if (__all(ok)) break;
    __builtin_amdgcn_s_sleep(2);
  }
  asm volatile("" ::: "memory");
}
DI void wait_flags16(char* ws, unsigned off, u64 tag, int lane) {
  gu64* f = (gu64*)(ws + off) + (lane & 15);
  for (unsigned spins = 0; spins < (1u << 20); ++spins) {
    if (__all(__hip_atomic_load(f, __ATOMIC_RELAXED, __HIP_MEMORY_SCOPE_AGENT) == tag)) break;
    __builtin_amdgcn_s_sleep(1);
  }
  asm volatile("" ::: "memory");
}

constexpr int P1_WSLOT = 64 * 192;
constexpr int P1_SLOT = 16384 + P1_WSLOT;
constexpr int P1_CTL = 5 * P1_SLOT + P1_WSLOT;
constexpr int P1_SINK = P1_CTL + 64;
constexpr int LDS_BYTES = P1_SINK + 256;

constexpr unsigned DROP = 0x80000000u;
template <bool V>
DI void p1_pack(const f32x16& a, int br, int ct, int tile, int lane, u32x4* sv, unsigned* so) {
  const int type = ct / 14, cbase = (ct % 14) * 32;
  if (!V) {
    const float sc = type == 0 ? QSCALE : 1.f;
    const unsigned o = (type == 0 ? WS_Q : WS_K) + br * QK_BR + (tile * 56 + (cbase >> 3)) * 512 + lane * 16;
#pragma unroll
    for (int g = 0; g < 2; ++g) {
      sv[g] = widen(pack2(a[8 * g] * sc, a[8 * g + 1] * sc), pack2(a[8 * g + 2] * sc, a[8 * g + 3] * sc),
                    pack2(a[8 * g + 4] * sc, a[8 * g + 5] * sc), pack2(a[8 * g + 6] * sc, a[8 * g + 7] * sc));
      so[g] = o + g * 1024;
    }
  } else {
    const unsigned f = cbase + (lane & 31), head = f / 28, d = f % 28;
    const unsigned o = WS_V + ((br * 16 * 64 + tile) * 2) * 1024 + (head * 64 * 2 * 64 + d + (lane & 32)) * 16;
    sv[0] = pack_step1<0>(a); so[0] = o;
    sv[1] = pack_step1<1>(a); so[1] = o + 1024;
    const unsigned c = d == 0 ? 0x3F803F80u : 0u;
    sv[2] = u32x4{c, c, c, c}; so[2] = d < 4 ? o + 28 * 16 : DROP;
    sv[3] = sv[2]; so[3] = d < 4 ? o + 28 * 16 + 1024 : DROP;
  }
}

DI void warm_l2(const u16* x, const u16* WA, const u16* WB, char LDSP* smem, const u64 pc) {
  const int b = blockIdx.x, xcd = b & 7;
  const unsigned g = ((b >> 3) - 28) * 512 + threadIdx.x;
  constexpr unsigned len = 60 * 1024;
  __builtin_amdgcn_global_load_lds((const GLBP void*)((const char*)pc + (g * 128 + 4 <= len ? g * 128 : 0u)), (LDSP void*)(smem + P1_SINK), 4, 0, 0);
#pragma unroll
  for (int e = 0; e < 4; ++e) {
    const unsigned i = e * 2048 + g;
    const char* p;
    if (i < 1024) {
      p = (const char*)x + ((xcd & 3) * 512 + (i >> 1)) * 896 + 640 + (i & 1) * 128;
    } else {
      const unsigned idx = i - 1024, row = idx / 12, c = idx % 12;
      unsigned off = (row < 128 ? row + 320 : row - 128) * 2688 + (xcd >> 2) * 1280 + c * 128;
      off = off < (unsigned)(DM * N3 * 2 - 4) ? off : 0u;
      p = (const char*)(row < 128 ? WB : WA) + off;
    }
    __builtin_amdgcn_global_load_lds((const GLBP void*)p, (LDSP void*)(smem + P1_SINK), 4, 0, 0);
  }
}

DI void lds_arrive(char LDSP* smem, int idx, int last, char* ws, unsigned flag_off, u64 tag, int lane) {
  if (lane == 0) {
    if (__hip_atomic_fetch_add((int LDSP*)(smem + P1_CTL) + idx, 1, __ATOMIC_RELAXED, __HIP_MEMORY_SCOPE_WORKGROUP) == last) set_flag(ws, flag_off, tag);
  }
}

template <int NT, int NV>
DI void p1_body(const u16* __restrict__ x, const u16* __restrict__ WA, const u16* __restrict__ WB, char* __restrict__ ws, rsrc_t rs,
                int mt, int nt, char LDSP* smem, u64& tag, u32x4 (&sv)[8], unsigned (&so)[8]) {
  const int tid = threadIdx.x, lane = tid & 63;
  const int w = __builtin_amdgcn_readfirstlane(tid >> 6);
  const int msub = w & 3;
  const int j0 = NT == 2 ? 0 : 2;
  const int r = lane & 31, h = lane >> 5;
  constexpr int NS = 2 * (NT - NV) + 4 * NV;
  constexpr int NW = NT == 2 ? 1 : 2;
  const int wi0 = NT == 2 ? w : 2 * w - 4;
  const u16* xs[2];
  int wso[2];
#pragma unroll
  for (int i = 0; i < 2; ++i) {
    int lr = (w * 2 + i) * 8 + (lane >> 3);
    int logical = (lane & 7) ^ ((lr >> 1) & 7);
    xs[i] = x + (mt * 128 + lr) * DM + logical * 8;
    const int id = (wi0 + i) * 64 + lane;
    wso[i] = (id / 12) * N3 + nt * 96 + (id % 12) * 8;
  }
  auto wbase = [&](int s) {
    return s < 7 ? (s % 5) * P1_SLOT + 16384 : (s < 13 ? (2 + (s - 7) / 2) * P1_SLOT + ((s - 7) & 1) * P1_WSLOT : 5 * P1_SLOT);
  };
  auto issue_x = [&](int c, int i) {
    __builtin_amdgcn_global_load_lds((const GLBP void*)(xs[i] + c * 64), (LDSP void*)(smem + (c % 5) * P1_SLOT + (w * 2 + i) * 1024), 16, 0, 0);
  };
  auto issue_w = [&](int s) {
#pragma unroll
    for (int i = 0; i < NW; ++i)
      __builtin_amdgcn_global_load_lds((const GLBP void*)((s < 7 ? WB : WA) + wso[i] + (s % 7) * 64 * N3),
                                       (LDSP void*)(smem + wbase(s) + (wi0 + i) * 1024), 16, 0, 0);
  };
  int xoff[4];
  {
    int lr = 32 * msub + r, sw = (lr >> 1) & 7;
#pragma unroll
    for (int t = 0; t < 4; ++t) xoff[t] = lr * 128 + (((2 * t + h) ^ sw) << 4);
  }
  const int q4 = (lane & 15) >> 2, p4 = lane & 3, g16 = (lane >> 4) & 1;
  const int woff = (8 * h + q4) * 192 + 64 * j0 + 32 * g16 + 8 * p4;

  f32x16 acc[NT] = {};
  bf16x8 xf[7][4];
  s16x4 wl[2][4][NT], wh[2][4][NT];
  auto readf = [&](int s, int m) {
    const int t = m / NT, j = m % NT;
    if (j == 0 && s < 7) xf[s][t] = *(const bf16x8 LDSP*)(smem + (s % 5) * P1_SLOT + xoff[t]);
    const char LDSP* wp = smem + woff + wbase(s) + t * 16 * 192 + j * 64;
    wl[s & 1][t][j] = tr_read(wp);
    wh[s & 1][t][j] = tr_read(wp + 4 * 192);
  };
#pragma unroll
  for (int c = 0; c < 5; ++c) { issue_x(c, 0); issue_x(c, 1); issue_w(c); }
  issue_w(13);
  tag = read_tag(ws);
  if (tid < 8) ((int LDSP*)(smem + P1_CTL))[tid] = 0;
  __builtin_amdgcn_s_waitcnt(WAITVL(4 * (NW + 2) + NW));
  RAWBAR();
#pragma unroll
  for (int m = 0; m < 4 * NT; ++m) readf(0, m);
#pragma unroll
  for (int s = 0; s < 14; ++s) {
    if (s < 7 || s == 8) {
      if (s < 3) __builtin_amdgcn_s_waitcnt(WAITVL(3 * (NW + 2) + NW));
      else if (s == 3) __builtin_amdgcn_s_waitcnt(WAITVL(2 * (NW + 2) + 3 * NW));
      else if (s == 4) __builtin_amdgcn_s_waitcnt(WAITVL(NW + 2 + 4 * NW));
      else if (s == 5) __builtin_amdgcn_s_waitcnt(WAITVL(6 * NW));
      else if (s == 6) __builtin_amdgcn_s_waitcnt(WAITVL(4 * NW));
      else __builtin_amdgcn_s_waitcnt(WAITVL(NS));
      RAWBAR();
    }
    if (s == 12) {
      __builtin_amdgcn_s_waitcnt(WAITV(0));
      asm volatile("" ::: "memory");
      lds_arrive(smem, 4, 7, ws, F1B + (nt * 16 + mt) * 8, tag, lane);
    }
    __builtin_amdgcn_sched_barrier(0);
#pragma unroll
    for (int m = 0; m < 4 * NT; ++m) {
      const int t = m / NT, j = m % NT;
      const bf16x8 wfr = cat(wl[s & 1][t][j], wh[s & 1][t][j]);
      if (j < NT - NV) acc[j] = MFMA(wfr, xf[s % 7][t], acc[j]);
      else acc[j] = MFMA(xf[s % 7][t], wfr, acc[j]);
      __builtin_amdgcn_sched_barrier(0);
      if (s < 13) readf(s + 1, m);
      if (j == 0) {
        if (s < 2) { if (t < 2) issue_x(s + 5, t); else if (t == 2) issue_w(s + 5); }
        else if (s < 5 && t < 2) issue_w(7 + 2 * (s - 2) + t);
      }
      __builtin_amdgcn_sched_barrier(0);
    }
    if (s == 6 || s == 13) {
#pragma unroll
      for (int e = 0; e < 8; ++e) so[e] = DROP;
#pragma unroll
      for (int j = 0; j < NT; ++j) {
        if (j < NT - NV) p1_pack<false>(acc[j], s == 6, nt * 3 + j0 + j, mt * 4 + msub, lane, sv + 4 * j, so + 4 * j);
        else p1_pack<true>(acc[j], s == 6, nt * 3 + j0 + j, mt * 4 + msub, lane, sv + 4 * j, so + 4 * j);
        if (s == 6) {
#pragma unroll
          for (int e = 0; e < (j < NT - NV ? 2 : 4); ++e) bstore(sv[4 * j + e], rs, so[4 * j + e], 0);
        }
        acc[j] = f32x16{};
      }
      __builtin_amdgcn_sched_barrier(0);
    }
  }
}

DI void p1_spare(const u16* __restrict__ x, const u16* __restrict__ gate_w, const u16* __restrict__ WoA, const u16* __restrict__ WoB,
                 char* __restrict__ ws, rsrc_t rs, int sp, char LDSP* smem, u64& tag) {
  const int tid = threadIdx.x, lane = tid & 63, r = lane & 31, h = lane >> 5;
  tag = read_tag(ws);
  if (tid < 8) ((int LDSP*)(smem + P1_CTL))[tid] = 0;
  __syncthreads();
  if (tid == 0) set_flag(ws, F1B + ((14 + (sp & 1)) * 16 + (sp >> 1)) * 8, tag);
#pragma unroll
  for (int e = 0; e < 4; ++e) {
    const int fid = sp * 32 + (tid >> 6) * 4 + e;
    const int sfr = fid & 1, vh = (fid >> 1) & 31, nt = (fid >> 6) & 1, nb = fid >> 7;
    const int n = 56 * nb + 32 * nt + r;
    const u16* Wo = (vh >> 4) ? WoB : WoA;
    unsigned v[8];
#pragma unroll
    for (int jj = 0; jj < 8; ++jj) {
      const int d = 16 * sfr + 8 * (jj >> 2) + 4 * h + (jj & 3);
      const bool ok = d < DH && n < DM;
      const unsigned tv = Wo[ok ? ((vh & 15) * DH + d) * DM + n : 0];
      v[jj] = ok ? tv : 0u;
    }
    bstore(u32x4{v[0] | (v[1] << 16), v[2] | (v[3] << 16), v[4] | (v[5] << 16), v[6] | (v[7] << 16)}, rs, WS_W + fid * 1024 + lane * 16, 0);
  }
  const int row = sp * 64 + (tid >> 3), part = tid & 7;
  const u32x4* xp = (const u32x4*)(x + row * DM) + part;
  const u32x4* gp = (const u32x4*)(gate_w) + part;
  float acc = 0.f;
#pragma unroll
  for (int i = 0; i < 7; ++i) {
    u32x4 a = xp[i * 8], bb = gp[i * 8];
#pragma unroll
    for (int e = 0; e < 4; ++e) {
      acc = fmaf(bf_lo(a[e]), bf_lo(bb[e]), acc);
      acc = fmaf(bf_hi(a[e]), bf_hi(bb[e]), acc);
    }
  }
  acc += __shfl_xor(acc, 1);
  acc += __shfl_xor(acc, 2);
  acc += __shfl_xor(acc, 4);
  if (part == 0)
    __hip_atomic_store((GLBP float*)(ws + WS_G) + row, 1.f / (1.f + __expf(-acc)), __ATOMIC_RELAXED, __HIP_MEMORY_SCOPE_AGENT);
}

constexpr unsigned long long lmask(int i, bool prev) {
  unsigned long long m = 0;
  for (int l = 0; l < 64; ++l) {
    int r = l & 31, h = l >> 5;
    int kr = (i & 3) + 8 * (i >> 2) + 4 * h;
    int diff = r - kr + (prev ? 32 : 0);
    if (diff >= 0 && diff <= 14 && (diff & 1) == 0) m |= 1ull << l;
  }
  return m;
}
DI float sel_mask(float val, float other, unsigned long long mask) {
  return __builtin_amdgcn_inverse_ballot_w64(mask) ? val : other;
}
template <int ODD>
DI void set_foreign(u32x4& f0, u32x4& f1, int h, unsigned a, unsigned b) {
  if (ODD) { if (!h) { f0[0] = a; f0[1] = b; } }
  else { if (h) { f1[2] = a; f1[3] = b; } }
}
DI float gload(char* ws, int idx) { return __hip_atomic_load((GLBP float*)(ws + WS_G) + idx, __ATOMIC_RELAXED, __HIP_MEMORY_SCOPE_AGENT); }

template <int ODD>
DI void p2_body(char* __restrict__ ws, u16* __restrict__ out, rsrc_t rs, char LDSP* smem, const u64 tag, const int fidx,
                const u32x4 (&sv)[8], const unsigned (&so)[8]) {
  f32x4 LDSP* part = (f32x4 LDSP*)smem;
  float LDSP* cw = (float LDSP*)(smem + 131072);
  volatile int LDSP* ctl = (volatile int LDSP*)(smem + P1_CTL);
  const int tid = threadIdx.x;
  int lane = tid & 63;
  const int w = __builtin_amdgcn_readfirstlane(tid >> 6);
  int r = lane & 31, h = lane >> 5;
  const int b = blockIdx.x;
  const int head = (b & 7) * 2 + ODD, qb = b >> 4;
  const int oqb = (b & 7) * 2 + (b >> 7), oqh = (b >> 3) & 1, nb = (b >> 4) & 7;
  const unsigned p0 = (7 * head) >> 1;
  unsigned l16 = lane * 16;
  const unsigned qoff = WS_Q + QK_BR + (qb * 4 * 56 + p0) * 512;
  const unsigned koff = WS_K + QK_BR + (w * 8 * 56 + p0) * 512;
  const unsigned voff = WS_V + ((16 + head) * 64 + w * 8) * 2048;
  if (w == 0) {
    const int sel = lane >> 4;
    const int c0 = sel < 2 ? 448 + 8 * (int)p0 + (sel & 1) * 31 : 896 + 28 * head + (sel & 1) * 27;
    gu64* f = (gu64*)(ws + F1B) + (c0 / 96) * 16 + (lane & 15);
    gu64* fq = (gu64*)(ws + F1B) + ((8 * (int)p0 + (lane & 1) * 31) / 96) * 16 + qb;
    for (unsigned spins = 0; spins < (1u << 20); ++spins) {
      bool ok = __hip_atomic_load(f, __ATOMIC_RELAXED, __HIP_MEMORY_SCOPE_AGENT) == tag;
      ok &= __hip_atomic_load(fq, __ATOMIC_RELAXED, __HIP_MEMORY_SCOPE_AGENT) == tag;
      if (__all(ok)) break;
      __builtin_amdgcn_s_sleep(1);
    }
    asm volatile("" ::: "memory");
  }
  __syncthreads();
  ST(1, 0);
  ST(2, 0);
  u32x4 q[4][2];
  u32x4 kf[4][2], vf[4][2];
  auto loadkv = [&](int t) {
    kf[t & 3][0] = bload(rs, l16, koff + t * 28672);
    kf[t & 3][1] = bload(rs, l16 + 1024, koff + t * 28672);
    vf[t & 3][0] = bload(rs, l16, voff + t * 2048);
    vf[t & 3][1] = bload(rs, l16 + 1024, voff + t * 2048);
  };
  auto store_a = [&]() {
#pragma unroll
    for (int e = 0; e < 8; ++e) bstore(sv[e], rs, so[e], 0);
  };
  if (w >= 4) {
    store_a();
    asm volatile("s_waitcnt vmcnt(0)" ::: "memory");
    lds_arrive(smem, 2, 7, ws, F1A + fidx * 8, tag, lane);
    if (b == 0 && w == 5) {
      wait_flags256(ws, F1B, tag, lane);
      if (lane == 0) set_flag(ws, WS_C, tag);
    }
    __builtin_amdgcn_s_setprio(1);
  }
  kf[0][0] = bload(rs, l16, koff);
  kf[0][1] = bload(rs, l16 + 1024, koff);
#pragma unroll
  for (int i = 0; i < 4; ++i) { q[i][0] = bload(rs, l16, qoff + i * 28672); q[i][1] = bload(rs, l16 + 1024, qoff + i * 28672); }
  vf[0][0] = bload(rs, l16, voff);
  vf[0][1] = bload(rs, l16 + 1024, voff);
  loadkv(1);
  loadkv(2);
  if (w < 4) store_a();
  __builtin_amdgcn_sched_barrier(0);

  auto fold_shift = [&](int i, float m) {
    m = swap_max(m);
    if (!h) cw[(i * 32 + r) * 8 + w] = m;
    const float c = -m;
    unsigned p01 = pack2(c, 0.f);
    float r1 = c - bf_lo(p01);
    unsigned p1 = pack2(r1, 0.f);
    float r2 = r1 - bf_lo(p1);
    unsigned p2 = pack2(r2, 0.f);
    set_foreign<ODD>(q[i][0], q[i][1], h, (p01 & 0xffffu) | (p1 << 16), p2 & 0xffffu);
  };
  const int ri = w >> 1, rs1 = w & 1;
  f32x4 a0, a1;
  float l;
  auto write_part = [&](f32x16 (&o)[4]) {
#pragma unroll
    for (int i = 0; i < 4; ++i)
#pragma unroll
      for (int g = 0; g < 4; ++g)
        part[((w * 4 + i) * 4 + g) * 64 + lane] = f32x4{o[i][4 * g], o[i][4 * g + 1], o[i][4 * g + 2], o[i][4 * g + 3]};
  };
  auto reduce = [&]() {
    const f32x4 c0 = *(f32x4 LDSP*)(cw + (ri * 32 + r) * 8), c1 = *(f32x4 LDSP*)(cw + (ri * 32 + r) * 8 + 4);
    const float cm = fmaxf(fmaxf(fmaxf(c0[0], c0[1]), fmaxf(c0[2], c0[3])), fmaxf(fmaxf(c1[0], c1[1]), fmaxf(c1[2], c1[3])));
    a0 = f32x4{}; a1 = f32x4{}; l = 0.f;
#pragma unroll
    for (int ww = 0; ww < 8; ++ww) {
      const float wt = __builtin_amdgcn_exp2f((ww < 4 ? c0[ww & 3] : c1[ww & 3]) - cm);
      a0 += part[((ww * 4 + ri) * 4 + 2 * rs1) * 64 + lane] * wt;
      a1 += part[((ww * 4 + ri) * 4 + 2 * rs1 + 1) * 64 + lane] * wt;
      l += ((float LDSP*)(part + ((ww * 4 + ri) * 4 + 3) * 64 + 32 + r))[0] * wt;
    }
  };
  const int qt = w & 1, kq = w >> 1;
  u32x4 wfr[2][2][8];
  auto loadW = [&]() {
#pragma unroll
    for (int br = 0; br < 2; ++br)
#pragma unroll
      for (int nt = 0; nt < 2; ++nt) {
        const unsigned wo = WS_W + (((nb * 2 + nt) * 32 + br * 16 + 4 * kq) * 2) * 1024;
#pragma unroll
        for (int e = 0; e < 8; ++e) wfr[br][nt][e] = bload(rs, l16 + (e & 3) * 1024, wo + (e >> 2) * 4096);
      }
  };

#pragma unroll
  for (int i = 0; i < 4; ++i) {
    set_foreign<ODD>(q[i][0], q[i][1], h, 0u, 0u);
    f32x16 s = MFMA(as_frag(kf[0][0]), as_frag(q[i][0]), f32x16{});
    s = MFMA(as_frag(kf[0][1]), as_frag(q[i][1]), s);
    fold_shift(i, fmaxf(fmaxf(s[0], s[1]), s[2]));
  }
  __builtin_amdgcn_s_setprio(0);
  ST(1, 1);
  {
    f32x16 o[4] = {};
    set_foreign<ODD>(kf[0][0], kf[0][1], h, 0x3F803F80u, 0x00003F80u);
    f32x16 sa = MFMA(as_frag(kf[0][0]), as_frag(q[0][0]), f32x16{});
    sa = MFMA(as_frag(kf[0][1]), as_frag(q[0][1]), sa);
    f32x16 sb;
    u32x4 pa;
    u32x4 pbp;
    float e3[4];
#define SB __builtin_amdgcn_sched_barrier(0)
#define EXPQ(S, g) { S[4 * g] = __builtin_amdgcn_exp2f(S[4 * g]); S[4 * g + 1] = __builtin_amdgcn_exp2f(S[4 * g + 1]); \
                     S[4 * g + 2] = __builtin_amdgcn_exp2f(S[4 * g + 2]); S[4 * g + 3] = __builtin_amdgcn_exp2f(S[4 * g + 3]); }
    auto step = [&](auto nc, f32x16& sc, f32x16& sn) {
      constexpr int n = decltype(nc)::value;
      constexpr int t = n >> 2, i = n & 3;
      constexpr int t1 = ((n + 1) >> 2) & 3, i1 = (n + 1) & 3;
      constexpr int tp = n > 0 ? ((n - 1) >> 2) & 3 : 0, ip = n > 0 ? (n - 1) & 3 : 0;
      if ((n & 7) == 0 && n > 0) ST(2, n >> 3);
      if (n == 8) asm volatile("" :: "v"(e3[0]), "v"(e3[1]), "v"(e3[2]), "v"(e3[3]), "v"(pbp[0]), "v"(pbp[1]));
      if (n == 8 && w < 4) {
        asm volatile("s_waitcnt vmcnt(8)" ::: "memory");
        lds_arrive(smem, 2, 7, ws, F1A + fidx * 8, tag, lane);
      }
      if (i == 1 && t + 3 < 8) loadkv(t + 3);
      if (i == 3 && n + 1 < 32) set_foreign<ODD>(kf[t1][0], kf[t1][1], h, 0x3F803F80u, 0x00003F80u);
      SB;
      if (n + 1 < 32) sn = MFMA(as_frag(kf[t1][0]), as_frag(q[i1][0]), f32x16{});
      SB;
      if (n > 0) { pbp[2] = pack2(e3[0], e3[1]); pbp[3] = pack2(e3[2], e3[3]); }
      SB;
      EXPQ(sc, 0);
      SB;
      if (n + 1 < 32) sn = MFMA(as_frag(kf[t1][1]), as_frag(q[i1][1]), sn);
      SB;
      pa[0] = pack2(sc[0], sc[1]); pa[1] = pack2(sc[2], sc[3]);
      SB;
      EXPQ(sc, 1);
      SB;
      if (n > 0) o[ip] = MFMA(as_frag(vf[tp][1]), as_frag(pbp), o[ip]);
      SB;
      pa[2] = pack2(sc[4], sc[5]); pa[3] = pack2(sc[6], sc[7]);
      SB;
      EXPQ(sc, 2);
      SB;
      o[i] = MFMA(as_frag(vf[t & 3][0]), as_frag(pa), o[i]);
      SB;
      pbp[0] = pack2(sc[8], sc[9]); pbp[1] = pack2(sc[10], sc[11]);
      SB;
      EXPQ(sc, 3);
      e3[0] = sc[12]; e3[1] = sc[13]; e3[2] = sc[14]; e3[3] = sc[15];
      SB;
    };
#define STEP2(n) step(std::integral_constant<int, n>{}, sa, sb); step(std::integral_constant<int, n + 1>{}, sb, sa);
    STEP2(0) STEP2(2) STEP2(4) STEP2(6) STEP2(8) STEP2(10) STEP2(12) STEP2(14)
    STEP2(16) STEP2(18) STEP2(20) STEP2(22) STEP2(24) STEP2(26) STEP2(28) STEP2(30)
#undef STEP2
    pbp[2] = pack2(e3[0], e3[1]); pbp[3] = pack2(e3[2], e3[3]);
    o[3] = MFMA(as_frag(vf[3][1]), as_frag(pbp), o[3]);
    ST(1, 2);
    asm volatile("" : "+v"(lane));
    r = lane & 31; h = lane >> 5; l16 = lane * 16;
    write_part(o);
  }
  SB;
  if (w < 4) {
    ST(2, 4);
    {
      gu64* f = (gu64*)(ws + F1A) + (qb > 0 ? qb - 1 : 0) * 16 + (lane & 31) - ((lane & 15) >= 14 ? 2 : 0);
      for (unsigned spins = 0; spins < (1u << 20); ++spins) {
        if (__all(__hip_atomic_load(f, __ATOMIC_RELAXED, __HIP_MEMORY_SCOPE_AGENT) == tag)) break;
        __builtin_amdgcn_s_sleep(2);
      }
      asm volatile("" ::: "memory");
    const int ltile = qb * 4 + (w & 3);
    const unsigned lqo = WS_Q + (ltile * 56 + p0) * 512, lko = WS_K + (ltile * 56 + p0) * 512, lvo = WS_V + (head * 64 + ltile) * 2048;
    const unsigned pd = ltile > 0 ? 1 : 0;
    u32x4 lq0 = bload(rs, l16, lqo), lq1 = bload(rs, l16 + 1024, lqo);
    u32x4 lk0 = bload(rs, l16, lko), lk1 = bload(rs, l16 + 1024, lko);
    u32x4 pk0 = bload(rs, l16, lko - pd * 28672), pk1 = bload(rs, l16 + 1024, lko - pd * 28672);
    u32x4 lv0 = bload(rs, l16, lvo), lv1 = bload(rs, l16 + 1024, lvo);
    u32x4 pv1 = bload(rs, l16 + 1024, lvo - pd * 2048);
    set_foreign<ODD>(lq0, lq1, h, 0u, 0u);
    f32x16 sp = MFMA(as_frag(pk0), as_frag(lq0), f32x16{});
    sp = MFMA(as_frag(pk1), as_frag(lq1), sp);
    f32x16 sc = MFMA(as_frag(lk0), as_frag(lq0), f32x16{});
    sc = MFMA(as_frag(lk1), as_frag(lq1), sc);
    const bool first = (ltile == 0);
    const float NEG = -3.0e38f;
    float m = NEG;
#define LM_C(i) { sc[i] = sel_mask(sc[i], NEG, lmask(i, false)); m = fmaxf(m, sc[i]); }
#define LM_P(i) { sp[i] = sel_mask(sp[i], NEG, first ? 0ull : lmask(i, true)); m = fmaxf(m, sp[i]); }
    LM_C(0) LM_C(1) LM_C(2) LM_C(3) LM_C(4) LM_C(5) LM_C(6) LM_C(7)
    LM_C(8) LM_C(9) LM_C(10) LM_C(11) LM_C(12) LM_C(13) LM_C(14) LM_C(15)
    LM_P(8) LM_P(9) LM_P(10) LM_P(11) LM_P(12) LM_P(13) LM_P(14) LM_P(15)
#undef LM_C
#undef LM_P
    m = swap_max(m);
#pragma unroll
    for (int i = 0; i < 16; ++i) sc[i] = __builtin_amdgcn_exp2f(sc[i] - m);
#pragma unroll
    for (int i = 8; i < 16; ++i) sp[i] = __builtin_amdgcn_exp2f(sp[i] - m);
    f32x16 o = MFMA(as_frag(pv1), as_frag(pack_step1<1>(sp)), f32x16{});
    o = MFMA(as_frag(lv0), as_frag(pack_step1<0>(sc)), o);
    o = MFMA(as_frag(lv1), as_frag(pack_step1<1>(sc)), o);
    float ll = __shfl(o[12], 32 + r);
    float inv = 1.f / ll;
    u32x4 o0 = pack_step<0>(o, inv), o1 = pack_step<1>(o, inv);
    if (h) { o1[2] = 0; o1[3] = 0; }
    const unsigned oo = WS_O + ((ltile * 32 + head) * 2) * 1024;
    bstore(o0, rs, l16, oo);
    bstore(o1, rs, l16 + 1024, oo);
    asm volatile("s_waitcnt vmcnt(0)" ::: "memory");
    lds_arrive(smem, 1, 3, ws, F2A + (qb * 16 + head) * 8, tag, lane);
    }
    ST(2, 5);
    if (w == 0) {
      gu64* f = (gu64*)(ws + F1A) + ((lane & 31) >> 1) * 16 + 14 + (lane & 1);
      for (unsigned spins = 0; spins < (1u << 20); ++spins) {
        if (__all(__hip_atomic_load(f, __ATOMIC_RELAXED, __HIP_MEMORY_SCOPE_AGENT) == tag)) break;
        __builtin_amdgcn_s_sleep(2);
      }
      asm volatile("" ::: "memory");
    }
  }
  __syncthreads();
  loadW();
  reduce();
  ST(1, 3);
  {
    float am = fmaxf(fmaxf(fmaxf(fabsf(a0[0]), fabsf(a0[1])), fmaxf(fabsf(a0[2]), fabsf(a0[3]))),
                     fmaxf(fmaxf(fabsf(a1[0]), fabsf(a1[1])), fmaxf(fabsf(a1[2]), fabsf(a1[3]))));
    float chk = (a0[0] + a0[1] + a0[2] + a0[3]) * 0.f + (a1[0] + a1[1] + a1[2] + a1[3]) * 0.f;
    bool bad = !(l < 1.0e30f) || !(am < 3.0e38f) || !(chk == 0.f);
    if (bad) ctl[0] = 1;
    __syncthreads();
  }
  if (__builtin_amdgcn_readfirstlane(ctl[0])) {
#pragma unroll
    for (int i = 0; i < 4; ++i) {
      q[i][0] = bload(rs, l16, qoff + i * 28672); q[i][1] = bload(rs, l16 + 1024, qoff + i * 28672);
      set_foreign<ODD>(q[i][0], q[i][1], h, 0u, 0u);
    }
    float m[4] = {-3.0e38f, -3.0e38f, -3.0e38f, -3.0e38f};
#pragma unroll 1
    for (int t = 0; t < 8; ++t) {
      u32x4 k0 = bload(rs, l16, koff + t * 28672), k1 = bload(rs, l16 + 1024, koff + t * 28672);
#pragma unroll
      for (int i = 0; i < 4; ++i) {
        f32x16 s = MFMA(as_frag(k0), as_frag(q[i][0]), f32x16{});
        s = MFMA(as_frag(k1), as_frag(q[i][1]), s);
#pragma unroll
        for (int j = 0; j < 16; ++j) m[i] = fmaxf(m[i], s[j]);
      }
    }
#pragma unroll
    for (int i = 0; i < 4; ++i) fold_shift(i, m[i]);
    f32x16 o[4] = {};
#pragma unroll 1
    for (int t = 0; t < 8; ++t) {
      u32x4 k0 = bload(rs, l16, koff + t * 28672), k1 = bload(rs, l16 + 1024, koff + t * 28672);
      u32x4 v0 = bload(rs, l16, voff + t * 2048), v1 = bload(rs, l16 + 1024, voff + t * 2048);
      set_foreign<ODD>(k0, k1, h, 0x3F803F80u, 0x00003F80u);
#pragma unroll
      for (int i = 0; i < 4; ++i) {
        f32x16 s = MFMA(as_frag(k0), as_frag(q[i][0]), f32x16{});
        s = MFMA(as_frag(k1), as_frag(q[i][1]), s);
#pragma unroll
        for (int j = 0; j < 16; ++j) s[j] = __builtin_amdgcn_exp2f(s[j]);
        o[i] = MFMA(as_frag(v0), as_frag(pack_step1<0>(s)), o[i]);
        o[i] = MFMA(as_frag(v1), as_frag(pack_step1<1>(s)), o[i]);
      }
    }
    write_part(o);
    __syncthreads();
    reduce();
    loadW();
  }
  {
    const float inv = 1.f / l;
    u32x4 ov;
    ov[0] = pack2(a0[0] * inv, a0[1] * inv);
    ov[1] = pack2(a0[2] * inv, a0[3] * inv);
    ov[2] = pack2(a1[0] * inv, a1[1] * inv);
    ov[3] = pack2(a1[2] * inv, a1[3] * inv);
    if (h && rs1) { ov[2] = 0; ov[3] = 0; }
    bstore(ov, rs, l16, WS_O + (((qb * 4 + ri) * 32 + 16 + head) * 2 + rs1) * 1024);
  }
  const int oqt = oqb * 4 + oqh * 2 + qt;
  const unsigned ofo = WS_O + ((oqt * 32 + 4 * kq) * 2) * 1024;
  const float gt = gload(ws, oqt * 32 + r);
  if (w == 1) wait_flags16(ws, F2A + oqb * 128, tag, lane);
  asm volatile("s_waitcnt vmcnt(0)" ::: "memory");
  __syncthreads();
  ST(1, 4);
  if (tid == 0) set_flag(ws, F2B + (qb * 16 + head) * 8, tag);
  {
    u32x4 ofr[8];
    f32x16 acc[2][2] = {};
#pragma unroll
    for (int e = 0; e < 8; ++e) ofr[e] = bload(rs, l16 + (e & 3) * 1024, ofo + (e >> 2) * 4096);
#pragma unroll
    for (int e = 0; e < 8; ++e)
#pragma unroll
      for (int nt = 0; nt < 2; ++nt) acc[0][nt] = MFMA(as_frag(wfr[0][nt][e]), as_frag(ofr[e]), acc[0][nt]);
    if (w == 0) wait_flags16(ws, F2B + oqb * 128, tag, lane);
    __syncthreads();
    ST(1, 5);
#pragma unroll
    for (int e = 0; e < 8; ++e) ofr[e] = bload(rs, l16 + (e & 3) * 1024, ofo + 32768 + (e >> 2) * 4096);
#pragma unroll
    for (int e = 0; e < 8; ++e)
#pragma unroll
      for (int nt = 0; nt < 2; ++nt) acc[1][nt] = MFMA(as_frag(wfr[1][nt][e]), as_frag(ofr[e]), acc[1][nt]);
    ST(1, 6);
#pragma unroll
    for (int nt = 0; nt < 2; ++nt)
#pragma unroll
      for (int g = 0; g < 4; ++g) {
        f32x4 v;
#pragma unroll
        for (int e = 0; e < 4; ++e) v[e] = acc[0][nt][4 * g + e] * gt + acc[1][nt][4 * g + e] * (1.f - gt);
        part[(((nt * 2 + qt) * 4 + kq) * 4 + g) * 64 + lane] = v;
      }
  }
  __syncthreads();
  {
    const int ft = w & 3, fg = w >> 2;
    f32x4 s0 = {}, s1 = {};
#pragma unroll
    for (int k = 0; k < 4; ++k) {
      s0 += part[((ft * 4 + k) * 4 + 2 * fg) * 64 + lane];
      s1 += part[((ft * 4 + k) * 4 + 2 * fg + 1) * 64 + lane];
    }
    const u32x4 ov = widen(pack2(s0[0], s0[1]), pack2(s0[2], s0[3]), pack2(s1[0], s1[1]), pack2(s1[2], s1[3]));
    const int col = 32 * (ft >> 1) + 16 * fg + 8 * h;
    if (col < 56) *(u32x4*)(out + (oqb * 128 + oqh * 64 + (ft & 1) * 32 + r) * DM + nb * 56 + col) = ov;
  }
}

__global__ __launch_bounds__(512, 2) void fused_block(const u16* __restrict__ x, const u16* __restrict__ WA, const u16* __restrict__ WB,
                                                      const u16* __restrict__ gate_w, const u16* __restrict__ WoA,
                                                      const u16* __restrict__ WoB, char* __restrict__ ws, u16* __restrict__ out) {
  extern __shared__ __attribute__((aligned(16))) char smem_[];
  char LDSP* smem = (char LDSP*)smem_;
  const rsrc_t rs = __builtin_amdgcn_make_buffer_rsrc(ws, 0, 32u << 20, 0x00020000);
  u64 tag = __builtin_amdgcn_s_getpc();
  ST(0, 0);
  u32x4 sv[8];
  unsigned so[8];
  const int b = blockIdx.x, xcd = b & 7, j = b >> 3;
  if (j < 28) {
    const int mt = 4 * (xcd & 3) + (j & 3);
    const int nt = 7 * (xcd >> 2) + (j >> 2);
    if (__builtin_amdgcn_readfirstlane(threadIdx.x >> 8) == 0) {
      if (nt < 9) p1_body<2, 0>(x, WA, WB, ws, rs, mt, nt, smem, tag, sv, so);
      else if (nt == 9) p1_body<2, 1>(x, WA, WB, ws, rs, mt, nt, smem, tag, sv, so);
      else p1_body<2, 2>(x, WA, WB, ws, rs, mt, nt, smem, tag, sv, so);
    } else {
      if (nt < 9) p1_body<1, 0>(x, WA, WB, ws, rs, mt, nt, smem, tag, sv, so);
      else p1_body<1, 1>(x, WA, WB, ws, rs, mt, nt, smem, tag, sv, so);
    }
  } else {
    warm_l2(x, WA, WB, smem, tag);
    p1_spare(x, gate_w, WoA, WoB, ws, rs, xcd * 4 + j - 28, smem, tag);
#pragma unroll
    for (int e = 0; e < 8; ++e) { sv[e] = u32x4{}; so[e] = DROP; }
  }
  ST(0, 1);
  const int sp = xcd * 4 + j - 28;
  const int fidx = j < 28 ? (4 * (xcd & 3) + (j & 3)) * 16 + 7 * (xcd >> 2) + (j >> 2) : (sp >> 1) * 16 + 14 + (sp & 1);
  if ((b >> 3) & 1) p2_body<1>(ws, out, rs, smem, tag, fidx, sv, so);
  else p2_body<0>(ws, out, rs, smem, tag, fidx, sv, so);
}

extern "C" void kernel_launch(void* const* d_in, const int* in_sizes, int n_in, void* d_out, int out_size, void* d_ws,
                              size_t ws_size, hipStream_t stream) {
  const u16* x = (const u16*)d_in[0];
  const u16* WA = (const u16*)d_in[1];
  const u16* WoA = (const u16*)d_in[2];
  const u16* WB = (const u16*)d_in[4];
  const u16* WoB = (const u16*)d_in[5];
  const u16* gw = (const u16*)d_in[6];
  (void)hipFuncSetAttribute((const void*)fused_block, hipFuncAttributeMaxDynamicSharedMemorySize, LDS_BYTES);
  fused_block<<<256, 512, LDS_BYTES, stream>>>(x, WA, WB, gw, WoA, WoB, (char*)d_ws, (u16*)d_out);
}
```

```cpp
#include <hip/hip_runtime.h>
#include <hip/hip_bf16.h>
#include <stdint.h>
#include <type_traits>

typedef short bf16x8 __attribute__((ext_vector_type(8)));
typedef short s16x4 __attribute__((ext_vector_type(4)));
typedef float f32x16 __attribute__((ext_vector_type(16)));
typedef float f32x4 __attribute__((ext_vector_type(4)));
typedef float f32x2 __attribute__((ext_vector_type(2)));
typedef unsigned u32x4 __attribute__((ext_vector_type(4)));
typedef unsigned u32x2 __attribute__((ext_vector_type(2)));
typedef __bf16 bf2 __attribute__((ext_vector_type(2)));
typedef unsigned short u16;

#define DI __device__ __forceinline__
#define MFMA(a, b, c) __builtin_amdgcn_mfma_f32_32x32x16_bf16((a), (b), (c), 0, 0, 0)
#define LDSP __attribute__((address_space(3)))
#define GLBP __attribute__((address_space(1)))

constexpr int SEQ = 2048, DM = 448, NH = 16, DH = 28, N3 = 1344;
constexpr float QSCALE = 0.18898223650461362f * 1.4426950408889634f;

#define ST(k, i)
constexpr unsigned WS_Q = 0;
constexpr unsigned WS_K = 4u << 20;
constexpr unsigned WS_V = 8u << 20;
constexpr unsigned WS_O = 12u << 20;
constexpr unsigned WS_G = 16u << 20;
constexpr unsigned WS_W = 17u << 20;
constexpr unsigned WS_C = 19u << 20;
constexpr unsigned F1B = WS_C + 4096, F1A = WS_C + 8192, F2A = WS_C + 12288, F2B = WS_C + 16384;
constexpr unsigned QK_BR = 64 * 56 * 512;

DI unsigned pack2(float lo, float hi) {
  f32x2 v = {lo, hi};
  return __builtin_bit_cast(unsigned, __builtin_convertvector(v, bf2));
}
DI bf16x8 as_frag(u32x4 v) { return __builtin_bit_cast(bf16x8, v); }
DI float bf_lo(unsigned v) { return __uint_as_float(v << 16); }
DI float bf_hi(unsigned v) { return __uint_as_float(v & 0xffff0000u); }
DI float swap_max(float v) {
  auto rr = __builtin_amdgcn_permlane32_swap(__float_as_uint(v), __float_as_uint(v), false, false);
  return fmaxf(__uint_as_float(rr[0]), __uint_as_float(rr[1]));
}
DI s16x4 tr_read(const char LDSP* p) {
  return __builtin_amdgcn_ds_read_tr16_b64_v4i16((s16x4 LDSP*)p);
}
DI bf16x8 cat(s16x4 a, s16x4 b) { return __builtin_shufflevector(a, b, 0, 1, 2, 3, 4, 5, 6, 7); }
template <int S>
DI u32x4 pack_step(const f32x16& x, float sc) {
  u32x4 p;
  p[0] = pack2(x[8 * S + 0] * sc, x[8 * S + 1] * sc);
  p[1] = pack2(x[8 * S + 2] * sc, x[8 * S + 3] * sc);
  p[2] = pack2(x[8 * S + 4] * sc, x[8 * S + 5] * sc);
  p[3] = pack2(x[8 * S + 6] * sc, x[8 * S + 7] * sc);
  return p;
}
template <int S>
DI u32x4 pack_step1(const f32x16& x) {
  u32x4 p;
  p[0] = pack2(x[8 * S + 0], x[8 * S + 1]);
  p[1] = pack2(x[8 * S + 2], x[8 * S + 3]);
  p[2] = pack2(x[8 * S + 4], x[8 * S + 5]);
  p[3] = pack2(x[8 * S + 6], x[8 * S + 7]);
  return p;
}

#define RAWBAR() do { asm volatile("" ::: "memory"); __builtin_amdgcn_s_barrier(); asm volatile("" ::: "memory"); } while (0)
#define WAITV(n) ((((n) & 15) | (((n) >> 4) << 14)) | 0x0F70)
#define WAITVL(n) ((((n) & 15) | (((n) >> 4) << 14)) | 0x0070)
DI u32x4 widen(unsigned a0, unsigned a1, unsigned b0, unsigned b1) {
  auto r0 = __builtin_amdgcn_permlane32_swap(a0, b0, false, false);
  auto r1 = __builtin_amdgcn_permlane32_swap(a1, b1, false, false);
  return u32x4{r0[0], r1[0], r0[1], r1[1]};
}

typedef __amdgpu_buffer_rsrc_t rsrc_t;
typedef unsigned v4u_t __attribute__((ext_vector_type(4)));
typedef unsigned long long u64;
typedef GLBP u64 gu64;
DI u32x4 bload(rsrc_t rs, unsigned voff, unsigned soff) {
  return __builtin_bit_cast(u32x4, __builtin_amdgcn_raw_buffer_load_b128(rs, voff, soff, 16));
}
DI void bstore(u32x4 v, rsrc_t rs, unsigned voff, unsigned soff) {
  __builtin_amdgcn_raw_buffer_store_b128(__builtin_bit_cast(v4u_t, v), rs, voff + soff, 0, 16);
}
DI void set_flag(char* ws, unsigned off, u64 tag) {
  __hip_atomic_store((gu64*)(ws + off), tag, __ATOMIC_RELAXED, __HIP_MEMORY_SCOPE_AGENT);
}
DI u64 read_tag(const char* ws) {
  u64 e;
  asm volatile("s_load_dwordx2 %0, %1, 0x0\n\ts_waitcnt lgkmcnt(0)" : "=s"(e) : "s"(ws + WS_C) : "memory");
  return e + 1;
}
DI void wait_flags256(char* ws, unsigned off, u64 tag, int lane) {
  gu64* f = (gu64*)(ws + off) + lane;
  for (unsigned spins = 0; spins < (1u << 20); ++spins) {
    bool ok = true;
#pragma unroll
    for (int k = 0; k < 4; ++k) ok &= __hip_atomic_load(f + 64 * k, __ATOMIC_RELAXED, __HIP_MEMORY_SCOPE_AGENT) == tag;
    if (__all(ok)) break;
    __builtin_amdgcn_s_sleep(2);
  }
  asm volatile("" ::: "memory");
}
DI void wait_flags16(char* ws, unsigned off, u64 tag, int lane) {
  gu64* f = (gu64*)(ws + off) + (lane & 15);
  for (unsigned spins = 0; spins < (1u << 20); ++spins) {
    if (__all(__hip_atomic_load(f, __ATOMIC_RELAXED, __HIP_MEMORY_SCOPE_AGENT) == tag)) break;
    __builtin_amdgcn_s_sleep(1);
  }
  asm volatile("" ::: "memory");
}

constexpr int P1_WSLOT = 64 * 192;
constexpr int P1_SLOT = 16384 + P1_WSLOT;
constexpr int P1_CTL = 5 * P1_SLOT + P1_WSLOT;
constexpr int P1_SINK = P1_CTL + 64;
constexpr int LDS_BYTES = P1_SINK + 256;

constexpr unsigned DROP = 0x80000000u;
template <bool V>
DI void p1_pack(const f32x16& a, int br, int ct, int tile, int lane, u32x4* sv, unsigned* so) {
  const int type = ct / 14, cbase = (ct % 14) * 32;
  if (!V) {
    const float sc = type == 0 ? QSCALE : 1.f;
    const unsigned o = (type == 0 ? WS_Q : WS_K) + br * QK_BR + (tile * 56 + (cbase >> 3)) * 512 + lane * 16;
#pragma unroll
    for (int g = 0; g < 2; ++g) {
      sv[g] = widen(pack2(a[8 * g] * sc, a[8 * g + 1] * sc), pack2(a[8 * g + 2] * sc, a[8 * g + 3] * sc),
                    pack2(a[8 * g + 4] * sc, a[8 * g + 5] * sc), pack2(a[8 * g + 6] * sc, a[8 * g + 7] * sc));
      so[g] = o + g * 1024;
    }
  } else {
    const unsigned f = cbase + (lane & 31), head = f / 28, d = f % 28;
    const unsigned o = WS_V + ((br * 16 * 64 + tile) * 2) * 1024 + (head * 64 * 2 * 64 + d + (lane & 32)) * 16;
    sv[0] = pack_step1<0>(a); so[0] = o;
    sv[1] = pack_step1<1>(a); so[1] = o + 1024;
    const unsigned c = d == 0 ? 0x3F803F80u : 0u;
    sv[2] = u32x4{c, c, c, c}; so[2] = d < 4 ? o + 28 * 16 : DROP;
    sv[3] = sv[2]; so[3] = d < 4 ? o + 28 * 16 + 1024 : DROP;
  }
}

DI void warm_l2(const u16* x, const u16* WA, const u16* WB, char LDSP* smem, const u64 pc) {
  const int b = blockIdx.x, xcd = b & 7;
  const unsigned g = ((b >> 3) - 28) * 512 + threadIdx.x;
  constexpr unsigned len = 60 * 1024;
  __builtin_amdgcn_global_load_lds((const GLBP void*)((const char*)pc + (g * 128 + 4 <= len ? g * 128 : 0u)), (LDSP void*)(smem + P1_SINK), 4, 0, 0);
#pragma unroll
  for (int e = 0; e < 4; ++e) {
    const unsigned i = e * 2048 + g;
    const char* p;
    if (i < 1024) {
      p = (const char*)x + ((xcd & 3) * 512 + (i >> 1)) * 896 + 640 + (i & 1) * 128;
    } else {
      const unsigned idx = i - 1024, row = idx / 12, c = idx % 12;
      unsigned off = (row < 128 ? row + 320 : row - 128) * 2688 + (xcd >> 2) * 1280 + c * 128;
      off = off < (unsigned)(DM * N3 * 2 - 4) ? off : 0u;
      p = (const char*)(row < 128 ? WB : WA) + off;
    }
    __builtin_amdgcn_global_load_lds((const GLBP void*)p, (LDSP void*)(smem + P1_SINK), 4, 0, 0);
  }
}

DI void lds_arrive(char LDSP* smem, int idx, int last, char* ws, unsigned flag_off, u64 tag, int lane) {
  if (lane == 0) {
    if (__hip_atomic_fetch_add((int LDSP*)(smem + P1_CTL) + idx, 1, __ATOMIC_RELAXED, __HIP_MEMORY_SCOPE_WORKGROUP) == last) set_flag(ws, flag_off, tag);
  }
}

DI void p1_first(const u16* __restrict__ x, const u16* __restrict__ WA, const u16* __restrict__ WB, int mt, int nt, char LDSP* smem) {
  const int lane = threadIdx.x & 63;
  const int w = __builtin_amdgcn_readfirstlane(threadIdx.x >> 6);
  const int wi0 = w < 4 ? w : 2 * w - 4;
  const u16* xs[2];
  int wso[2];
#pragma unroll
  for (int i = 0; i < 2; ++i) {
    int lr = (w * 2 + i) * 8 + (lane >> 3);
    int logical = (lane & 7) ^ ((lr >> 1) & 7);
    xs[i] = x + (mt * 128 + lr) * DM + logical * 8;
    const int id = (wi0 + i) * 64 + lane;
    wso[i] = (id / 12) * N3 + nt * 96 + (id % 12) * 8;
  }
#pragma unroll
  for (int c = 0; c < 6; ++c) {
    if (c < 5) {
#pragma unroll
      for (int i = 0; i < 2; ++i)
        __builtin_amdgcn_global_load_lds((const GLBP void*)(xs[i] + c * 64), (LDSP void*)(smem + c * P1_SLOT + (w * 2 + i) * 1024), 16, 0, 0);
    }
    const u16* Wc = c < 5 ? WB + c * 64 * N3 : WA + 6 * 64 * N3;
    char LDSP* dst = smem + (c < 5 ? c * P1_SLOT + 16384 : 5 * P1_SLOT) + wi0 * 1024;
    __builtin_amdgcn_global_load_lds((const GLBP void*)(Wc + wso[0]), (LDSP void*)dst, 16, 0, 0);
    if (w >= 4) __builtin_amdgcn_global_load_lds((const GLBP void*)(Wc + wso[1]), (LDSP void*)(dst + 1024), 16, 0, 0);
  }
}

template <int NT, int NV>
DI void p1_body(const u16* __restrict__ x, const u16* __restrict__ WA, const u16* __restrict__ WB, char* __restrict__ ws, rsrc_t rs,
                int mt, int nt, char LDSP* smem, u64& tag, u32x4 (&sv)[8], unsigned (&so)[8]) {
  const int tid = threadIdx.x, lane = tid & 63;
  const int w = __builtin_amdgcn_readfirstlane(tid >> 6);
  const int msub = w & 3;
  const int j0 = NT == 2 ? 0 : 2;
  const int r = lane & 31, h = lane >> 5;
  constexpr int NS = 2 * (NT - NV) + 4 * NV;
  constexpr int NW = NT == 2 ? 1 : 2;
  const int wi0 = NT == 2 ? w : 2 * w - 4;
  const u16* xs[2];
  int wso[2];
#pragma unroll
  for (int i = 0; i < 2; ++i) {
    int lr = (w * 2 + i) * 8 + (lane >> 3);
    int logical = (lane & 7) ^ ((lr >> 1) & 7);
    xs[i] = x + (mt * 128 + lr) * DM + logical * 8;
    const int id = (wi0 + i) * 64 + lane;
    wso[i] = (id / 12) * N3 + nt * 96 + (id % 12) * 8;
  }
  auto wbase = [&](int s) {
    return s < 7 ? (s % 5) * P1_SLOT + 16384 : (s < 13 ? (2 + (s - 7) / 2) * P1_SLOT + ((s - 7) & 1) * P1_WSLOT : 5 * P1_SLOT);
  };
  auto issue_x = [&](int c, int i) {
    __builtin_amdgcn_global_load_lds((const GLBP void*)(xs[i] + c * 64), (LDSP void*)(smem + (c % 5) * P1_SLOT + (w * 2 + i) * 1024), 16, 0, 0);
  };
  auto issue_w = [&](int s) {
#pragma unroll
    for (int i = 0; i < NW; ++i)
      __builtin_amdgcn_global_load_lds((const GLBP void*)((s < 7 ? WB : WA) + wso[i] + (s % 7) * 64 * N3),
                                       (LDSP void*)(smem + wbase(s) + (wi0 + i) * 1024), 16, 0, 0);
  };
  int xoff[4];
  {
    int lr = 32 * msub + r, sw = (lr >> 1) & 7;
#pragma unroll
    for (int t = 0; t < 4; ++t) xoff[t] = lr * 128 + (((2 * t + h) ^ sw) << 4);
  }
  const int q4 = (lane & 15) >> 2, p4 = lane & 3, g16 = (lane >> 4) & 1;
  const int woff = (8 * h + q4) * 192 + 64 * j0 + 32 * g16 + 8 * p4;

  f32x16 acc[NT] = {};
  bf16x8 xf[7][4];
  s16x4 wl[2][4][NT], wh[2][4][NT];
  auto readf = [&](int s, int m) {
    const int t = m / NT, j = m % NT;
    if (j == 0 && s < 7) xf[s][t] = *(const bf16x8 LDSP*)(smem + (s % 5) * P1_SLOT + xoff[t]);
    const char LDSP* wp = smem + woff + wbase(s) + t * 16 * 192 + j * 64;
    wl[s & 1][t][j] = tr_read(wp);
    wh[s & 1][t][j] = tr_read(wp + 4 * 192);
  };
  tag = read_tag(ws);
  if (tid < 8) ((int LDSP*)(smem + P1_CTL))[tid] = 0;
  __builtin_amdgcn_s_waitcnt(WAITVL(4 * (NW + 2) + NW));
  RAWBAR();
#pragma unroll
  for (int m = 0; m < 4 * NT; ++m) readf(0, m);
#pragma unroll
  for (int s = 0; s < 14; ++s) {
    if (s < 7 || s == 8) {
      if (s < 3) __builtin_amdgcn_s_waitcnt(WAITVL(3 * (NW + 2) + NW));
      else if (s == 3) __builtin_amdgcn_s_waitcnt(WAITVL(2 * (NW + 2) + 3 * NW));
      else if (s == 4) __builtin_amdgcn_s_waitcnt(WAITVL(NW + 2 + 4 * NW));
      else if (s == 5) __builtin_amdgcn_s_waitcnt(WAITVL(6 * NW));
      else if (s == 6) __builtin_amdgcn_s_waitcnt(WAITVL(4 * NW));
      else __builtin_amdgcn_s_waitcnt(WAITVL(NS));
      RAWBAR();
    }
    if (s == 12) {
      __builtin_amdgcn_s_waitcnt(WAITV(0));
      asm volatile("" ::: "memory");
      lds_arrive(smem, 4, 7, ws, F1B + (nt * 16 + mt) * 8, tag, lane);
    }
    __builtin_amdgcn_sched_barrier(0);
#pragma unroll
    for (int m = 0; m < 4 * NT; ++m) {
      const int t = m / NT, j = m % NT;
      const bf16x8 wfr = cat(wl[s & 1][t][j], wh[s & 1][t][j]);
      if (j < NT - NV) acc[j] = MFMA(wfr, xf[s % 7][t], acc[j]);
      else acc[j] = MFMA(xf[s % 7][t], wfr, acc[j]);
      __builtin_amdgcn_sched_barrier(0);
      if (s < 13) readf(s + 1, m);
      if (j == 0) {
        if (s < 2) { if (t < 2) issue_x(s + 5, t); else if (t == 2) issue_w(s + 5); }
        else if (s < 5 && t < 2) issue_w(7 + 2 * (s - 2) + t);
      }
      __builtin_amdgcn_sched_barrier(0);
    }
    if (s == 6 || s == 13) {
#pragma unroll
      for (int e = 0; e < 8; ++e) so[e] = DROP;
#pragma unroll
      for (int j = 0; j < NT; ++j) {
        if (j < NT - NV) p1_pack<false>(acc[j], s == 6, nt * 3 + j0 + j, mt * 4 + msub, lane, sv + 4 * j, so + 4 * j);
        else p1_pack<true>(acc[j], s == 6, nt * 3 + j0 + j, mt * 4 + msub, lane, sv + 4 * j, so + 4 * j);
        if (s == 6) {
#pragma unroll
          for (int e = 0; e < (j < NT - NV ? 2 : 4); ++e) bstore(sv[4 * j + e], rs, so[4 * j + e], 0);
        }
        acc[j] = f32x16{};
      }
      __builtin_amdgcn_sched_barrier(0);
    }
  }
}

DI void p1_spare(const u16* __restrict__ x, const u16* __restrict__ gate_w, const u16* __restrict__ WoA, const u16* __restrict__ WoB,
                 char* __restrict__ ws, rsrc_t rs, int sp, char LDSP* smem, u64& tag) {
  const int tid = threadIdx.x, lane = tid & 63, r = lane & 31, h = lane >> 5;
  tag = read_tag(ws);
  if (tid < 8) ((int LDSP*)(smem + P1_CTL))[tid] = 0;
  __builtin_amdgcn_s_waitcnt(0xC07F);
  RAWBAR();
  if (tid == 0) set_flag(ws, F1B + ((14 + (sp & 1)) * 16 + (sp >> 1)) * 8, tag);
#pragma unroll
  for (int e = 0; e < 4; ++e) {
    const int fid = sp * 32 + (tid >> 6) * 4 + e;
    const int sfr = fid & 1, vh = (fid >> 1) & 31, nt = (fid >> 6) & 1, nb = fid >> 7;
    const int n = 56 * nb + 32 * nt + r;
    const u16* Wo = (vh >> 4) ? WoB : WoA;
    unsigned v[8];
#pragma unroll
    for (int jj = 0; jj < 8; ++jj) {
      const int d = 16 * sfr + 8 * (jj >> 2) + 4 * h + (jj & 3);
      const bool ok = d < DH && n < DM;
      const unsigned tv = Wo[ok ? ((vh & 15) * DH + d) * DM + n : 0];
      v[jj] = ok ? tv : 0u;
    }
    bstore(u32x4{v[0] | (v[1] << 16), v[2] | (v[3] << 16), v[4] | (v[5] << 16), v[6] | (v[7] << 16)}, rs, WS_W + fid * 1024 + lane * 16, 0);
  }
  const int row = sp * 64 + (tid >> 3), part = tid & 7;
  const u32x4* xp = (const u32x4*)(x + row * DM) + part;
  const u32x4* gp = (const u32x4*)(gate_w) + part;
  float acc = 0.f;
#pragma unroll
  for (int i = 0; i < 7; ++i) {
    u32x4 a = xp[i * 8], bb = gp[i * 8];
#pragma unroll
    for (int e = 0; e < 4; ++e) {
      acc = fmaf(bf_lo(a[e]), bf_lo(bb[e]), acc);
      acc = fmaf(bf_hi(a[e]), bf_hi(bb[e]), acc);
    }
  }
  acc += __shfl_xor(acc, 1);
  acc += __shfl_xor(acc, 2);
  acc += __shfl_xor(acc, 4);
  if (part == 0)
    __hip_atomic_store((GLBP float*)(ws + WS_G) + row, 1.f / (1.f + __expf(-acc)), __ATOMIC_RELAXED, __HIP_MEMORY_SCOPE_AGENT);
}

constexpr unsigned long long lmask(int i, bool prev) {
  unsigned long long m = 0;
  for (int l = 0; l < 64; ++l) {
    int r = l & 31, h = l >> 5;
    int kr = (i & 3) + 8 * (i >> 2) + 4 * h;
    int diff = r - kr + (prev ? 32 : 0);
    if (diff >= 0 && diff <= 14 && (diff & 1) == 0) m |= 1ull << l;
  }
  return m;
}
DI float sel_mask(float val, float other, unsigned long long mask) {
  return __builtin_amdgcn_inverse_ballot_w64(mask) ? val : other;
}
template <int ODD>
DI void set_foreign(u32x4& f0, u32x4& f1, int h, unsigned a, unsigned b) {
  if (ODD) { if (!h) { f0[0] = a; f0[1] = b; } }
  else { if (h) { f1[2] = a; f1[3] = b; } }
}
DI float gload(char* ws, int idx) { return __hip_atomic_load((GLBP float*)(ws + WS_G) + idx, __ATOMIC_RELAXED, __HIP_MEMORY_SCOPE_AGENT); }

template <int ODD>
DI void p2_body(char* __restrict__ ws, u16* __restrict__ out, rsrc_t rs, char LDSP* smem, const u64 tag, const int fidx,
                const u32x4 (&sv)[8], const unsigned (&so)[8]) {
  f32x4 LDSP* part = (f32x4 LDSP*)smem;
  float LDSP* cw = (float LDSP*)(smem + 131072);
  volatile int LDSP* ctl = (volatile int LDSP*)(smem + P1_CTL);
  const int tid = threadIdx.x;
  int lane = tid & 63;
  const int w = __builtin_amdgcn_readfirstlane(tid >> 6);
  int r = lane & 31, h = lane >> 5;
  const int b = blockIdx.x;
  const int head = (b & 7) * 2 + ODD, qb = b >> 4;
  const int oqb = (b & 7) * 2 + (b >> 7), oqh = (b >> 3) & 1, nb = (b >> 4) & 7;
  const unsigned p0 = (7 * head) >> 1;
  unsigned l16 = lane * 16;
  const unsigned qoff = WS_Q + QK_BR + (qb * 4 * 56 + p0) * 512;
  const unsigned koff = WS_K + QK_BR + (w * 8 * 56 + p0) * 512;
  const unsigned voff = WS_V + ((16 + head) * 64 + w * 8) * 2048;
  __syncthreads();
  if (w < 4) {
#pragma unroll
    for (int e = 0; e < 8; ++e) {
      ((u32x4 LDSP*)(smem + 65536))[(w * 8 + e) * 64 + lane] = sv[e];
      ((unsigned LDSP*)(smem + 65536 + 32768))[(w * 8 + e) * 64 + lane] = so[e];
    }
  }
  if (w == 0) {
    const int sel = lane >> 4;
    const int c0 = sel < 2 ? 448 + 8 * (int)p0 + (sel & 1) * 31 : 896 + 28 * head + (sel & 1) * 27;
    gu64* f = (gu64*)(ws + F1B) + (c0 / 96) * 16 + (lane & 15);
    gu64* fq = (gu64*)(ws + F1B) + ((8 * (int)p0 + (lane & 1) * 31) / 96) * 16 + qb;
    for (unsigned spins = 0; spins < (1u << 20); ++spins) {
      bool ok = __hip_atomic_load(f, __ATOMIC_RELAXED, __HIP_MEMORY_SCOPE_AGENT) == tag;
      ok &= __hip_atomic_load(fq, __ATOMIC_RELAXED, __HIP_MEMORY_SCOPE_AGENT) == tag;
      if (__all(ok)) break;
      __builtin_amdgcn_s_sleep(1);
    }
    asm volatile("" ::: "memory");
  }
  __syncthreads();
  ST(1, 0);
  ST(2, 0);
  u32x4 q[4][2];
  u32x4 kf[4][2], vf[4][2];
  auto loadkv = [&](int t) {
    kf[t & 3][0] = bload(rs, l16, koff + t * 28672);
    kf[t & 3][1] = bload(rs, l16 + 1024, koff + t * 28672);
    vf[t & 3][0] = bload(rs, l16, voff + t * 2048);
    vf[t & 3][1] = bload(rs, l16 + 1024, voff + t * 2048);
  };
  auto store_a = [&]() {
#pragma unroll
    for (int e = 0; e < 4; ++e) bstore(sv[e], rs, so[e], 0);
#pragma unroll
    for (int e = 0; e < 8; ++e)
      bstore(((u32x4 LDSP*)(smem + 65536))[((w - 4) * 8 + e) * 64 + lane], rs, ((unsigned LDSP*)(smem + 65536 + 32768))[((w - 4) * 8 + e) * 64 + lane], 0);
  };
  if (w >= 4) {
    store_a();
    asm volatile("s_waitcnt vmcnt(0)" ::: "memory");
    lds_arrive(smem, 2, 3, ws, F1A + fidx * 8, tag, lane);
    if (b == 0 && w == 5) {
      wait_flags256(ws, F1B, tag, lane);
      if (lane == 0) set_flag(ws, WS_C, tag);
    }
    __builtin_amdgcn_s_setprio(1);
  }
  kf[0][0] = bload(rs, l16, koff);
  kf[0][1] = bload(rs, l16 + 1024, koff);
#pragma unroll
  for (int i = 0; i < 4; ++i) { q[i][0] = bload(rs, l16, qoff + i * 28672); q[i][1] = bload(rs, l16 + 1024, qoff + i * 28672); }
  vf[0][0] = bload(rs, l16, voff);
  vf[0][1] = bload(rs, l16 + 1024, voff);
  loadkv(1);
  loadkv(2);
  __builtin_amdgcn_sched_barrier(0);

  auto fold_shift = [&](int i, float m) {
    m = swap_max(m);
    if (!h) cw[(i * 32 + r) * 8 + w] = m;
    const float c = -m;
    unsigned p01 = pack2(c, 0.f);
    float r1 = c - bf_lo(p01);
    unsigned p1 = pack2(r1, 0.f);
    float r2 = r1 - bf_lo(p1);
    unsigned p2 = pack2(r2, 0.f);
    set_foreign<ODD>(q[i][0], q[i][1], h, (p01 & 0xffffu) | (p1 << 16), p2 & 0xffffu);
  };
  const int ri = w >> 1, rs1 = w & 1;
  f32x4 a0, a1;
  float l;
  auto write_part = [&](f32x16 (&o)[4]) {
#pragma unroll
    for (int i = 0; i < 4; ++i)
#pragma unroll
      for (int g = 0; g < 4; ++g)
        part[((w * 4 + i) * 4 + g) * 64 + lane] = f32x4{o[i][4 * g], o[i][4 * g + 1], o[i][4 * g + 2], o[i][4 * g + 3]};
  };
  auto reduce = [&]() {
    const f32x4 c0 = *(f32x4 LDSP*)(cw + (ri * 32 + r) * 8), c1 = *(f32x4 LDSP*)(cw + (ri * 32 + r) * 8 + 4);
    const float cm = fmaxf(fmaxf(fmaxf(c0[0], c0[1]), fmaxf(c0[2], c0[3])), fmaxf(fmaxf(c1[0], c1[1]), fmaxf(c1[2], c1[3])));
    a0 = f32x4{}; a1 = f32x4{}; l = 0.f;
#pragma unroll
    for (int ww = 0; ww < 8; ++ww) {
      const float wt = __builtin_amdgcn_exp2f((ww < 4 ? c0[ww & 3] : c1[ww & 3]) - cm);
      a0 += part[((ww * 4 + ri) * 4 + 2 * rs1) * 64 + lane] * wt;
      a1 += part[((ww * 4 + ri) * 4 + 2 * rs1 + 1) * 64 + lane] * wt;
      l += ((float LDSP*)(part + ((ww * 4 + ri) * 4 + 3) * 64 + 32 + r))[0] * wt;
    }
  };
  const int qt = w & 1, kq = w >> 1;
  u32x4 wfr[2][2][8];
  auto loadW = [&]() {
#pragma unroll
    for (int br = 0; br < 2; ++br)
#pragma unroll
      for (int nt = 0; nt < 2; ++nt) {
        const unsigned wo = WS_W + (((nb * 2 + nt) * 32 + br * 16 + 4 * kq) * 2) * 1024;
#pragma unroll
        for (int e = 0; e < 8; ++e) wfr[br][nt][e] = bload(rs, l16 + (e & 3) * 1024, wo + (e >> 2) * 4096);
      }
  };

#pragma unroll
  for (int i = 0; i < 4; ++i) {
    set_foreign<ODD>(q[i][0], q[i][1], h, 0u, 0u);
    f32x16 s = MFMA(as_frag(kf[0][0]), as_frag(q[i][0]), f32x16{});
    s = MFMA(as_frag(kf[0][1]), as_frag(q[i][1]), s);
    fold_shift(i, fmaxf(fmaxf(s[0], s[1]), s[2]));
  }
  __builtin_amdgcn_s_setprio(0);
  ST(1, 1);
  {
    f32x16 o[4] = {};
    set_foreign<ODD>(kf[0][0], kf[0][1], h, 0x3F803F80u, 0x00003F80u);
    f32x16 sa = MFMA(as_frag(kf[0][0]), as_frag(q[0][0]), f32x16{});
    sa = MFMA(as_frag(kf[0][1]), as_frag(q[0][1]), sa);
    f32x16 sb;
    u32x4 pa;
    u32x4 pbp;
    float e3[4];
#define SB __builtin_amdgcn_sched_barrier(0)
#define EXPQ(S, g) { S[4 * g] = __builtin_amdgcn_exp2f(S[4 * g]); S[4 * g + 1] = __builtin_amdgcn_exp2f(S[4 * g + 1]); \
                     S[4 * g + 2] = __builtin_amdgcn_exp2f(S[4 * g + 2]); S[4 * g + 3] = __builtin_amdgcn_exp2f(S[4 * g + 3]); }
    auto step = [&](auto nc, f32x16& sc, f32x16& sn) {
      constexpr int n = decltype(nc)::value;
      constexpr int t = n >> 2, i = n & 3;
      constexpr int t1 = ((n + 1) >> 2) & 3, i1 = (n + 1) & 3;
      constexpr int tp = n > 0 ? ((n - 1) >> 2) & 3 : 0, ip = n > 0 ? (n - 1) & 3 : 0;
      if ((n & 7) == 0 && n > 0) ST(2, n >> 3);
      if (i == 1 && t + 3 < 8) loadkv(t + 3);
      if (i == 3 && n + 1 < 32) set_foreign<ODD>(kf[t1][0], kf[t1][1], h, 0x3F803F80u, 0x00003F80u);
      SB;
      if (n + 1 < 32) sn = MFMA(as_frag(kf[t1][0]), as_frag(q[i1][0]), f32x16{});
      SB;
      if (n > 0) { pbp[2] = pack2(e3[0], e3[1]); pbp[3] = pack2(e3[2], e3[3]); }
      SB;
      EXPQ(sc, 0);
      SB;
      if (n + 1 < 32) sn = MFMA(as_frag(kf[t1][1]), as_frag(q[i1][1]), sn);
      SB;
      pa[0] = pack2(sc[0], sc[1]); pa[1] = pack2(sc[2], sc[3]);
      SB;
      EXPQ(sc, 1);
      SB;
      if (n > 0) o[ip] = MFMA(as_frag(vf[tp][1]), as_frag(pbp), o[ip]);
      SB;
      pa[2] = pack2(sc[4], sc[5]); pa[3] = pack2(sc[6], sc[7]);
      SB;
      EXPQ(sc, 2);
      SB;
      o[i] = MFMA(as_frag(vf[t & 3][0]), as_frag(pa), o[i]);
      SB;
      pbp[0] = pack2(sc[8], sc[9]); pbp[1] = pack2(sc[10], sc[11]);
      SB;
      EXPQ(sc, 3);
      e3[0] = sc[12]; e3[1] = sc[13]; e3[2] = sc[14]; e3[3] = sc[15];
      SB;
    };
#define STEP2(n) step(std::integral_constant<int, n>{}, sa, sb); step(std::integral_constant<int, n + 1>{}, sb, sa);
    STEP2(0) STEP2(2) STEP2(4) STEP2(6) STEP2(8) STEP2(10) STEP2(12) STEP2(14)
    STEP2(16) STEP2(18) STEP2(20) STEP2(22) STEP2(24) STEP2(26) STEP2(28) STEP2(30)
#undef STEP2
    pbp[2] = pack2(e3[0], e3[1]); pbp[3] = pack2(e3[2], e3[3]);
    o[3] = MFMA(as_frag(vf[3][1]), as_frag(pbp), o[3]);
    ST(1, 2);
    asm volatile("" : "+v"(lane));
    r = lane & 31; h = lane >> 5; l16 = lane * 16;
    write_part(o);
  }
  SB;
  if (w < 4) {
    ST(2, 4);
    {
      gu64* f = (gu64*)(ws + F1A) + (qb > 0 ? qb - 1 : 0) * 16 + (lane & 31) - ((lane & 15) >= 14 ? 2 : 0);
      for (unsigned spins = 0; spins < (1u << 20); ++spins) {
        if (__all(__hip_atomic_load(f, __ATOMIC_RELAXED, __HIP_MEMORY_SCOPE_AGENT) == tag)) break;
        __builtin_amdgcn_s_sleep(2);
      }
      asm volatile("" ::: "memory");
    const int ltile = qb * 4 + (w & 3);
    const unsigned lqo = WS_Q + (ltile * 56 + p0) * 512, lko = WS_K + (ltile * 56 + p0) * 512, lvo = WS_V + (head * 64 + ltile) * 2048;
    const unsigned pd = ltile > 0 ? 1 : 0;
    u32x4 lq0 = bload(rs, l16, lqo), lq1 = bload(rs, l16 + 1024, lqo);
    u32x4 lk0 = bload(rs, l16, lko), lk1 = bload(rs, l16 + 1024, lko);
    u32x4 pk0 = bload(rs, l16, lko - pd * 28672), pk1 = bload(rs, l16 + 1024, lko - pd * 28672);
    u32x4 lv0 = bload(rs, l16, lvo), lv1 = bload(rs, l16 + 1024, lvo);
    u32x4 pv1 = bload(rs, l16 + 1024, lvo - pd * 2048);
    set_foreign<ODD>(lq0, lq1, h, 0u, 0u);
    f32x16 sp = MFMA(as_frag(pk0), as_frag(lq0), f32x16{});
    sp = MFMA(as_frag(pk1), as_frag(lq1), sp);
    f32x16 sc = MFMA(as_frag(lk0), as_frag(lq0), f32x16{});
    sc = MFMA(as_frag(lk1), as_frag(lq1), sc);
    const bool first = (ltile == 0);
    const float NEG = -3.0e38f;
    float m = NEG;
#define LM_C(i) { sc[i] = sel_mask(sc[i], NEG, lmask(i, false)); m = fmaxf(m, sc[i]); }
#define LM_P(i) { sp[i] = sel_mask(sp[i], NEG, first ? 0ull : lmask(i, true)); m = fmaxf(m, sp[i]); }
    LM_C(0) LM_C(1) LM_C(2) LM_C(3) LM_C(4) LM_C(5) LM_C(6) LM_C(7)
    LM_C(8) LM_C(9) LM_C(10) LM_C(11) LM_C(12) LM_C(13) LM_C(14) LM_C(15)
    LM_P(8) LM_P(9) LM_P(10) LM_P(11) LM_P(12) LM_P(13) LM_P(14) LM_P(15)
#undef LM_C
#undef LM_P
    m = swap_max(m);
#pragma unroll
    for (int i = 0; i < 16; ++i) sc[i] = __builtin_amdgcn_exp2f(sc[i] - m);
#pragma unroll
    for (int i = 8; i < 16; ++i) sp[i] = __builtin_amdgcn_exp2f(sp[i] - m);
    f32x16 o = MFMA(as_frag(pv1), as_frag(pack_step1<1>(sp)), f32x16{});
    o = MFMA(as_frag(lv0), as_frag(pack_step1<0>(sc)), o);
    o = MFMA(as_frag(lv1), as_frag(pack_step1<1>(sc)), o);
    float ll = __shfl(o[12], 32 + r);
    float inv = 1.f / ll;
    u32x4 o0 = pack_step<0>(o, inv), o1 = pack_step<1>(o, inv);
    if (h) { o1[2] = 0; o1[3] = 0; }
    const unsigned oo = WS_O + ((ltile * 32 + head) * 2) * 1024;
    bstore(o0, rs, l16, oo);
    bstore(o1, rs, l16 + 1024, oo);
    asm volatile("s_waitcnt vmcnt(0)" ::: "memory");
    lds_arrive(smem, 1, 3, ws, F2A + (qb * 16 + head) * 8, tag, lane);
    }
    ST(2, 5);
    if (w == 0) {
      gu64* f = (gu64*)(ws + F1A) + ((lane & 31) >> 1) * 16 + 14 + (lane & 1);
      for (unsigned spins = 0; spins < (1u << 20); ++spins) {
        if (__all(__hip_atomic_load(f, __ATOMIC_RELAXED, __HIP_MEMORY_SCOPE_AGENT) == tag)) break;
        __builtin_amdgcn_s_sleep(2);
      }
      asm volatile("" ::: "memory");
    }
  }
  __syncthreads();
  loadW();
  reduce();
  ST(1, 3);
  {
    float am = fmaxf(fmaxf(fmaxf(fabsf(a0[0]), fabsf(a0[1])), fmaxf(fabsf(a0[2]), fabsf(a0[3]))),
                     fmaxf(fmaxf(fabsf(a1[0]), fabsf(a1[1])), fmaxf(fabsf(a1[2]), fabsf(a1[3]))));
    float chk = (a0[0] + a0[1] + a0[2] + a0[3]) * 0.f + (a1[0] + a1[1] + a1[2] + a1[3]) * 0.f;
    bool bad = !(l < 1.0e30f) || !(am < 3.0e38f) || !(chk == 0.f);
    if (bad) ctl[0] = 1;
    __syncthreads();
  }
  if (__builtin_amdgcn_readfirstlane(ctl[0])) {
#pragma unroll
    for (int i = 0; i < 4; ++i) {
      q[i][0] = bload(rs, l16, qoff + i * 28672); q[i][1] = bload(rs, l16 + 1024, qoff + i * 28672);
      set_foreign<ODD>(q[i][0], q[i][1], h, 0u, 0u);
    }
    float m[4] = {-3.0e38f, -3.0e38f, -3.0e38f, -3.0e38f};
#pragma unroll 1
    for (int t = 0; t < 8; ++t) {
      u32x4 k0 = bload(rs, l16, koff + t * 28672), k1 = bload(rs, l16 + 1024, koff + t * 28672);
#pragma unroll
      for (int i = 0; i < 4; ++i) {
        f32x16 s = MFMA(as_frag(k0), as_frag(q[i][0]), f32x16{});
        s = MFMA(as_frag(k1), as_frag(q[i][1]), s);
#pragma unroll
        for (int j = 0; j < 16; ++j) m[i] = fmaxf(m[i], s[j]);
      }
    }
#pragma unroll
    for (int i = 0; i < 4; ++i) fold_shift(i, m[i]);
    f32x16 o[4] = {};
#pragma unroll 1
    for (int t = 0; t < 8; ++t) {
      u32x4 k0 = bload(rs, l16, koff + t * 28672), k1 = bload(rs, l16 + 1024, koff + t * 28672);
      u32x4 v0 = bload(rs, l16, voff + t * 2048), v1 = bload(rs, l16 + 1024, voff + t * 2048);
      set_foreign<ODD>(k0, k1, h, 0x3F803F80u, 0x00003F80u);
#pragma unroll
      for (int i = 0; i < 4; ++i) {
        f32x16 s = MFMA(as_frag(k0), as_frag(q[i][0]), f32x16{});
        s = MFMA(as_frag(k1), as_frag(q[i][1]), s);
#pragma unroll
        for (int j = 0; j < 16; ++j) s[j] = __builtin_amdgcn_exp2f(s[j]);
        o[i] = MFMA(as_frag(v0), as_frag(pack_step1<0>(s)), o[i]);
        o[i] = MFMA(as_frag(v1), as_frag(pack_step1<1>(s)), o[i]);
      }
    }
    write_part(o);
    __syncthreads();
    reduce();
    loadW();
  }
  {
    const float inv = 1.f / l;
    u32x4 ov;
    ov[0] = pack2(a0[0] * inv, a0[1] * inv);
    ov[1] = pack2(a0[2] * inv, a0[3] * inv);
    ov[2] = pack2(a1[0] * inv, a1[1] * inv);
    ov[3] = pack2(a1[2] * inv, a1[3] * inv);
    if (h && rs1) { ov[2] = 0; ov[3] = 0; }
    bstore(ov, rs, l16, WS_O + (((qb * 4 + ri) * 32 + 16 + head) * 2 + rs1) * 1024);
  }
  const int oqt = oqb * 4 + oqh * 2 + qt;
  const unsigned ofo = WS_O + ((oqt * 32 + 4 * kq) * 2) * 1024;
  const float gt = gload(ws, oqt * 32 + r);
  if (w == 1) wait_flags16(ws, F2A + oqb * 128, tag, lane);
  asm volatile("s_waitcnt vmcnt(0)" ::: "memory");
  __syncthreads();
  ST(1, 4);
  if (tid == 0) set_flag(ws, F2B + (qb * 16 + head) * 8, tag);
  {
    u32x4 ofr[8];
    f32x16 acc[2][2] = {};
#pragma unroll
    for (int e = 0; e < 8; ++e) ofr[e] = bload(rs, l16 + (e & 3) * 1024, ofo + (e >> 2) * 4096);
#pragma unroll
    for (int e = 0; e < 8; ++e)
#pragma unroll
      for (int nt = 0; nt < 2; ++nt) acc[0][nt] = MFMA(as_frag(wfr[0][nt][e]), as_frag(ofr[e]), acc[0][nt]);
    if (w == 0) wait_flags16(ws, F2B + oqb * 128, tag, lane);
    __syncthreads();
    ST(1, 5);
#pragma unroll
    for (int e = 0; e < 8; ++e) ofr[e] = bload(rs, l16 + (e & 3) * 1024, ofo + 32768 + (e >> 2) * 4096);
#pragma unroll
    for (int e = 0; e < 8; ++e)
#pragma unroll
      for (int nt = 0; nt < 2; ++nt) acc[1][nt] = MFMA(as_frag(wfr[1][nt][e]), as_frag(ofr[e]), acc[1][nt]);
    ST(1, 6);
#pragma unroll
    for (int nt = 0; nt < 2; ++nt)
#pragma unroll
      for (int g = 0; g < 4; ++g) {
        f32x4 v;
#pragma unroll
        for (int e = 0; e < 4; ++e) v[e] = acc[0][nt][4 * g + e] * gt + acc[1][nt][4 * g + e] * (1.f - gt);
        part[(((nt * 2 + qt) * 4 + kq) * 4 + g) * 64 + lane] = v;
      }
  }
  __syncthreads();
  {
    const int ft = w & 3, fg = w >> 2;
    f32x4 s0 = {}, s1 = {};
#pragma unroll
    for (int k = 0; k < 4; ++k) {
      s0 += part[((ft * 4 + k) * 4 + 2 * fg) * 64 + lane];
      s1 += part[((ft * 4 + k) * 4 + 2 * fg + 1) * 64 + lane];
    }
    const u32x4 ov = widen(pack2(s0[0], s0[1]), pack2(s0[2], s0[3]), pack2(s1[0], s1[1]), pack2(s1[2], s1[3]));
    const int col = 32 * (ft >> 1) + 16 * fg + 8 * h;
    if (col < 56) *(u32x4*)(out + (oqb * 128 + oqh * 64 + (ft & 1) * 32 + r) * DM + nb * 56 + col) = ov;
  }
}

__global__ __launch_bounds__(512, 2) void fused_block(const u16* __restrict__ x, const u16* __restrict__ WA, const u16* __restrict__ WB,
                                                      const u16* __restrict__ gate_w, const u16* __restrict__ WoA,
                                                      const u16* __restrict__ WoB, char* __restrict__ ws, u16* __restrict__ out) {
  extern __shared__ __attribute__((aligned(16))) char smem_[];
  char LDSP* smem = (char LDSP*)smem_;
  const rsrc_t rs = __builtin_amdgcn_make_buffer_rsrc(ws, 0, 32u << 20, 0x00020000);
  u64 tag;
  asm volatile("s_getpc_b64 %0" : "=s"(tag));
  ST(0, 0);
  u32x4 sv[8];
  unsigned so[8];
  const int b = blockIdx.x, xcd = b & 7, j = b >> 3;
  if (__builtin_expect(j < 28, 1)) {
    const int mt = 4 * (xcd & 3) + (j & 3);
    const int nt = 7 * (xcd >> 2) + (j >> 2);
    p1_first(x, WA, WB, mt, nt, smem);
    if (__builtin_amdgcn_readfirstlane(threadIdx.x >> 8) == 0) {
      if (nt < 9) p1_body<2, 0>(x, WA, WB, ws, rs, mt, nt, smem, tag, sv, so);
      else if (nt == 9) p1_body<2, 1>(x, WA, WB, ws, rs, mt, nt, smem, tag, sv, so);
      else p1_body<2, 2>(x, WA, WB, ws, rs, mt, nt, smem, tag, sv, so);
    } else {
      if (nt < 9) p1_body<1, 0>(x, WA, WB, ws, rs, mt, nt, smem, tag, sv, so);
      else p1_body<1, 1>(x, WA, WB, ws, rs, mt, nt, smem, tag, sv, so);
    }
  } else {
    warm_l2(x, WA, WB, smem, tag);
    p1_spare(x, gate_w, WoA, WoB, ws, rs, xcd * 4 + j - 28, smem, tag);
    asm volatile("s_waitcnt vmcnt(0)" ::: "memory");
#pragma unroll
    for (int e = 0; e < 8; ++e) { sv[e] = u32x4{}; so[e] = DROP; }
  }
  ST(0, 1);
  const int sp = xcd * 4 + j - 28;
  const int fidx = j < 28 ? (4 * (xcd & 3) + (j & 3)) * 16 + 7 * (xcd >> 2) + (j >> 2) : (sp >> 1) * 16 + 14 + (sp & 1);
  if ((b >> 3) & 1) p2_body<1>(ws, out, rs, smem, tag, fidx, sv, so);
  else p2_body<0>(ws, out, rs, smem, tag, fidx, sv, so);
}

extern "C" void kernel_launch(void* const* d_in, const int* in_sizes, int n_in, void* d_out, int out_size, void* d_ws,
                              size_t ws_size, hipStream_t stream) {
  const u16* x = (const u16*)d_in[0];
  const u16* WA = (const u16*)d_in[1];
  const u16* WoA = (const u16*)d_in[2];
  const u16* WB = (const u16*)d_in[4];
  const u16* WoB = (const u16*)d_in[5];
  const u16* gw = (const u16*)d_in[6];
  (void)hipFuncSetAttribute((const void*)fused_block, hipFuncAttributeMaxDynamicSharedMemorySize, LDS_BYTES);
  fused_block<<<256, 512, LDS_BYTES, stream>>>(x, WA, WB, gw, WoA, WoB, (char*)d_ws, (u16*)d_out);
}
```

```cpp
#include <hip/hip_runtime.h>
#include <hip/hip_bf16.h>
#include <stdint.h>
#include <type_traits>

typedef short bf16x8 __attribute__((ext_vector_type(8)));
typedef short s16x4 __attribute__((ext_vector_type(4)));
typedef float f32x16 __attribute__((ext_vector_type(16)));
typedef float f32x4 __attribute__((ext_vector_type(4)));
typedef float f32x2 __attribute__((ext_vector_type(2)));
typedef unsigned u32x4 __attribute__((ext_vector_type(4)));
typedef unsigned u32x2 __attribute__((ext_vector_type(2)));
typedef __bf16 bf2 __attribute__((ext_vector_type(2)));
typedef unsigned short u16;

#define DI __device__ __forceinline__
#define MFMA(a, b, c) __builtin_amdgcn_mfma_f32_32x32x16_bf16((a), (b), (c), 0, 0, 0)
#define LDSP __attribute__((address_space(3)))
#define GLBP __attribute__((address_space(1)))

constexpr int SEQ = 2048, DM = 448, NH = 16, DH = 28, N3 = 1344;
constexpr float QSCALE = 0.18898223650461362f * 1.4426950408889634f;

constexpr unsigned WS_Q = 0;
constexpr unsigned WS_K = 4u << 20;
constexpr unsigned WS_V = 8u << 20;
constexpr unsigned WS_O = 12u << 20;
constexpr unsigned WS_G = 16u << 20;
constexpr unsigned WS_W = 17u << 20;
constexpr unsigned WS_C = 19u << 20;
constexpr unsigned F1B = WS_C + 4096, F1A = WS_C + 8192, F2A = WS_C + 12288, F2B = WS_C + 16384;
constexpr unsigned QK_BR = 64 * 56 * 512;

DI unsigned pack2(float lo, float hi) {
  f32x2 v = {lo, hi};
  return __builtin_bit_cast(unsigned, __builtin_convertvector(v, bf2));
}
DI bf16x8 as_frag(u32x4 v) { return __builtin_bit_cast(bf16x8, v); }
DI float bf_lo(unsigned v) { return __uint_as_float(v << 16); }
DI float bf_hi(unsigned v) { return __uint_as_float(v & 0xffff0000u); }
DI float swap_max(float v) {
  auto rr = __builtin_amdgcn_permlane32_swap(__float_as_uint(v), __float_as_uint(v), false, false);
  return fmaxf(__uint_as_float(rr[0]), __uint_as_float(rr[1]));
}
DI s16x4 tr_read(const char LDSP* p) {
  return __builtin_amdgcn_ds_read_tr16_b64_v4i16((s16x4 LDSP*)p);
}
DI bf16x8 cat(s16x4 a, s16x4 b) { return __builtin_shufflevector(a, b, 0, 1, 2, 3, 4, 5, 6, 7); }
template <int S>
DI u32x4 pack_step(const f32x16& x, float sc) {
  u32x4 p;
  p[0] = pack2(x[8 * S + 0] * sc, x[8 * S + 1] * sc);
  p[1] = pack2(x[8 * S + 2] * sc, x[8 * S + 3] * sc);
  p[2] = pack2(x[8 * S + 4] * sc, x[8 * S + 5] * sc);
  p[3] = pack2(x[8 * S + 6] * sc, x[8 * S + 7] * sc);
  return p;
}
template <int S>
DI u32x4 pack_step1(const f32x16& x) {
  u32x4 p;
  p[0] = pack2(x[8 * S + 0], x[8 * S + 1]);
  p[1] = pack2(x[8 * S + 2], x[8 * S + 3]);
  p[2] = pack2(x[8 * S + 4], x[8 * S + 5]);
  p[3] = pack2(x[8 * S + 6], x[8 * S + 7]);
  return p;
}

#define RAWBAR() do { asm volatile("" ::: "memory"); __builtin_amdgcn_s_barrier(); asm volatile("" ::: "memory"); } while (0)
#define WAITV(n) ((((n) & 15) | (((n) >> 4) << 14)) | 0x0F70)
#define WAITVL(n) ((((n) & 15) | (((n) >> 4) << 14)) | 0x0070)
DI u32x4 widen(unsigned a0, unsigned a1, unsigned b0, unsigned b1) {
  auto r0 = __builtin_amdgcn_permlane32_swap(a0, b0, false, false);
  auto r1 = __builtin_amdgcn_permlane32_swap(a1, b1, false, false);
  return u32x4{r0[0], r1[0], r0[1], r1[1]};
}

typedef __amdgpu_buffer_rsrc_t rsrc_t;
typedef unsigned v4u_t __attribute__((ext_vector_type(4)));
typedef unsigned long long u64;
typedef GLBP u64 gu64;
DI u32x4 bload(rsrc_t rs, unsigned voff, unsigned soff) {
  return __builtin_bit_cast(u32x4, __builtin_amdgcn_raw_buffer_load_b128(rs, voff, soff, 16));
}
DI void bstore(u32x4 v, rsrc_t rs, unsigned voff, unsigned soff) {
  __builtin_amdgcn_raw_buffer_store_b128(__builtin_bit_cast(v4u_t, v), rs, voff + soff, 0, 16);
}
DI void set_flag(char* ws, unsigned off, u64 tag) {
  __hip_atomic_store((gu64*)(ws + off), tag, __ATOMIC_RELAXED, __HIP_MEMORY_SCOPE_AGENT);
}
DI u64 read_tag(const char* ws) {
  u64 e;
  asm volatile("s_load_dwordx2 %0, %1, 0x0\n\ts_waitcnt lgkmcnt(0)" : "=s"(e) : "s"(ws + WS_C) : "memory");
  return e + 1;
}
DI void wait_flags256(char* ws, unsigned off, u64 tag, int lane) {
  gu64* f = (gu64*)(ws + off) + lane;
  for (unsigned spins = 0; spins < (1u << 20); ++spins) {
    bool ok = true;
#pragma unroll
    for (int k = 0; k < 4; ++k) ok &= __hip_atomic_load(f + 64 * k, __ATOMIC_RELAXED, __HIP_MEMORY_SCOPE_AGENT) == tag;
    if (__all(ok)) break;
    __builtin_amdgcn_s_sleep(2);
  }
  asm volatile("" ::: "memory");
}
DI void wait_flags16(char* ws, unsigned off, u64 tag, int lane) {
  gu64* f = (gu64*)(ws + off) + (lane & 15);
  for (unsigned spins = 0; spins < (1u << 20); ++spins) {
    if (__all(__hip_atomic_load(f, __ATOMIC_RELAXED, __HIP_MEMORY_SCOPE_AGENT) == tag)) break;
    __builtin_amdgcn_s_sleep(1);
  }
  asm volatile("" ::: "memory");
}

constexpr int P1_WSLOT = 64 * 192;
constexpr int P1_SLOT = 16384 + P1_WSLOT;
constexpr int P1_CTL = 5 * P1_SLOT + P1_WSLOT;
constexpr int P1_SINK = P1_CTL + 64;
constexpr int LDS_BYTES = P1_SINK + 256;

constexpr unsigned DROP = 0x80000000u;
template <bool V>
DI void p1_pack(const f32x16& a, int br, int ct, int tile, int lane, u32x4* sv, unsigned* so) {
  const int type = ct / 14, cbase = (ct % 14) * 32;
  if (!V) {
    const float sc = type == 0 ? QSCALE : 1.f;
    const unsigned o = (type == 0 ? WS_Q : WS_K) + br * QK_BR + (tile * 56 + (cbase >> 3)) * 512 + lane * 16;
#pragma unroll
    for (int g = 0; g < 2; ++g) {
      sv[g] = widen(pack2(a[8 * g] * sc, a[8 * g + 1] * sc), pack2(a[8 * g + 2] * sc, a[8 * g + 3] * sc),
                    pack2(a[8 * g + 4] * sc, a[8 * g + 5] * sc), pack2(a[8 * g + 6] * sc, a[8 * g + 7] * sc));
      so[g] = o + g * 1024;
    }
  } else {
    const unsigned f = cbase + (lane & 31), head = f / 28, d = f % 28;
    const unsigned o = WS_V + ((br * 16 * 64 + tile) * 2) * 1024 + (head * 64 * 2 * 64 + d + (lane & 32)) * 16;
    sv[0] = pack_step1<0>(a); so[0] = o;
    sv[1] = pack_step1<1>(a); so[1] = o + 1024;
    const unsigned c = d == 0 ? 0x3F803F80u : 0u;
    sv[2] = u32x4{c, c, c, c}; so[2] = d < 4 ? o + 28 * 16 : DROP;
    sv[3] = sv[2]; so[3] = d < 4 ? o + 28 * 16 + 1024 : DROP;
  }
}

DI void warm_l2(const u16* x, const u16* WA, const u16* WB, char LDSP* smem) {
  const int b = blockIdx.x, xcd = b & 7;
  const unsigned g = ((b >> 3) - 28) * 512 + threadIdx.x;
#pragma unroll
  for (int e = 0; e < 4; ++e) {
    const unsigned i = e * 2048 + g;
    const char* p;
    if (i < 1024) {
      p = (const char*)x + ((xcd & 3) * 512 + (i >> 1)) * 896 + 640 + (i & 1) * 128;
    } else {
      const unsigned idx = i - 1024, row = idx / 12, c = idx % 12;
      unsigned off = (row < 128 ? row + 320 : row - 128) * 2688 + (xcd >> 2) * 1280 + c * 128;
      off = off < (unsigned)(DM * N3 * 2 - 4) ? off : 0u;
      p = (const char*)(row < 128 ? WB : WA) + off;
    }
    __builtin_amdgcn_global_load_lds((const GLBP void*)p, (LDSP void*)(smem + P1_SINK), 4, 0, 0);
  }
}

DI void lds_arrive(char LDSP* smem, int idx, int last, char* ws, unsigned flag_off, u64 tag, int lane) {
  if (lane == 0) {
    if (__hip_atomic_fetch_add((int LDSP*)(smem + P1_CTL) + idx, 1, __ATOMIC_RELAXED, __HIP_MEMORY_SCOPE_WORKGROUP) == last) set_flag(ws, flag_off, tag);
  }
}

DI void p1_first(const u16* __restrict__ x, const u16* __restrict__ WA, const u16* __restrict__ WB, int mt, int nt, char LDSP* smem) {
  const int lane = threadIdx.x & 63;
  const int w = __builtin_amdgcn_readfirstlane(threadIdx.x >> 6);
  const int wi0 = w < 4 ? w : 2 * w - 4;
  const u16* xs[2];
  int wso[2];
#pragma unroll
  for (int i = 0; i < 2; ++i) {
    int lr = (w * 2 + i) * 8 + (lane >> 3);
    int logical = (lane & 7) ^ ((lr >> 1) & 7);
    xs[i] = x + (mt * 128 + lr) * DM + logical * 8;
    const int id = (wi0 + i) * 64 + lane;
    wso[i] = (id / 12) * N3 + nt * 96 + (id % 12) * 8;
  }
#pragma unroll
  for (int c = 0; c < 6; ++c) {
    if (c < 5) {
#pragma unroll
      for (int i = 0; i < 2; ++i)
        __builtin_amdgcn_global_load_lds((const GLBP void*)(xs[i] + c * 64), (LDSP void*)(smem + c * P1_SLOT + (w * 2 + i) * 1024), 16, 0, 0);
    }
    const u16* Wc = c < 5 ? WB + c * 64 * N3 : WA + 6 * 64 * N3;
    char LDSP* dst = smem + (c < 5 ? c * P1_SLOT + 16384 : 5 * P1_SLOT) + wi0 * 1024;
    __builtin_amdgcn_global_load_lds((const GLBP void*)(Wc + wso[0]), (LDSP void*)dst, 16, 0, 0);
    if (w >= 4) __builtin_amdgcn_global_load_lds((const GLBP void*)(Wc + wso[1]), (LDSP void*)(dst + 1024), 16, 0, 0);
  }
}

template <int NT, int NV>
DI void p1_body(const u16* __restrict__ x, const u16* __restrict__ WA, const u16* __restrict__ WB, char* __restrict__ ws, rsrc_t rs,
                int mt, int nt, char LDSP* smem, u64& tag, u32x4 (&sv)[8], unsigned (&so)[8]) {
  const int tid = threadIdx.x, lane = tid & 63;
  const int w = __builtin_amdgcn_readfirstlane(tid >> 6);
  const int msub = w & 3;
  const int j0 = NT == 2 ? 0 : 2;
  const int r = lane & 31, h = lane >> 5;
  constexpr int NS = 2 * (NT - NV) + 4 * NV;
  constexpr int NW = NT == 2 ? 1 : 2;
  const int wi0 = NT == 2 ? w : 2 * w - 4;
  const u16* xs[2];
  int wso[2];
#pragma unroll
  for (int i = 0; i < 2; ++i) {
    int lr = (w * 2 + i) * 8 + (lane >> 3);
    int logical = (lane & 7) ^ ((lr >> 1) & 7);
    xs[i] = x + (mt * 128 + lr) * DM + logical * 8;
    const int id = (wi0 + i) * 64 + lane;
    wso[i] = (id / 12) * N3 + nt * 96 + (id % 12) * 8;
  }
  auto wbase = [&](int s) {
    return s < 7 ? (s % 5) * P1_SLOT + 16384 : (s < 13 ? (2 + (s - 7) / 2) * P1_SLOT + ((s - 7) & 1) * P1_WSLOT : 5 * P1_SLOT);
  };
  auto issue_x = [&](int c, int i) {
    __builtin_amdgcn_global_load_lds((const GLBP void*)(xs[i] + c * 64), (LDSP void*)(smem + (c % 5) * P1_SLOT + (w * 2 + i) * 1024), 16, 0, 0);
  };
  auto issue_w = [&](int s) {
#pragma unroll
    for (int i = 0; i < NW; ++i)
      __builtin_amdgcn_global_load_lds((const GLBP void*)((s < 7 ? WB : WA) + wso[i] + (s % 7) * 64 * N3),
                                       (LDSP void*)(smem + wbase(s) + (wi0 + i) * 1024), 16, 0, 0);
  };
  int xoff[4];
  {
    int lr = 32 * msub + r, sw = (lr >> 1) & 7;
#pragma unroll
    for (int t = 0; t < 4; ++t) xoff[t] = lr * 128 + (((2 * t + h) ^ sw) << 4);
  }
  const int q4 = (lane & 15) >> 2, p4 = lane & 3, g16 = (lane >> 4) & 1;
  const int woff = (8 * h + q4) * 192 + 64 * j0 + 32 * g16 + 8 * p4;

  f32x16 acc[NT] = {};
  bf16x8 xf[7][4];
  s16x4 wl[2][4][NT], wh[2][4][NT];
  auto readf = [&](int s, int m) {
    const int t = m / NT, j = m % NT;
    if (j == 0 && s < 7) xf[s][t] = *(const bf16x8 LDSP*)(smem + (s % 5) * P1_SLOT + xoff[t]);
    const char LDSP* wp = smem + woff + wbase(s) + t * 16 * 192 + j * 64;
    wl[s & 1][t][j] = tr_read(wp);
    wh[s & 1][t][j] = tr_read(wp + 4 * 192);
  };
  tag = read_tag(ws);
  if (tid < 8) ((int LDSP*)(smem + P1_CTL))[tid] = 0;
  __builtin_amdgcn_s_waitcnt(WAITVL(4 * (NW + 2) + NW));
  RAWBAR();
#pragma unroll
  for (int m = 0; m < 4 * NT; ++m) readf(0, m);
#pragma unroll
  for (int s = 0; s < 14; ++s) {
    if (s < 7 || s == 8) {
      if (s < 3) __builtin_amdgcn_s_waitcnt(WAITVL(3 * (NW + 2) + NW));
      else if (s == 3) __builtin_amdgcn_s_waitcnt(WAITVL(2 * (NW + 2) + 3 * NW));
      else if (s == 4) __builtin_amdgcn_s_waitcnt(WAITVL(NW + 2 + 4 * NW));
      else if (s == 5) __builtin_amdgcn_s_waitcnt(WAITVL(6 * NW));
      else if (s == 6) __builtin_amdgcn_s_waitcnt(WAITVL(4 * NW));
      else __builtin_amdgcn_s_waitcnt(WAITVL(NS));
      RAWBAR();
    }
    if (s == 12) {
      __builtin_amdgcn_s_waitcnt(WAITV(0));
      asm volatile("" ::: "memory");
      lds_arrive(smem, 4, 7, ws, F1B + (nt * 16 + mt) * 8, tag, lane);
    }
    __builtin_amdgcn_sched_barrier(0);
#pragma unroll
    for (int m = 0; m < 4 * NT; ++m) {
      const int t = m / NT, j = m % NT;
      const bf16x8 wfr = cat(wl[s & 1][t][j], wh[s & 1][t][j]);
      if (j < NT - NV) acc[j] = MFMA(wfr, xf[s % 7][t], acc[j]);
      else acc[j] = MFMA(xf[s % 7][t], wfr, acc[j]);
      __builtin_amdgcn_sched_barrier(0);
      if (s < 13) readf(s + 1, m);
      if (j == 0) {
        if (s < 2) { if (t < 2) issue_x(s + 5, t); else if (t == 2) issue_w(s + 5); }
        else if (s < 5 && t < 2) issue_w(7 + 2 * (s - 2) + t);
      }
      __builtin_amdgcn_sched_barrier(0);
    }
    if (s == 6 || s == 13) {
#pragma unroll
      for (int e = 0; e < 8; ++e) so[e] = DROP;
#pragma unroll
      for (int j = 0; j < NT; ++j) {
        if (j < NT - NV) p1_pack<false>(acc[j], s == 6, nt * 3 + j0 + j, mt * 4 + msub, lane, sv + 4 * j, so + 4 * j);
        else p1_pack<true>(acc[j], s == 6, nt * 3 + j0 + j, mt * 4 + msub, lane, sv + 4 * j, so + 4 * j);
        if (s == 6) {
#pragma unroll
          for (int e = 0; e < (j < NT - NV ? 2 : 4); ++e) bstore(sv[4 * j + e], rs, so[4 * j + e], 0);
        }
        acc[j] = f32x16{};
      }
      __builtin_amdgcn_sched_barrier(0);
    }
  }
}

DI void p1_spare(const u16* __restrict__ x, const u16* __restrict__ gate_w, const u16* __restrict__ WoA, const u16* __restrict__ WoB,
                 char* __restrict__ ws, rsrc_t rs, int sp, char LDSP* smem, u64& tag) {
  const int tid = threadIdx.x, lane = tid & 63, r = lane & 31, h = lane >> 5;
  tag = read_tag(ws);
  if (tid < 8) ((int LDSP*)(smem + P1_CTL))[tid] = 0;
  __builtin_amdgcn_s_waitcnt(0xC07F);
  RAWBAR();
  if (tid == 0) set_flag(ws, F1B + ((14 + (sp & 1)) * 16 + (sp >> 1)) * 8, tag);
#pragma unroll
  for (int e = 0; e < 4; ++e) {
    const int fid = sp * 32 + (tid >> 6) * 4 + e;
    const int sfr = fid & 1, vh = (fid >> 1) & 31, nt = (fid >> 6) & 1, nb = fid >> 7;
    const int n = 56 * nb + 32 * nt + r;
    const u16* Wo = (vh >> 4) ? WoB : WoA;
    unsigned v[8];
#pragma unroll
    for (int jj = 0; jj < 8; ++jj) {
      const int d = 16 * sfr + 8 * (jj >> 2) + 4 * h + (jj & 3);
      const bool ok = d < DH && n < DM;
      const unsigned tv = Wo[ok ? ((vh & 15) * DH + d) * DM + n : 0];
      v[jj] = ok ? tv : 0u;
    }
    bstore(u32x4{v[0] | (v[1] << 16), v[2] | (v[3] << 16), v[4] | (v[5] << 16), v[6] | (v[7] << 16)}, rs, WS_W + fid * 1024 + lane * 16, 0);
  }
  const int row = sp * 64 + (tid >> 3), part = tid & 7;
  const u32x4* xp = (const u32x4*)(x + row * DM) + part;
  const u32x4* gp = (const u32x4*)(gate_w) + part;
  float acc = 0.f;
#pragma unroll
  for (int i = 0; i < 7; ++i) {
    u32x4 a = xp[i * 8], bb = gp[i * 8];
#pragma unroll
    for (int e = 0; e < 4; ++e) {
      acc = fmaf(bf_lo(a[e]), bf_lo(bb[e]), acc);
      acc = fmaf(bf_hi(a[e]), bf_hi(bb[e]), acc);
    }
  }
  acc += __shfl_xor(acc, 1);
  acc += __shfl_xor(acc, 2);
  acc += __shfl_xor(acc, 4);
  if (part == 0)
    __hip_atomic_store((GLBP float*)(ws + WS_G) + row, 1.f / (1.f + __expf(-acc)), __ATOMIC_RELAXED, __HIP_MEMORY_SCOPE_AGENT);
}

constexpr unsigned long long lmask(int i, bool prev) {
  unsigned long long m = 0;
  for (int l = 0; l < 64; ++l) {
    int r = l & 31, h = l >> 5;
    int kr = (i & 3) + 8 * (i >> 2) + 4 * h;
    int diff = r - kr + (prev ? 32 : 0);
    if (diff >= 0 && diff <= 14 && (diff & 1) == 0) m |= 1ull << l;
  }
  return m;
}
DI float sel_mask(float val, float other, unsigned long long mask) {
  return __builtin_amdgcn_inverse_ballot_w64(mask) ? val : other;
}
template <int ODD>
DI void set_foreign(u32x4& f0, u32x4& f1, int h, unsigned a, unsigned b) {
  if (ODD) { if (!h) { f0[0] = a; f0[1] = b; } }
  else { if (h) { f1[2] = a; f1[3] = b; } }
}
DI float gload(char* ws, int idx) { return __hip_atomic_load((GLBP float*)(ws + WS_G) + idx, __ATOMIC_RELAXED, __HIP_MEMORY_SCOPE_AGENT); }

template <int ODD>
DI void p2_body(char* __restrict__ ws, u16* __restrict__ out, rsrc_t rs, char LDSP* smem, const u64 tag, const int fidx,
                const u32x4 (&sv)[8], const unsigned (&so)[8]) {
  f32x4 LDSP* part = (f32x4 LDSP*)smem;
  float LDSP* cw = (float LDSP*)(smem + 131072);
  volatile int LDSP* ctl = (volatile int LDSP*)(smem + P1_CTL);
  const int tid = threadIdx.x;
  int lane = tid & 63;
  const int w = __builtin_amdgcn_readfirstlane(tid >> 6);
  int r = lane & 31, h = lane >> 5;
  const int b = blockIdx.x;
  const int head = (b & 7) * 2 + ODD, qb = b >> 4;
  const int oqb = (b & 7) * 2 + (b >> 7), oqh = (b >> 3) & 1, nb = (b >> 4) & 7;
  const unsigned p0 = (7 * head) >> 1;
  unsigned l16 = lane * 16;
  const unsigned qoff = WS_Q + QK_BR + (qb * 4 * 56 + p0) * 512;
  const unsigned koff = WS_K + QK_BR + (w * 8 * 56 + p0) * 512;
  const unsigned voff = WS_V + ((16 + head) * 64 + w * 8) * 2048;
  __syncthreads();
  if (w < 4) {
#pragma unroll
    for (int e = 0; e < 8; ++e) {
      ((u32x4 LDSP*)(smem + 65536))[(w * 8 + e) * 64 + lane] = sv[e];
      ((unsigned LDSP*)(smem + 65536 + 32768))[(w * 8 + e) * 64 + lane] = so[e];
    }
  }
  if (w == 0) {
    const int sel = lane >> 4;
    const int c0 = sel < 2 ? 448 + 8 * (int)p0 + (sel & 1) * 31 : 896 + 28 * head + (sel & 1) * 27;
    gu64* f = (gu64*)(ws + F1B) + (c0 / 96) * 16 + (lane & 15);
    gu64* fq = (gu64*)(ws + F1B) + ((8 * (int)p0 + (lane & 1) * 31) / 96) * 16 + qb;
    for (unsigned spins = 0; spins < (1u << 20); ++spins) {
      bool ok = __hip_atomic_load(f, __ATOMIC_RELAXED, __HIP_MEMORY_SCOPE_AGENT) == tag;
      ok &= __hip_atomic_load(fq, __ATOMIC_RELAXED, __HIP_MEMORY_SCOPE_AGENT) == tag;
      if (__all(ok)) break;
      __builtin_amdgcn_s_sleep(1);
    }
    asm volatile("" ::: "memory");
  }
  __syncthreads();
  u32x4 q[4][2];
  u32x4 kf[4][2], vf[4][2];
  auto loadkv = [&](int t) {
    kf[t & 3][0] = bload(rs, l16, koff + t * 28672);
    kf[t & 3][1] = bload(rs, l16 + 1024, koff + t * 28672);
    vf[t & 3][0] = bload(rs, l16, voff + t * 2048);
    vf[t & 3][1] = bload(rs, l16 + 1024, voff + t * 2048);
  };
  auto store_a = [&]() {
#pragma unroll
    for (int e = 0; e < 4; ++e) bstore(sv[e], rs, so[e], 0);
#pragma unroll
    for (int e = 0; e < 8; ++e)
      bstore(((u32x4 LDSP*)(smem + 65536))[((w - 4) * 8 + e) * 64 + lane], rs, ((unsigned LDSP*)(smem + 65536 + 32768))[((w - 4) * 8 + e) * 64 + lane], 0);
  };
  if (w >= 4) {
    store_a();
    asm volatile("s_waitcnt vmcnt(0)" ::: "memory");
    lds_arrive(smem, 2, 3, ws, F1A + fidx * 8, tag, lane);
    if (b == 0 && w == 5) {
      wait_flags256(ws, F1B, tag, lane);
      if (lane == 0) set_flag(ws, WS_C, tag);
    }
    __builtin_amdgcn_s_setprio(1);
  }
  kf[0][0] = bload(rs, l16, koff);
  kf[0][1] = bload(rs, l16 + 1024, koff);
#pragma unroll
  for (int i = 0; i < 4; ++i) { q[i][0] = bload(rs, l16, qoff + i * 28672); q[i][1] = bload(rs, l16 + 1024, qoff + i * 28672); }
  vf[0][0] = bload(rs, l16, voff);
  vf[0][1] = bload(rs, l16 + 1024, voff);
  loadkv(1);
  loadkv(2);
  __builtin_amdgcn_sched_barrier(0);

  auto fold_shift = [&](int i, float m) {
    m = swap_max(m);
    if (!h) cw[(i * 32 + r) * 8 + w] = m;
    const float c = -m;
    unsigned p01 = pack2(c, 0.f);
    float r1 = c - bf_lo(p01);
    unsigned p1 = pack2(r1, 0.f);
    float r2 = r1 - bf_lo(p1);
    unsigned p2 = pack2(r2, 0.f);
    set_foreign<ODD>(q[i][0], q[i][1], h, (p01 & 0xffffu) | (p1 << 16), p2 & 0xffffu);
  };
  const int ri = w >> 1, rs1 = w & 1;
  f32x4 a0, a1;
  float l;
  auto write_part = [&](f32x16 (&o)[4]) {
#pragma unroll
    for (int i = 0; i < 4; ++i)
#pragma unroll
      for (int g = 0; g < 4; ++g)
        part[((w * 4 + i) * 4 + g) * 64 + lane] = f32x4{o[i][4 * g], o[i][4 * g + 1], o[i][4 * g + 2], o[i][4 * g + 3]};
  };
  auto reduce = [&]() {
    const f32x4 c0 = *(f32x4 LDSP*)(cw + (ri * 32 + r) * 8), c1 = *(f32x4 LDSP*)(cw + (ri * 32 + r) * 8 + 4);
    const float cm = fmaxf(fmaxf(fmaxf(c0[0], c0[1]), fmaxf(c0[2], c0[3])), fmaxf(fmaxf(c1[0], c1[1]), fmaxf(c1[2], c1[3])));
    a0 = f32x4{}; a1 = f32x4{}; l = 0.f;
#pragma unroll
    for (int ww = 0; ww < 8; ++ww) {
      const float wt = __builtin_amdgcn_exp2f((ww < 4 ? c0[ww & 3] : c1[ww & 3]) - cm);
      a0 += part[((ww * 4 + ri) * 4 + 2 * rs1) * 64 + lane] * wt;
      a1 += part[((ww * 4 + ri) * 4 + 2 * rs1 + 1) * 64 + lane] * wt;
      l += ((float LDSP*)(part + ((ww * 4 + ri) * 4 + 3) * 64 + 32 + r))[0] * wt;
    }
  };
  const int qt = w & 1, kq = w >> 1;
  u32x4 wfr[2][2][8];
  auto loadW = [&]() {
#pragma unroll
    for (int br = 0; br < 2; ++br)
#pragma unroll
      for (int nt = 0; nt < 2; ++nt) {
        const unsigned wo = WS_W + (((nb * 2 + nt) * 32 + br * 16 + 4 * kq) * 2) * 1024;
#pragma unroll
        for (int e = 0; e < 8; ++e) wfr[br][nt][e] = bload(rs, l16 + (e & 3) * 1024, wo + (e >> 2) * 4096);
      }
  };

#pragma unroll
  for (int i = 0; i < 4; ++i) {
    set_foreign<ODD>(q[i][0], q[i][1], h, 0u, 0u);
    f32x16 s = MFMA(as_frag(kf[0][0]), as_frag(q[i][0]), f32x16{});
    s = MFMA(as_frag(kf[0][1]), as_frag(q[i][1]), s);
    fold_shift(i, fmaxf(fmaxf(s[0], s[1]), s[2]));
  }
  __builtin_amdgcn_s_setprio(0);
  {
    f32x16 o[4] = {};
    set_foreign<ODD>(kf[0][0], kf[0][1], h, 0x3F803F80u, 0x00003F80u);
    f32x16 sa = MFMA(as_frag(kf[0][0]), as_frag(q[0][0]), f32x16{});
    sa = MFMA(as_frag(kf[0][1]), as_frag(q[0][1]), sa);
    f32x16 sb;
    u32x4 pa;
    u32x4 pbp;
    float e3[4];
#define SB __builtin_amdgcn_sched_barrier(0)
#define EXPQ(S, g) { S[4 * g] = __builtin_amdgcn_exp2f(S[4 * g]); S[4 * g + 1] = __builtin_amdgcn_exp2f(S[4 * g + 1]); \
                     S[4 * g + 2] = __builtin_amdgcn_exp2f(S[4 * g + 2]); S[4 * g + 3] = __builtin_amdgcn_exp2f(S[4 * g + 3]); }
    auto step = [&](auto nc, f32x16& sc, f32x16& sn) {
      constexpr int n = decltype(nc)::value;
      constexpr int t = n >> 2, i = n & 3;
      constexpr int t1 = ((n + 1) >> 2) & 3, i1 = (n + 1) & 3;
      constexpr int tp = n > 0 ? ((n - 1) >> 2) & 3 : 0, ip = n > 0 ? (n - 1) & 3 : 0;
      if (i == 1 && t + 3 < 8) loadkv(t + 3);
      if (i == 3 && n + 1 < 32) set_foreign<ODD>(kf[t1][0], kf[t1][1], h, 0x3F803F80u, 0x00003F80u);
      SB;
      if (n + 1 < 32) sn = MFMA(as_frag(kf[t1][0]), as_frag(q[i1][0]), f32x16{});
      SB;
      if (n > 0) { pbp[2] = pack2(e3[0], e3[1]); pbp[3] = pack2(e3[2], e3[3]); }
      SB;
      EXPQ(sc, 0);
      SB;
      if (n + 1 < 32) sn = MFMA(as_frag(kf[t1][1]), as_frag(q[i1][1]), sn);
      SB;
      pa[0] = pack2(sc[0], sc[1]); pa[1] = pack2(sc[2], sc[3]);
      SB;
      EXPQ(sc, 1);
      SB;
      if (n > 0) o[ip] = MFMA(as_frag(vf[tp][1]), as_frag(pbp), o[ip]);
      SB;
      pa[2] = pack2(sc[4], sc[5]); pa[3] = pack2(sc[6], sc[7]);
      SB;
      EXPQ(sc, 2);
      SB;
      o[i] = MFMA(as_frag(vf[t & 3][0]), as_frag(pa), o[i]);
      SB;
      pbp[0] = pack2(sc[8], sc[9]); pbp[1] = pack2(sc[10], sc[11]);
      SB;
      EXPQ(sc, 3);
      e3[0] = sc[12]; e3[1] = sc[13]; e3[2] = sc[14]; e3[3] = sc[15];
      SB;
    };
#define STEP2(n) step(std::integral_constant<int, n>{}, sa, sb); step(std::integral_constant<int, n + 1>{}, sb, sa);
    STEP2(0) STEP2(2) STEP2(4) STEP2(6) STEP2(8) STEP2(10) STEP2(12) STEP2(14)
    STEP2(16) STEP2(18) STEP2(20) STEP2(22) STEP2(24) STEP2(26) STEP2(28) STEP2(30)
#undef STEP2
    pbp[2] = pack2(e3[0], e3[1]); pbp[3] = pack2(e3[2], e3[3]);
    o[3] = MFMA(as_frag(vf[3][1]), as_frag(pbp), o[3]);
    asm volatile("" : "+v"(lane));
    r = lane & 31; h = lane >> 5; l16 = lane * 16;
    write_part(o);
  }
  SB;
  if (w < 4) {
    {
      gu64* f = (gu64*)(ws + F1A) + (qb > 0 ? qb - 1 : 0) * 16 + (lane & 31) - ((lane & 15) >= 14 ? 2 : 0);
      for (unsigned spins = 0; spins < (1u << 20); ++spins) {
        if (__all(__hip_atomic_load(f, __ATOMIC_RELAXED, __HIP_MEMORY_SCOPE_AGENT) == tag)) break;
        __builtin_amdgcn_s_sleep(2);
      }
      asm volatile("" ::: "memory");
    const int ltile = qb * 4 + (w & 3);
    const unsigned lqo = WS_Q + (ltile * 56 + p0) * 512, lko = WS_K + (ltile * 56 + p0) * 512, lvo = WS_V + (head * 64 + ltile) * 2048;
    const unsigned pd = ltile > 0 ? 1 : 0;
    u32x4 lq0 = bload(rs, l16, lqo), lq1 = bload(rs, l16 + 1024, lqo);
    u32x4 lk0 = bload(rs, l16, lko), lk1 = bload(rs, l16 + 1024, lko);
    u32x4 pk0 = bload(rs, l16, lko - pd * 28672), pk1 = bload(rs, l16 + 1024, lko - pd * 28672);
    u32x4 lv0 = bload(rs, l16, lvo), lv1 = bload(rs, l16 + 1024, lvo);
    u32x4 pv1 = bload(rs, l16 + 1024, lvo - pd * 2048);
    set_foreign<ODD>(lq0, lq1, h, 0u, 0u);
    f32x16 sp = MFMA(as_frag(pk0), as_frag(lq0), f32x16{});
    sp = MFMA(as_frag(pk1), as_frag(lq1), sp);
    f32x16 sc = MFMA(as_frag(lk0), as_frag(lq0), f32x16{});
    sc = MFMA(as_frag(lk1), as_frag(lq1), sc);
    const bool first = (ltile == 0);
    const float NEG = -3.0e38f;
    float m = NEG;
#define LM_C(i) { sc[i] = sel_mask(sc[i], NEG, lmask(i, false)); m = fmaxf(m, sc[i]); }
#define LM_P(i) { sp[i] = sel_mask(sp[i], NEG, first ? 0ull : lmask(i, true)); m = fmaxf(m, sp[i]); }
    LM_C(0) LM_C(1) LM_C(2) LM_C(3) LM_C(4) LM_C(5) LM_C(6) LM_C(7)
    LM_C(8) LM_C(9) LM_C(10) LM_C(11) LM_C(12) LM_C(13) LM_C(14) LM_C(15)
    LM_P(8) LM_P(9) LM_P(10) LM_P(11) LM_P(12) LM_P(13) LM_P(14) LM_P(15)
#undef LM_C
#undef LM_P
    m = swap_max(m);
#pragma unroll
    for (int i = 0; i < 16; ++i) sc[i] = __builtin_amdgcn_exp2f(sc[i] - m);
#pragma unroll
    for (int i = 8; i < 16; ++i) sp[i] = __builtin_amdgcn_exp2f(sp[i] - m);
    f32x16 o = MFMA(as_frag(pv1), as_frag(pack_step1<1>(sp)), f32x16{});
    o = MFMA(as_frag(lv0), as_frag(pack_step1<0>(sc)), o);
    o = MFMA(as_frag(lv1), as_frag(pack_step1<1>(sc)), o);
    float ll = __shfl(o[12], 32 + r);
    float inv = 1.f / ll;
    u32x4 o0 = pack_step<0>(o, inv), o1 = pack_step<1>(o, inv);
    if (h) { o1[2] = 0; o1[3] = 0; }
    const unsigned oo = WS_O + ((ltile * 32 + head) * 2) * 1024;
    bstore(o0, rs, l16, oo);
    bstore(o1, rs, l16 + 1024, oo);
    asm volatile("s_waitcnt vmcnt(0)" ::: "memory");
    lds_arrive(smem, 1, 3, ws, F2A + (qb * 16 + head) * 8, tag, lane);
    }
    if (w == 0) {
      gu64* f = (gu64*)(ws + F1A) + ((lane & 31) >> 1) * 16 + 14 + (lane & 1);
      for (unsigned spins = 0; spins < (1u << 20); ++spins) {
        if (__all(__hip_atomic_load(f, __ATOMIC_RELAXED, __HIP_MEMORY_SCOPE_AGENT) == tag)) break;
        __builtin_amdgcn_s_sleep(2);
      }
      asm volatile("" ::: "memory");
    }
  }
  __syncthreads();
  loadW();
  reduce();
  {
    float am = fmaxf(fmaxf(fmaxf(fabsf(a0[0]), fabsf(a0[1])), fmaxf(fabsf(a0[2]), fabsf(a0[3]))),
                     fmaxf(fmaxf(fabsf(a1[0]), fabsf(a1[1])), fmaxf(fabsf(a1[2]), fabsf(a1[3]))));
    float chk = (a0[0] + a0[1] + a0[2] + a0[3]) * 0.f + (a1[0] + a1[1] + a1[2] + a1[3]) * 0.f;
    bool bad = !(l < 1.0e30f) || !(am < 3.0e38f) || !(chk == 0.f);
    if (bad) ctl[0] = 1;
    __syncthreads();
  }
  if (__builtin_amdgcn_readfirstlane(ctl[0])) {
#pragma unroll
    for (int i = 0; i < 4; ++i) {
      q[i][0] = bload(rs, l16, qoff + i * 28672); q[i][1] = bload(rs, l16 + 1024, qoff + i * 28672);
      set_foreign<ODD>(q[i][0], q[i][1], h, 0u, 0u);
    }
    float m[4] = {-3.0e38f, -3.0e38f, -3.0e38f, -3.0e38f};
#pragma unroll 1
    for (int t = 0; t < 8; ++t) {
      u32x4 k0 = bload(rs, l16, koff + t * 28672), k1 = bload(rs, l16 + 1024, koff + t * 28672);
#pragma unroll
      for (int i = 0; i < 4; ++i) {
        f32x16 s = MFMA(as_frag(k0), as_frag(q[i][0]), f32x16{});
        s = MFMA(as_frag(k1), as_frag(q[i][1]), s);
#pragma unroll
        for (int j = 0; j < 16; ++j) m[i] = fmaxf(m[i], s[j]);
      }
    }
#pragma unroll
    for (int i = 0; i < 4; ++i) fold_shift(i, m[i]);
    f32x16 o[4] = {};
#pragma unroll 1
    for (int t = 0; t < 8; ++t) {
      u32x4 k0 = bload(rs, l16, koff + t * 28672), k1 = bload(rs, l16 + 1024, koff + t * 28672);
      u32x4 v0 = bload(rs, l16, voff + t * 2048), v1 = bload(rs, l16 + 1024, voff + t * 2048);
      set_foreign<ODD>(k0, k1, h, 0x3F803F80u, 0x00003F80u);
#pragma unroll
      for (int i = 0; i < 4; ++i) {
        f32x16 s = MFMA(as_frag(k0), as_frag(q[i][0]), f32x16{});
        s = MFMA(as_frag(k1), as_frag(q[i][1]), s);
#pragma unroll
        for (int j = 0; j < 16; ++j) s[j] = __builtin_amdgcn_exp2f(s[j]);
        o[i] = MFMA(as_frag(v0), as_frag(pack_step1<0>(s)), o[i]);
        o[i] = MFMA(as_frag(v1), as_frag(pack_step1<1>(s)), o[i]);
      }
    }
    write_part(o);
    __syncthreads();
    reduce();
    loadW();
  }
  {
    const float inv = 1.f / l;
    u32x4 ov;
    ov[0] = pack2(a0[0] * inv, a0[1] * inv);
    ov[1] = pack2(a0[2] * inv, a0[3] * inv);
    ov[2] = pack2(a1[0] * inv, a1[1] * inv);
    ov[3] = pack2(a1[2] * inv, a1[3] * inv);
    if (h && rs1) { ov[2] = 0; ov[3] = 0; }
    bstore(ov, rs, l16, WS_O + (((qb * 4 + ri) * 32 + 16 + head) * 2 + rs1) * 1024);
  }
  const int oqt = oqb * 4 + oqh * 2 + qt;
  const unsigned ofo = WS_O + ((oqt * 32 + 4 * kq) * 2) * 1024;
  const float gt = gload(ws, oqt * 32 + r);
  if (w == 1) wait_flags16(ws, F2A + oqb * 128, tag, lane);
  asm volatile("s_waitcnt vmcnt(0)" ::: "memory");
  __syncthreads();
  if (tid == 0) set_flag(ws, F2B + (qb * 16 + head) * 8, tag);
  {
    u32x4 ofr[8];
    f32x16 acc[2][2] = {};
#pragma unroll
    for (int e = 0; e < 8; ++e) ofr[e] = bload(rs, l16 + (e & 3) * 1024, ofo + (e >> 2) * 4096);
#pragma unroll
    for (int e = 0; e < 8; ++e)
#pragma unroll
      for (int nt = 0; nt < 2; ++nt) acc[0][nt] = MFMA(as_frag(wfr[0][nt][e]), as_frag(ofr[e]), acc[0][nt]);
    if (w == 0) wait_flags16(ws, F2B + oqb * 128, tag, lane);
    __syncthreads();
#pragma unroll
    for (int e = 0; e < 8; ++e) ofr[e] = bload(rs, l16 + (e & 3) * 1024, ofo + 32768 + (e >> 2) * 4096);
#pragma unroll
    for (int e = 0; e < 8; ++e)
#pragma unroll
      for (int nt = 0; nt < 2; ++nt) acc[1][nt] = MFMA(as_frag(wfr[1][nt][e]), as_frag(ofr[e]), acc[1][nt]);
#pragma unroll
    for (int nt = 0; nt < 2; ++nt)
#pragma unroll
      for (int g = 0; g < 4; ++g) {
        f32x4 v;
#pragma unroll
        for (int e = 0; e < 4; ++e) v[e] = acc[0][nt][4 * g + e] * gt + acc[1][nt][4 * g + e] * (1.f - gt);
        part[(((nt * 2 + qt) * 4 + kq) * 4 + g) * 64 + lane] = v;
      }
  }
  __syncthreads();
  {
    const int ft = w & 3, fg = w >> 2;
    f32x4 s0 = {}, s1 = {};
#pragma unroll
    for (int k = 0; k < 4; ++k) {
      s0 += part[((ft * 4 + k) * 4 + 2 * fg) * 64 + lane];
      s1 += part[((ft * 4 + k) * 4 + 2 * fg + 1) * 64 + lane];
    }
    const u32x4 ov = widen(pack2(s0[0], s0[1]), pack2(s0[2], s0[3]), pack2(s1[0], s1[1]), pack2(s1[2], s1[3]));
    const int col = 32 * (ft >> 1) + 16 * fg + 8 * h;
    if (col < 56) *(u32x4*)(out + (oqb * 128 + oqh * 64 + (ft & 1) * 32 + r) * DM + nb * 56 + col) = ov;
  }
}

__global__ __launch_bounds__(512, 2) void fused_block(const u16* __restrict__ x, const u16* __restrict__ WA, const u16* __restrict__ WB,
                                                      const u16* __restrict__ gate_w, const u16* __restrict__ WoA,
                                                      const u16* __restrict__ WoB, char* __restrict__ ws, u16* __restrict__ out) {
  extern __shared__ __attribute__((aligned(16))) char smem_[];
  char LDSP* smem = (char LDSP*)smem_;
  const rsrc_t rs = __builtin_amdgcn_make_buffer_rsrc(ws, 0, 32u << 20, 0x00020000);
  u64 tag;
  asm volatile("s_getpc_b64 %0" : "=s"(tag));
  __builtin_amdgcn_global_load_lds((const GLBP void*)((const char*)tag + (((blockIdx.x >> 3) * 8 + (threadIdx.x >> 6)) * 2 + (threadIdx.x & 1)) * 128),
                                   (LDSP void*)(smem + P1_SINK), 4, 0, 0);
  u32x4 sv[8];
  unsigned so[8];
  const int b = blockIdx.x, xcd = b & 7, j = b >> 3;
  if (__builtin_expect(j < 28, 1)) {
    const int mt = 4 * (xcd & 3) + (j & 3);
    const int nt = 7 * (xcd >> 2) + (j >> 2);
    p1_first(x, WA, WB, mt, nt, smem);
    if (__builtin_amdgcn_readfirstlane(threadIdx.x >> 8) == 0) {
      if (nt < 9) p1_body<2, 0>(x, WA, WB, ws, rs, mt, nt, smem, tag, sv, so);
      else if (nt == 9) p1_body<2, 1>(x, WA, WB, ws, rs, mt, nt, smem, tag, sv, so);
      else p1_body<2, 2>(x, WA, WB, ws, rs, mt, nt, smem, tag, sv, so);
    } else {
      if (nt < 9) p1_body<1, 0>(x, WA, WB, ws, rs, mt, nt, smem, tag, sv, so);
      else p1_body<1, 1>(x, WA, WB, ws, rs, mt, nt, smem, tag, sv, so);
    }
  } else {
    warm_l2(x, WA, WB, smem);
    p1_spare(x, gate_w, WoA, WoB, ws, rs, xcd * 4 + j - 28, smem, tag);
    asm volatile("s_waitcnt vmcnt(0)" ::: "memory");
#pragma unroll
    for (int e = 0; e < 8; ++e) { sv[e] = u32x4{}; so[e] = DROP; }
  }
  const int sp = xcd * 4 + j - 28;
  const int fidx = j < 28 ? (4 * (xcd & 3) + (j & 3)) * 16 + 7 * (xcd >> 2) + (j >> 2) : (sp >> 1) * 16 + 14 + (sp & 1);
  if ((b >> 3) & 1) p2_body<1>(ws, out, rs, smem, tag, fidx, sv, so);
  else p2_body<0>(ws, out, rs, smem, tag, fidx, sv, so);
}

extern "C" void kernel_launch(void* const* d_in, const int* in_sizes, int n_in, void* d_out, int out_size, void* d_ws,
                              size_t ws_size, hipStream_t stream) {
  const u16* x = (const u16*)d_in[0];
  const u16* WA = (const u16*)d_in[1];
  const u16* WoA = (const u16*)d_in[2];
  const u16* WB = (const u16*)d_in[4];
  const u16* WoB = (const u16*)d_in[5];
  const u16* gw = (const u16*)d_in[6];
  (void)hipFuncSetAttribute((const void*)fused_block, hipFuncAttributeMaxDynamicSharedMemorySize, LDS_BYTES);
  fused_block<<<256, 512, LDS_BYTES, stream>>>(x, WA, WB, gw, WoA, WoB, (char*)d_ws + ((ws_size - (32u << 20)) & ~(size_t)0x1fffff), (u16*)d_out);
}
```

```cpp
#include <hip/hip_runtime.h>
#include <hip/hip_bf16.h>
#include <stdint.h>
#include <type_traits>

typedef short bf16x8 __attribute__((ext_vector_type(8)));
typedef short s16x4 __attribute__((ext_vector_type(4)));
typedef float f32x16 __attribute__((ext_vector_type(16)));
typedef float f32x4 __attribute__((ext_vector_type(4)));
typedef float f32x2 __attribute__((ext_vector_type(2)));
typedef unsigned u32x4 __attribute__((ext_vector_type(4)));
typedef unsigned u32x2 __attribute__((ext_vector_type(2)));
typedef __bf16 bf2 __attribute__((ext_vector_type(2)));
typedef unsigned short u16;

#define DI __device__ __forceinline__
#define MFMA(a, b, c) __builtin_amdgcn_mfma_f32_32x32x16_bf16((a), (b), (c), 0, 0, 0)
#define LDSP __attribute__((address_space(3)))
#define GLBP __attribute__((address_space(1)))

constexpr int SEQ = 2048, DM = 448, NH = 16, DH = 28, N3 = 1344;
constexpr float QSCALE = 0.18898223650461362f * 1.4426950408889634f;

constexpr unsigned WS_Q = 0;
constexpr unsigned WS_K = 4u << 20;
constexpr unsigned WS_V = 8u << 20;
constexpr unsigned WS_O = 12u << 20;
constexpr unsigned WS_G = 16u << 20;
constexpr unsigned WS_W = 17u << 20;
constexpr unsigned WS_C = 19u << 20;
constexpr unsigned F1B = WS_C + 4096, F1A = WS_C + 8192, F2A = WS_C + 12288, F2B = WS_C + 16384;
constexpr unsigned QK_BR = 64 * 56 * 512;

DI unsigned pack2(float lo, float hi) {
  f32x2 v = {lo, hi};
  return __builtin_bit_cast(unsigned, __builtin_convertvector(v, bf2));
}
DI bf16x8 as_frag(u32x4 v) { return __builtin_bit_cast(bf16x8, v); }
DI float bf_lo(unsigned v) { return __uint_as_float(v << 16); }
DI float bf_hi(unsigned v) { return __uint_as_float(v & 0xffff0000u); }
DI float swap_max(float v) {
  auto rr = __builtin_amdgcn_permlane32_swap(__float_as_uint(v), __float_as_uint(v), false, false);
  return fmaxf(__uint_as_float(rr[0]), __uint_as_float(rr[1]));
}
DI s16x4 tr_read(const char LDSP* p) {
  return __builtin_amdgcn_ds_read_tr16_b64_v4i16((s16x4 LDSP*)p);
}
DI bf16x8 cat(s16x4 a, s16x4 b) { return __builtin_shufflevector(a, b, 0, 1, 2, 3, 4, 5, 6, 7); }
template <int S>
DI u32x4 pack_step(const f32x16& x, float sc) {
  u32x4 p;
  p[0] = pack2(x[8 * S + 0] * sc, x[8 * S + 1] * sc);
  p[1] = pack2(x[8 * S + 2] * sc, x[8 * S + 3] * sc);
  p[2] = pack2(x[8 * S + 4] * sc, x[8 * S + 5] * sc);
  p[3] = pack2(x[8 * S + 6] * sc, x[8 * S + 7] * sc);
  return p;
}
template <int S>
DI u32x4 pack_step1(const f32x16& x) {
  u32x4 p;
  p[0] = pack2(x[8 * S + 0], x[8 * S + 1]);
  p[1] = pack2(x[8 * S + 2], x[8 * S + 3]);
  p[2] = pack2(x[8 * S + 4], x[8 * S + 5]);
  p[3] = pack2(x[8 * S + 6], x[8 * S + 7]);
  return p;
}

#define RAWBAR() do { asm volatile("" ::: "memory"); __builtin_amdgcn_s_barrier(); asm volatile("" ::: "memory"); } while (0)
#define WAITV(n) ((((n) & 15) | (((n) >> 4) << 14)) | 0x0F70)
#define WAITVL(n) ((((n) & 15) | (((n) >> 4) << 14)) | 0x0070)
DI u32x4 widen(unsigned a0, unsigned a1, unsigned b0, unsigned b1) {
  auto r0 = __builtin_amdgcn_permlane32_swap(a0, b0, false, false);
  auto r1 = __builtin_amdgcn_permlane32_swap(a1, b1, false, false);
  return u32x4{r0[0], r1[0], r0[1], r1[1]};
}

typedef __amdgpu_buffer_rsrc_t rsrc_t;
typedef unsigned v4u_t __attribute__((ext_vector_type(4)));
typedef unsigned long long u64;
typedef GLBP u64 gu64;
DI u32x4 bload(rsrc_t rs, unsigned voff, unsigned soff) {
  return __builtin_bit_cast(u32x4, __builtin_amdgcn_raw_buffer_load_b128(rs, voff, soff, 16));
}
DI void bstore(u32x4 v, rsrc_t rs, unsigned voff, unsigned soff) {
  __builtin_amdgcn_raw_buffer_store_b128(__builtin_bit_cast(v4u_t, v), rs, voff + soff, 0, 16);
}
DI void set_flag(char* ws, unsigned off, u64 tag) {
  __hip_atomic_store((gu64*)(ws + off), tag, __ATOMIC_RELAXED, __HIP_MEMORY_SCOPE_AGENT);
}
DI u64 read_tag(const char* ws) {
  u64 e;
  asm volatile("s_load_dwordx2 %0, %1, 0x0\n\ts_waitcnt lgkmcnt(0)" : "=s"(e) : "s"(ws + WS_C) : "memory");
  return e + 1;
}
DI void wait_flags256(char* ws, unsigned off, u64 tag, int lane) {
  gu64* f = (gu64*)(ws + off) + lane;
  for (unsigned spins = 0; spins < (1u << 20); ++spins) {
    bool ok = true;
#pragma unroll
    for (int k = 0; k < 4; ++k) ok &= __hip_atomic_load(f + 64 * k, __ATOMIC_RELAXED, __HIP_MEMORY_SCOPE_AGENT) == tag;
    if (__all(ok)) break;
    __builtin_amdgcn_s_sleep(2);
  }
  asm volatile("" ::: "memory");
}
DI void wait_flags16(char* ws, unsigned off, u64 tag, int lane) {
  gu64* f = (gu64*)(ws + off) + (lane & 15);
  for (unsigned spins = 0; spins < (1u << 20); ++spins) {
    if (__all(__hip_atomic_load(f, __ATOMIC_RELAXED, __HIP_MEMORY_SCOPE_AGENT) == tag)) break;
    __builtin_amdgcn_s_sleep(1);
  }
  asm volatile("" ::: "memory");
}

constexpr int P1_WSLOT = 64 * 192;
constexpr int P1_SLOT = 16384 + P1_WSLOT;
constexpr int P1_CTL = 5 * P1_SLOT + P1_WSLOT;
constexpr int P1_SINK = P1_CTL + 64;
constexpr int LDS_BYTES = P1_SINK + 256;

constexpr unsigned DROP = 0x80000000u;
template <bool V>
DI void p1_pack(const f32x16& a, int br, int ct, int tile, int lane, u32x4* sv, unsigned* so) {
  const int type = ct / 14, cbase = (ct % 14) * 32;
  if (!V) {
    const float sc = type == 0 ? QSCALE : 1.f;
    const unsigned o = (type == 0 ? WS_Q : WS_K) + br * QK_BR + (tile * 56 + (cbase >> 3)) * 512 + lane * 16;
#pragma unroll
    for (int g = 0; g < 2; ++g) {
      sv[g] = widen(pack2(a[8 * g] * sc, a[8 * g + 1] * sc), pack2(a[8 * g + 2] * sc, a[8 * g + 3] * sc),
                    pack2(a[8 * g + 4] * sc, a[8 * g + 5] * sc), pack2(a[8 * g + 6] * sc, a[8 * g + 7] * sc));
      so[g] = o + g * 1024;
    }
  } else {
    const unsigned f = cbase + (lane & 31), head = f / 28, d = f % 28;
    const unsigned o = WS_V + ((br * 16 * 64 + tile) * 2) * 1024 + (head * 64 * 2 * 64 + d + (lane & 32)) * 16;
    sv[0] = pack_step1<0>(a); so[0] = o;
    sv[1] = pack_step1<1>(a); so[1] = o + 1024;
    const unsigned c = d == 0 ? 0x3F803F80u : 0u;
    sv[2] = u32x4{c, c, c, c}; so[2] = d < 4 ? o + 28 * 16 : DROP;
    sv[3] = sv[2]; so[3] = d < 4 ? o + 28 * 16 + 1024 : DROP;
  }
}

DI void warm_l2(const u16* x, const u16* WA, const u16* WB, char LDSP* smem) {
  const int b = blockIdx.x, xcd = b & 7;
  const unsigned g = ((b >> 3) - 28) * 512 + threadIdx.x;
#pragma unroll
  for (int e = 0; e < 4; ++e) {
    const unsigned i = e * 2048 + g;
    const char* p;
    if (i < 1024) {
      p = (const char*)x + ((xcd & 3) * 512 + (i >> 1)) * 896 + 640 + (i & 1) * 128;
    } else {
      const unsigned idx = i - 1024, row = idx / 12, c = idx % 12;
      unsigned off = (row < 128 ? row + 320 : row - 128) * 2688 + (xcd >> 2) * 1280 + c * 128;
      off = off < (unsigned)(DM * N3 * 2 - 4) ? off : 0u;
      p = (const char*)(row < 128 ? WB : WA) + off;
    }
    __builtin_amdgcn_global_load_lds((const GLBP void*)p, (LDSP void*)(smem + P1_SINK), 4, 0, 0);
  }
}

DI void lds_arrive(char LDSP* smem, int idx, int last, char* ws, unsigned flag_off, u64 tag, int lane) {
  if (lane == 0) {
    if (__hip_atomic_fetch_add((int LDSP*)(smem + P1_CTL) + idx, 1, __ATOMIC_RELAXED, __HIP_MEMORY_SCOPE_WORKGROUP) == last) set_flag(ws, flag_off, tag);
  }
}

DI void p1_first(const u16* __restrict__ x, const u16* __restrict__ WA, const u16* __restrict__ WB, int mt, int nt, char LDSP* smem) {
  const int lane = threadIdx.x & 63;
  const int w = __builtin_amdgcn_readfirstlane(threadIdx.x >> 6);
  const int wi0 = w < 4 ? w : 2 * w - 4;
  const u16* xs[2];
  int wso[2];
#pragma unroll
  for (int i = 0; i < 2; ++i) {
    int lr = (w * 2 + i) * 8 + (lane >> 3);
    int logical = (lane & 7) ^ ((lr >> 1) & 7);
    xs[i] = x + (mt * 128 + lr) * DM + logical * 8;
    const int id = (wi0 + i) * 64 + lane;
    wso[i] = (id / 12) * N3 + nt * 96 + (id % 12) * 8;
  }
#pragma unroll
  for (int c = 0; c < 6; ++c) {
    if (c < 5) {
#pragma unroll
      for (int i = 0; i < 2; ++i)
        __builtin_amdgcn_global_load_lds((const GLBP void*)(xs[i] + c * 64), (LDSP void*)(smem + c * P1_SLOT + (w * 2 + i) * 1024), 16, 0, 0);
    }
    const u16* Wc = c < 5 ? WB + c * 64 * N3 : WA + 6 * 64 * N3;
    char LDSP* dst = smem + (c < 5 ? c * P1_SLOT + 16384 : 5 * P1_SLOT) + wi0 * 1024;
    __builtin_amdgcn_global_load_lds((const GLBP void*)(Wc + wso[0]), (LDSP void*)dst, 16, 0, 0);
    if (w >= 4) __builtin_amdgcn_global_load_lds((const GLBP void*)(Wc + wso[1]), (LDSP void*)(dst + 1024), 16, 0, 0);
  }
}

template <int NT, int NV>
DI void p1_body(const u16* __restrict__ x, const u16* __restrict__ WA, const u16* __restrict__ WB, char* __restrict__ ws, rsrc_t rs,
                int mt, int nt, char LDSP* smem, u64& tag, u32x4 (&sv)[8], unsigned (&so)[8]) {
  const int tid = threadIdx.x, lane = tid & 63;
  const int w = __builtin_amdgcn_readfirstlane(tid >> 6);
  const int msub = w & 3;
  const int j0 = NT == 2 ? 0 : 2;
  const int r = lane & 31, h = lane >> 5;
  constexpr int NS = 2 * (NT - NV) + 4 * NV;
  constexpr int NW = NT == 2 ? 1 : 2;
  const int wi0 = NT == 2 ? w : 2 * w - 4;
  const u16* xs[2];
  int wso[2];
#pragma unroll
  for (int i = 0; i < 2; ++i) {
    int lr = (w * 2 + i) * 8 + (lane >> 3);
    int logical = (lane & 7) ^ ((lr >> 1) & 7);
    xs[i] = x + (mt * 128 + lr) * DM + logical * 8;
    const int id = (wi0 + i) * 64 + lane;
    wso[i] = (id / 12) * N3 + nt * 96 + (id % 12) * 8;
  }
  auto wbase = [&](int s) {
    return s < 7 ? (s % 5) * P1_SLOT + 16384 : (s < 13 ? (2 + (s - 7) / 2) * P1_SLOT + ((s - 7) & 1) * P1_WSLOT : 5 * P1_SLOT);
  };
  auto issue_x = [&](int c, int i) {
    __builtin_amdgcn_global_load_lds((const GLBP void*)(xs[i] + c * 64), (LDSP void*)(smem + (c % 5) * P1_SLOT + (w * 2 + i) * 1024), 16, 0, 0);
  };
  auto issue_w = [&](int s) {
#pragma unroll
    for (int i = 0; i < NW; ++i)
      __builtin_amdgcn_global_load_lds((const GLBP void*)((s < 7 ? WB : WA) + wso[i] + (s % 7) * 64 * N3),
                                       (LDSP void*)(smem + wbase(s) + (wi0 + i) * 1024), 16, 0, 0);
  };
  int xoff[4];
  {
    int lr = 32 * msub + r, sw = (lr >> 1) & 7;
#pragma unroll
    for (int t = 0; t < 4; ++t) xoff[t] = lr * 128 + (((2 * t + h) ^ sw) << 4);
  }
  const int q4 = (lane & 15) >> 2, p4 = lane & 3, g16 = (lane >> 4) & 1;
  const int woff = (8 * h + q4) * 192 + 64 * j0 + 32 * g16 + 8 * p4;

  f32x16 acc[NT] = {};
  bf16x8 xf[7][4];
  s16x4 wl[2][4][NT], wh[2][4][NT];
  auto readf = [&](int s, int m) {
    const int t = m / NT, j = m % NT;
    if (j == 0 && s < 7) xf[s][t] = *(const bf16x8 LDSP*)(smem + (s % 5) * P1_SLOT + xoff[t]);
    const char LDSP* wp = smem + woff + wbase(s) + t * 16 * 192 + j * 64;
    wl[s & 1][t][j] = tr_read(wp);
    wh[s & 1][t][j] = tr_read(wp + 4 * 192);
  };
  tag = read_tag(ws);
  if (tid < 8) ((int LDSP*)(smem + P1_CTL))[tid] = 0;
  __builtin_amdgcn_s_waitcnt(WAITVL(4 * (NW + 2) + NW));
  RAWBAR();
#pragma unroll
  for (int m = 0; m < 4 * NT; ++m) readf(0, m);
#pragma unroll
  for (int s = 0; s < 14; ++s) {
    if (s < 7 || s == 8) {
      if (s < 3) __builtin_amdgcn_s_waitcnt(WAITVL(3 * (NW + 2) + NW));
      else if (s == 3) __builtin_amdgcn_s_waitcnt(WAITVL(2 * (NW + 2) + 3 * NW));
      else if (s == 4) __builtin_amdgcn_s_waitcnt(WAITVL(NW + 2 + 4 * NW));
      else if (s == 5) __builtin_amdgcn_s_waitcnt(WAITVL(6 * NW));
      else if (s == 6) __builtin_amdgcn_s_waitcnt(WAITVL(4 * NW));
      else __builtin_amdgcn_s_waitcnt(WAITVL(NS));
      RAWBAR();
    }
    if (s == 12) {
      __builtin_amdgcn_s_waitcnt(WAITV(0));
      asm volatile("" ::: "memory");
      lds_arrive(smem, 4, 7, ws, F1B + (nt * 16 + mt) * 8, tag, lane);
    }
    __builtin_amdgcn_sched_barrier(0);
#pragma unroll
    for (int m = 0; m < 4 * NT; ++m) {
      const int t = m / NT, j = m % NT;
      const bf16x8 wfr = cat(wl[s & 1][t][j], wh[s & 1][t][j]);
      if (j < NT - NV) acc[j] = MFMA(wfr, xf[s % 7][t], acc[j]);
      else acc[j] = MFMA(xf[s % 7][t], wfr, acc[j]);
      __builtin_amdgcn_sched_barrier(0);
      if (s < 13) readf(s + 1, m);
      if (j == 0) {
        if (s < 2) { if (t < 2) issue_x(s + 5, t); else if (t == 2) issue_w(s + 5); }
        else if (s < 5 && t < 2) issue_w(7 + 2 * (s - 2) + t);
      }
      __builtin_amdgcn_sched_barrier(0);
    }
    if (s == 6 || s == 13) {
#pragma unroll
      for (int e = 0; e < 8; ++e) so[e] = DROP;
#pragma unroll
      for (int j = 0; j < NT; ++j) {
        if (j < NT - NV) p1_pack<false>(acc[j], s == 6, nt * 3 + j0 + j, mt * 4 + msub, lane, sv + 4 * j, so + 4 * j);
        else p1_pack<true>(acc[j], s == 6, nt * 3 + j0 + j, mt * 4 + msub, lane, sv + 4 * j, so + 4 * j);
        if (s == 6) {
#pragma unroll
          for (int e = 0; e < (j < NT - NV ? 2 : 4); ++e) bstore(sv[4 * j + e], rs, so[4 * j + e], 0);
        }
        acc[j] = f32x16{};
      }
      __builtin_amdgcn_sched_barrier(0);
    }
  }
}

DI void p1_spare(const u16* __restrict__ x, const u16* __restrict__ gate_w, const u16* __restrict__ WoA, const u16* __restrict__ WoB,
                 char* __restrict__ ws, rsrc_t rs, int sp, char LDSP* smem, u64& tag) {
  const int tid = threadIdx.x, lane = tid & 63, h = lane >> 5;
  const int w = __builtin_amdgcn_readfirstlane(tid >> 6);
  {
    const u16* Wo = (sp & 1) ? WoB : WoA;
    const int col0 = 56 * (sp >> 2) + 32 * ((sp >> 1) & 1);
#pragma unroll
    for (int e = 0; e < 4; ++e) {
      const int id = (w * 4 + e) * 64 + lane;
      int idx = (id >> 2) * DM + col0 + (id & 3) * 8;
      idx = idx < DM * DM - 8 ? idx : DM * DM - 8;
      __builtin_amdgcn_global_load_lds((const GLBP void*)(Wo + idx), (LDSP void*)(smem + (w * 4 + e) * 1024), 16, 0, 0);
    }
  }
  tag = read_tag(ws);
  if (tid < 8) ((int LDSP*)(smem + P1_CTL))[tid] = 0;
  __builtin_amdgcn_s_waitcnt(0xC07F);
  RAWBAR();
  if (tid == 0) set_flag(ws, F1B + ((14 + (sp & 1)) * 16 + (sp >> 1)) * 8, tag);
  const int row = sp * 64 + (tid >> 3), part = tid & 7;
  const u32x4* xp = (const u32x4*)(x + row * DM) + part;
  const u32x4* gp = (const u32x4*)(gate_w) + part;
  float acc = 0.f;
#pragma unroll
  for (int i = 0; i < 7; ++i) {
    u32x4 a = xp[i * 8], bb = gp[i * 8];
#pragma unroll
    for (int e = 0; e < 4; ++e) {
      acc = fmaf(bf_lo(a[e]), bf_lo(bb[e]), acc);
      acc = fmaf(bf_hi(a[e]), bf_hi(bb[e]), acc);
    }
  }
  acc += __shfl_xor(acc, 1);
  acc += __shfl_xor(acc, 2);
  acc += __shfl_xor(acc, 4);
  if (part == 0)
    __hip_atomic_store((GLBP float*)(ws + WS_G) + row, 1.f / (1.f + __expf(-acc)), __ATOMIC_RELAXED, __HIP_MEMORY_SCOPE_AGENT);
  asm volatile("s_waitcnt vmcnt(0)" ::: "memory");
  __syncthreads();
  {
    const int q4 = (lane & 15) >> 2, p4 = lane & 3, g16 = (lane >> 4) & 1;
#pragma unroll
    for (int e = 0; e < 4; ++e) {
      const int f = w * 4 + e;
      const char LDSP* p = smem + (28 * (f >> 1) + 16 * (f & 1) + 4 * h + q4) * 64 + 32 * g16 + 8 * p4;
      bstore(__builtin_bit_cast(u32x4, cat(tr_read(p), tr_read(p + 8 * 64))), rs, WS_W + (sp * 32 + f) * 1024 + lane * 16, 0);
    }
  }
}

constexpr unsigned long long lmask(int i, bool prev) {
  unsigned long long m = 0;
  for (int l = 0; l < 64; ++l) {
    int r = l & 31, h = l >> 5;
    int kr = (i & 3) + 8 * (i >> 2) + 4 * h;
    int diff = r - kr + (prev ? 32 : 0);
    if (diff >= 0 && diff <= 14 && (diff & 1) == 0) m |= 1ull << l;
  }
  return m;
}
DI float sel_mask(float val, float other, unsigned long long mask) {
  return __builtin_amdgcn_inverse_ballot_w64(mask) ? val : other;
}
template <int ODD>
DI void set_foreign(u32x4& f0, u32x4& f1, int h, unsigned a, unsigned b) {
  if (ODD) { if (!h) { f0[0] = a; f0[1] = b; } }
  else { if (h) { f1[2] = a; f1[3] = b; } }
}
DI float gload(char* ws, int idx) { return __hip_atomic_load((GLBP float*)(ws + WS_G) + idx, __ATOMIC_RELAXED, __HIP_MEMORY_SCOPE_AGENT); }

template <int ODD>
DI void p2_body(char* __restrict__ ws, u16* __restrict__ out, rsrc_t rs, char LDSP* smem, const u64 tag, const int fidx,
                const u32x4 (&sv)[8], const unsigned (&so)[8]) {
  f32x4 LDSP* part = (f32x4 LDSP*)smem;
  float LDSP* cw = (float LDSP*)(smem + 131072);
  volatile int LDSP* ctl = (volatile int LDSP*)(smem + P1_CTL);
  const int tid = threadIdx.x;
  int lane = tid & 63;
  const int w = __builtin_amdgcn_readfirstlane(tid >> 6);
  int r = lane & 31, h = lane >> 5;
  const int b = blockIdx.x;
  const int head = (b & 7) * 2 + ODD, qb = b >> 4;
  const int oqb = (b & 7) * 2 + (b >> 7), oqh = (b >> 3) & 1, nb = (b >> 4) & 7;
  const unsigned p0 = (7 * head) >> 1;
  unsigned l16 = lane * 16;
  const unsigned qoff = WS_Q + QK_BR + (qb * 4 * 56 + p0) * 512;
  const unsigned koff = WS_K + QK_BR + (w * 8 * 56 + p0) * 512;
  const unsigned voff = WS_V + ((16 + head) * 64 + w * 8) * 2048;
  __syncthreads();
  if (w < 4) {
#pragma unroll
    for (int e = 0; e < 8; ++e) {
      ((u32x4 LDSP*)(smem + 65536))[(w * 8 + e) * 64 + lane] = sv[e];
      ((unsigned LDSP*)(smem + 65536 + 32768))[(w * 8 + e) * 64 + lane] = so[e];
    }
  }
  if (w == 0) {
    const int sel = lane >> 4;
    const int c0 = sel < 2 ? 448 + 8 * (int)p0 + (sel & 1) * 31 : 896 + 28 * head + (sel & 1) * 27;
    gu64* f = (gu64*)(ws + F1B) + (c0 / 96) * 16 + (lane & 15);
    gu64* fq = (gu64*)(ws + F1B) + ((8 * (int)p0 + (lane & 1) * 31) / 96) * 16 + qb;
    for (unsigned spins = 0; spins < (1u << 20); ++spins) {
      bool ok = __hip_atomic_load(f, __ATOMIC_RELAXED, __HIP_MEMORY_SCOPE_AGENT) == tag;
      ok &= __hip_atomic_load(fq, __ATOMIC_RELAXED, __HIP_MEMORY_SCOPE_AGENT) == tag;
      if (__all(ok)) break;
      __builtin_amdgcn_s_sleep(1);
    }
    asm volatile("" ::: "memory");
  }
  __syncthreads();
  u32x4 q[4][2];
  u32x4 kf[4][2], vf[4][2];
  auto loadkv = [&](int t) {
    kf[t & 3][0] = bload(rs, l16, koff + t * 28672);
    kf[t & 3][1] = bload(rs, l16 + 1024, koff + t * 28672);
    vf[t & 3][0] = bload(rs, l16, voff + t * 2048);
    vf[t & 3][1] = bload(rs, l16 + 1024, voff + t * 2048);
  };
  auto store_a = [&]() {
#pragma unroll
    for (int e = 0; e < 4; ++e) bstore(sv[e], rs, so[e], 0);
#pragma unroll
    for (int e = 0; e < 8; ++e)
      bstore(((u32x4 LDSP*)(smem + 65536))[((w - 4) * 8 + e) * 64 + lane], rs, ((unsigned LDSP*)(smem + 65536 + 32768))[((w - 4) * 8 + e) * 64 + lane], 0);
  };
  if (w >= 4) {
    store_a();
    asm volatile("s_waitcnt vmcnt(0)" ::: "memory");
    lds_arrive(smem, 2, 3, ws, F1A + fidx * 8, tag, lane);
    if (b == 0 && w == 5) {
      wait_flags256(ws, F1B, tag, lane);
      if (lane == 0) set_flag(ws, WS_C, tag);
    }
    __builtin_amdgcn_s_setprio(1);
  }
  kf[0][0] = bload(rs, l16, koff);
  kf[0][1] = bload(rs, l16 + 1024, koff);
#pragma unroll
  for (int i = 0; i < 4; ++i) { q[i][0] = bload(rs, l16, qoff + i * 28672); q[i][1] = bload(rs, l16 + 1024, qoff + i * 28672); }
  vf[0][0] = bload(rs, l16, voff);
  vf[0][1] = bload(rs, l16 + 1024, voff);
  loadkv(1);
  loadkv(2);
  __builtin_amdgcn_sched_barrier(0);

  auto fold_shift = [&](int i, float m) {
    m = swap_max(m);
    if (!h) cw[(i * 32 + r) * 8 + w] = m;
    const float c = -m;
    unsigned p01 = pack2(c, 0.f);
    float r1 = c - bf_lo(p01);
    unsigned p1 = pack2(r1, 0.f);
    float r2 = r1 - bf_lo(p1);
    unsigned p2 = pack2(r2, 0.f);
    set_foreign<ODD>(q[i][0], q[i][1], h, (p01 & 0xffffu) | (p1 << 16), p2 & 0xffffu);
  };
  const int ri = w >> 1, rs1 = w & 1;
  f32x4 a0, a1;
  float l;
  auto write_part = [&](f32x16 (&o)[4]) {
#pragma unroll
    for (int i = 0; i < 4; ++i)
#pragma unroll
      for (int g = 0; g < 4; ++g)
        part[((w * 4 + i) * 4 + g) * 64 + lane] = f32x4{o[i][4 * g], o[i][4 * g + 1], o[i][4 * g + 2], o[i][4 * g + 3]};
  };
  auto reduce = [&]() {
    const f32x4 c0 = *(f32x4 LDSP*)(cw + (ri * 32 + r) * 8), c1 = *(f32x4 LDSP*)(cw + (ri * 32 + r) * 8 + 4);
    const float cm = fmaxf(fmaxf(fmaxf(c0[0], c0[1]), fmaxf(c0[2], c0[3])), fmaxf(fmaxf(c1[0], c1[1]), fmaxf(c1[2], c1[3])));
    a0 = f32x4{}; a1 = f32x4{}; l = 0.f;
#pragma unroll
    for (int ww = 0; ww < 8; ++ww) {
      const float wt = __builtin_amdgcn_exp2f((ww < 4 ? c0[ww & 3] : c1[ww & 3]) - cm);
      a0 += part[((ww * 4 + ri) * 4 + 2 * rs1) * 64 + lane] * wt;
      a1 += part[((ww * 4 + ri) * 4 + 2 * rs1 + 1) * 64 + lane] * wt;
      l += ((float LDSP*)(part + ((ww * 4 + ri) * 4 + 3) * 64 + 32 + r))[0] * wt;
    }
  };
  const int qt = w & 1, kq = w >> 1;
  u32x4 wfr[2][2][8];
  auto loadW = [&]() {
#pragma unroll
    for (int br = 0; br < 2; ++br)
#pragma unroll
      for (int nt = 0; nt < 2; ++nt) {
        const unsigned wo = WS_W + (((nb * 2 + nt) * 32 + br * 16 + 4 * kq) * 2) * 1024;
#pragma unroll
        for (int e = 0; e < 8; ++e) wfr[br][nt][e] = bload(rs, l16 + (e & 3) * 1024, wo + (e >> 2) * 4096);
      }
  };

#pragma unroll
  for (int i = 0; i < 4; ++i) {
    set_foreign<ODD>(q[i][0], q[i][1], h, 0u, 0u);
    f32x16 s = MFMA(as_frag(kf[0][0]), as_frag(q[i][0]), f32x16{});
    s = MFMA(as_frag(kf[0][1]), as_frag(q[i][1]), s);
    fold_shift(i, fmaxf(fmaxf(s[0], s[1]), s[2]));
  }
  __builtin_amdgcn_s_setprio(0);
  {
    f32x16 o[4] = {};
    set_foreign<ODD>(kf[0][0], kf[0][1], h, 0x3F803F80u, 0x00003F80u);
    f32x16 sa = MFMA(as_frag(kf[0][0]), as_frag(q[0][0]), f32x16{});
    sa = MFMA(as_frag(kf[0][1]), as_frag(q[0][1]), sa);
    f32x16 sb;
    u32x4 pa;
    u32x4 pbp;
    float e3[4];
#define SB __builtin_amdgcn_sched_barrier(0)
#define EXPQ(S, g) { S[4 * g] = __builtin_amdgcn_exp2f(S[4 * g]); S[4 * g + 1] = __builtin_amdgcn_exp2f(S[4 * g + 1]); \
                     S[4 * g + 2] = __builtin_amdgcn_exp2f(S[4 * g + 2]); S[4 * g + 3] = __builtin_amdgcn_exp2f(S[4 * g + 3]); }
    auto step = [&](auto nc, f32x16& sc, f32x16& sn) {
      constexpr int n = decltype(nc)::value;
      constexpr int t = n >> 2, i = n & 3;
      constexpr int t1 = ((n + 1) >> 2) & 3, i1 = (n + 1) & 3;
      constexpr int tp = n > 0 ? ((n - 1) >> 2) & 3 : 0, ip = n > 0 ? (n - 1) & 3 : 0;
      if (i == 1 && t + 3 < 8) loadkv(t + 3);
      if (i == 3 && n + 1 < 32) set_foreign<ODD>(kf[t1][0], kf[t1][1], h, 0x3F803F80u, 0x00003F80u);
      SB;
      if (n + 1 < 32) sn = MFMA(as_frag(kf[t1][0]), as_frag(q[i1][0]), f32x16{});
      SB;
      if (n > 0) { pbp[2] = pack2(e3[0], e3[1]); pbp[3] = pack2(e3[2], e3[3]); }
      SB;
      EXPQ(sc, 0);
      SB;
      if (n + 1 < 32) sn = MFMA(as_frag(kf[t1][1]), as_frag(q[i1][1]), sn);
      SB;
      pa[0] = pack2(sc[0], sc[1]); pa[1] = pack2(sc[2], sc[3]);
      SB;
      EXPQ(sc, 1);
      SB;
      if (n > 0) o[ip] = MFMA(as_frag(vf[tp][1]), as_frag(pbp), o[ip]);
      SB;
      pa[2] = pack2(sc[4], sc[5]); pa[3] = pack2(sc[6], sc[7]);
      SB;
      EXPQ(sc, 2);
      SB;
      o[i] = MFMA(as_frag(vf[t & 3][0]), as_frag(pa), o[i]);
      SB;
      pbp[0] = pack2(sc[8], sc[9]); pbp[1] = pack2(sc[10], sc[11]);
      SB;
      EXPQ(sc, 3);
      e3[0] = sc[12]; e3[1] = sc[13]; e3[2] = sc[14]; e3[3] = sc[15];
      SB;
    };
#define STEP2(n) step(std::integral_constant<int, n>{}, sa, sb); step(std::integral_constant<int, n + 1>{}, sb, sa);
    STEP2(0) STEP2(2) STEP2(4) STEP2(6) STEP2(8) STEP2(10) STEP2(12) STEP2(14)
    STEP2(16) STEP2(18) STEP2(20) STEP2(22) STEP2(24) STEP2(26) STEP2(28) STEP2(30)
#undef STEP2
    pbp[2] = pack2(e3[0], e3[1]); pbp[3] = pack2(e3[2], e3[3]);
    o[3] = MFMA(as_frag(vf[3][1]), as_frag(pbp), o[3]);
    asm volatile("" : "+v"(lane));
    r = lane & 31; h = lane >> 5; l16 = lane * 16;
    write_part(o);
  }
  SB;
  if (w < 4) {
    {
      gu64* f = (gu64*)(ws + F1A) + (qb > 0 ? qb - 1 : 0) * 16 + (lane & 31) - ((lane & 15) >= 14 ? 2 : 0);
      for (unsigned spins = 0; spins < (1u << 20); ++spins) {
        if (__all(__hip_atomic_load(f, __ATOMIC_RELAXED, __HIP_MEMORY_SCOPE_AGENT) == tag)) break;
        __builtin_amdgcn_s_sleep(2);
      }
      asm volatile("" ::: "memory");
    const int ltile = qb * 4 + (w & 3);
    const unsigned lqo = WS_Q + (ltile * 56 + p0) * 512, lko = WS_K + (ltile * 56 + p0) * 512, lvo = WS_V + (head * 64 + ltile) * 2048;
    const unsigned pd = ltile > 0 ? 1 : 0;
    u32x4 lq0 = bload(rs, l16, lqo), lq1 = bload(rs, l16 + 1024, lqo);
    u32x4 lk0 = bload(rs, l16, lko), lk1 = bload(rs, l16 + 1024, lko);
    u32x4 pk0 = bload(rs, l16, lko - pd * 28672), pk1 = bload(rs, l16 + 1024, lko - pd * 28672);
    u32x4 lv0 = bload(rs, l16, lvo), lv1 = bload(rs, l16 + 1024, lvo);
    u32x4 pv1 = bload(rs, l16 + 1024, lvo - pd * 2048);
    set_foreign<ODD>(lq0, lq1, h, 0u, 0u);
    f32x16 sp = MFMA(as_frag(pk0), as_frag(lq0), f32x16{});
    sp = MFMA(as_frag(pk1), as_frag(lq1), sp);
    f32x16 sc = MFMA(as_frag(lk0), as_frag(lq0), f32x16{});
    sc = MFMA(as_frag(lk1), as_frag(lq1), sc);
    const bool first = (ltile == 0);
    const float NEG = -3.0e38f;
    float m = NEG;
#define LM_C(i) { sc[i] = sel_mask(sc[i], NEG, lmask(i, false)); m = fmaxf(m, sc[i]); }
#define LM_P(i) { sp[i] = sel_mask(sp[i], NEG, first ? 0ull : lmask(i, true)); m = fmaxf(m, sp[i]); }
    LM_C(0) LM_C(1) LM_C(2) LM_C(3) LM_C(4) LM_C(5) LM_C(6) LM_C(7)
    LM_C(8) LM_C(9) LM_C(10) LM_C(11) LM_C(12) LM_C(13) LM_C(14) LM_C(15)
    LM_P(8) LM_P(9) LM_P(10) LM_P(11) LM_P(12) LM_P(13) LM_P(14) LM_P(15)
#undef LM_C
#undef LM_P
    m = swap_max(m);
#pragma unroll
    for (int i = 0; i < 16; ++i) sc[i] = __builtin_amdgcn_exp2f(sc[i] - m);
#pragma unroll
    for (int i = 8; i < 16; ++i) sp[i] = __builtin_amdgcn_exp2f(sp[i] - m);
    f32x16 o = MFMA(as_frag(pv1), as_frag(pack_step1<1>(sp)), f32x16{});
    o = MFMA(as_frag(lv0), as_frag(pack_step1<0>(sc)), o);
    o = MFMA(as_frag(lv1), as_frag(pack_step1<1>(sc)), o);
    float ll = __shfl(o[12], 32 + r);
    float inv = 1.f / ll;
    u32x4 o0 = pack_step<0>(o, inv), o1 = pack_step<1>(o, inv);
    if (h) { o1[2] = 0; o1[3] = 0; }
    const unsigned oo = WS_O + ((ltile * 32 + head) * 2) * 1024;
    bstore(o0, rs, l16, oo);
    bstore(o1, rs, l16 + 1024, oo);
    asm volatile("s_waitcnt vmcnt(0)" ::: "memory");
    lds_arrive(smem, 1, 3, ws, F2A + (qb * 16 + head) * 8, tag, lane);
    }
    if (w == 0) {
      gu64* f = (gu64*)(ws + F1A) + ((lane & 31) >> 1) * 16 + 14 + (lane & 1);
      for (unsigned spins = 0; spins < (1u << 20); ++spins) {
        if (__all(__hip_atomic_load(f, __ATOMIC_RELAXED, __HIP_MEMORY_SCOPE_AGENT) == tag)) break;
        __builtin_amdgcn_s_sleep(2);
      }
      asm volatile("" ::: "memory");
    }
  }
  auto publish = [&]() {
    const float inv = 1.f / l;
    u32x4 ov;
    ov[0] = pack2(a0[0] * inv, a0[1] * inv);
    ov[1] = pack2(a0[2] * inv, a0[3] * inv);
    ov[2] = pack2(a1[0] * inv, a1[1] * inv);
    ov[3] = pack2(a1[2] * inv, a1[3] * inv);
    if (h && rs1) { ov[2] = 0; ov[3] = 0; }
    bstore(ov, rs, l16, WS_O + (((qb * 4 + ri) * 32 + 16 + head) * 2 + rs1) * 1024);
  };
  __syncthreads();
  reduce();
  publish();
  __builtin_amdgcn_sched_barrier(0);
  loadW();
  __builtin_amdgcn_sched_barrier(0);
  {
    float am = fmaxf(fmaxf(fmaxf(fabsf(a0[0]), fabsf(a0[1])), fmaxf(fabsf(a0[2]), fabsf(a0[3]))),
                     fmaxf(fmaxf(fabsf(a1[0]), fabsf(a1[1])), fmaxf(fabsf(a1[2]), fabsf(a1[3]))));
    float chk = (a0[0] + a0[1] + a0[2] + a0[3]) * 0.f + (a1[0] + a1[1] + a1[2] + a1[3]) * 0.f;
    bool bad = !(l < 1.0e30f) || !(am < 3.0e38f) || !(chk == 0.f);
    if (bad) ctl[0] = 1;
  }
  asm volatile("s_waitcnt vmcnt(32)" ::: "memory");
  __syncthreads();
  if (__builtin_amdgcn_readfirstlane(ctl[0])) {
#pragma unroll
    for (int i = 0; i < 4; ++i) {
      q[i][0] = bload(rs, l16, qoff + i * 28672); q[i][1] = bload(rs, l16 + 1024, qoff + i * 28672);
      set_foreign<ODD>(q[i][0], q[i][1], h, 0u, 0u);
    }
    float m[4] = {-3.0e38f, -3.0e38f, -3.0e38f, -3.0e38f};
#pragma unroll 1
    for (int t = 0; t < 8; ++t) {
      u32x4 k0 = bload(rs, l16, koff + t * 28672), k1 = bload(rs, l16 + 1024, koff + t * 28672);
#pragma unroll
      for (int i = 0; i < 4; ++i) {
        f32x16 s = MFMA(as_frag(k0), as_frag(q[i][0]), f32x16{});
        s = MFMA(as_frag(k1), as_frag(q[i][1]), s);
#pragma unroll
        for (int j = 0; j < 16; ++j) m[i] = fmaxf(m[i], s[j]);
      }
    }
#pragma unroll
    for (int i = 0; i < 4; ++i) fold_shift(i, m[i]);
    f32x16 o[4] = {};
#pragma unroll 1
    for (int t = 0; t < 8; ++t) {
      u32x4 k0 = bload(rs, l16, koff + t * 28672), k1 = bload(rs, l16 + 1024, koff + t * 28672);
      u32x4 v0 = bload(rs, l16, voff + t * 2048), v1 = bload(rs, l16 + 1024, voff + t * 2048);
      set_foreign<ODD>(k0, k1, h, 0x3F803F80u, 0x00003F80u);
#pragma unroll
      for (int i = 0; i < 4; ++i) {
        f32x16 s = MFMA(as_frag(k0), as_frag(q[i][0]), f32x16{});
        s = MFMA(as_frag(k1), as_frag(q[i][1]), s);
#pragma unroll
        for (int j = 0; j < 16; ++j) s[j] = __builtin_amdgcn_exp2f(s[j]);
        o[i] = MFMA(as_frag(v0), as_frag(pack_step1<0>(s)), o[i]);
        o[i] = MFMA(as_frag(v1), as_frag(pack_step1<1>(s)), o[i]);
      }
    }
    write_part(o);
    __syncthreads();
    reduce();
    publish();
    loadW();
    asm volatile("s_waitcnt vmcnt(0)" ::: "memory");
    __syncthreads();
  }
  const int oqt = oqb * 4 + oqh * 2 + qt;
  const unsigned ofo = WS_O + ((oqt * 32 + 4 * kq) * 2) * 1024;
  if (tid == 0) set_flag(ws, F2B + (qb * 16 + head) * 8, tag);
  const float gt = gload(ws, oqt * 32 + r);
  wait_flags16(ws, F2A + oqb * 128, tag, lane);
  {
    u32x4 ofr[8];
    f32x16 acc[2][2] = {};
#pragma unroll
    for (int e = 0; e < 8; ++e) ofr[e] = bload(rs, l16 + (e & 3) * 1024, ofo + (e >> 2) * 4096);
#pragma unroll
    for (int e = 0; e < 8; ++e)
#pragma unroll
      for (int nt = 0; nt < 2; ++nt) acc[0][nt] = MFMA(as_frag(wfr[0][nt][e]), as_frag(ofr[e]), acc[0][nt]);
    if (w == 0) wait_flags16(ws, F2B + oqb * 128, tag, lane);
    __syncthreads();
#pragma unroll
    for (int e = 0; e < 8; ++e) ofr[e] = bload(rs, l16 + (e & 3) * 1024, ofo + 32768 + (e >> 2) * 4096);
#pragma unroll
    for (int e = 0; e < 8; ++e)
#pragma unroll
      for (int nt = 0; nt < 2; ++nt) acc[1][nt] = MFMA(as_frag(wfr[1][nt][e]), as_frag(ofr[e]), acc[1][nt]);
#pragma unroll
    for (int nt = 0; nt < 2; ++nt)
#pragma unroll
      for (int g = 0; g < 4; ++g) {
        f32x4 v;
#pragma unroll
        for (int e = 0; e < 4; ++e) v[e] = acc[0][nt][4 * g + e] * gt + acc[1][nt][4 * g + e] * (1.f - gt);
        part[(((nt * 2 + qt) * 4 + kq) * 4 + g) * 64 + lane] = v;
      }
  }
  __syncthreads();
  {
    const int ft = w & 3, fg = w >> 2;
    f32x4 s0 = {}, s1 = {};
#pragma unroll
    for (int k = 0; k < 4; ++k) {
      s0 += part[((ft * 4 + k) * 4 + 2 * fg) * 64 + lane];
      s1 += part[((ft * 4 + k) * 4 + 2 * fg + 1) * 64 + lane];
    }
    const u32x4 ov = widen(pack2(s0[0], s0[1]), pack2(s0[2], s0[3]), pack2(s1[0], s1[1]), pack2(s1[2], s1[3]));
    const int col = 32 * (ft >> 1) + 16 * fg + 8 * h;
    if (col < 56) *(u32x4*)(out + (oqb * 128 + oqh * 64 + (ft & 1) * 32 + r) * DM + nb * 56 + col) = ov;
  }
}

__global__ __launch_bounds__(512, 2) void fused_block(const u16* __restrict__ x, const u16* __restrict__ WA, const u16* __restrict__ WB,
                                                      const u16* __restrict__ gate_w, const u16* __restrict__ WoA,
                                                      const u16* __restrict__ WoB, char* __restrict__ ws, u16* __restrict__ out) {
  extern __shared__ __attribute__((aligned(16))) char smem_[];
  char LDSP* smem = (char LDSP*)smem_;
  const rsrc_t rs = __builtin_amdgcn_make_buffer_rsrc(ws, 0, 32u << 20, 0x00020000);
  u64 tag;
  asm volatile("s_getpc_b64 %0" : "=s"(tag));
  __builtin_amdgcn_global_load_lds((const GLBP void*)((const char*)tag + (((blockIdx.x >> 3) * 8 + (threadIdx.x >> 6)) * 2 + (threadIdx.x & 1)) * 128),
                                   (LDSP void*)(smem + P1_SINK), 4, 0, 0);
  u32x4 sv[8];
  unsigned so[8];
  const int b = blockIdx.x, xcd = b & 7, j = b >> 3;
  if (__builtin_expect(j < 28, 1)) {
    const int mt = 4 * (xcd & 3) + (j & 3);
    const int nt = 7 * (xcd >> 2) + (j >> 2);
    p1_first(x, WA, WB, mt, nt, smem);
    if (__builtin_amdgcn_readfirstlane(threadIdx.x >> 8) == 0) {
      if (nt < 9) p1_body<2, 0>(x, WA, WB, ws, rs, mt, nt, smem, tag, sv, so);
      else if (nt == 9) p1_body<2, 1>(x, WA, WB, ws, rs, mt, nt, smem, tag, sv, so);
      else p1_body<2, 2>(x, WA, WB, ws, rs, mt, nt, smem, tag, sv, so);
    } else {
      if (nt < 9) p1_body<1, 0>(x, WA, WB, ws, rs, mt, nt, smem, tag, sv, so);
      else p1_body<1, 1>(x, WA, WB, ws, rs, mt, nt, smem, tag, sv, so);
    }
  } else {
    p1_spare(x, gate_w, WoA, WoB, ws, rs, xcd * 4 + j - 28, smem, tag);
    asm volatile("s_waitcnt vmcnt(0)" ::: "memory");
#pragma unroll
    for (int e = 0; e < 8; ++e) { sv[e] = u32x4{}; so[e] = DROP; }
  }
  const int sp = xcd * 4 + j - 28;
  const int fidx = j < 28 ? (4 * (xcd & 3) + (j & 3)) * 16 + 7 * (xcd >> 2) + (j >> 2) : (sp >> 1) * 16 + 14 + (sp & 1);
  if ((b >> 3) & 1) p2_body<1>(ws, out, rs, smem, tag, fidx, sv, so);
  else p2_body<0>(ws, out, rs, smem, tag, fidx, sv, so);
}

extern "C" void kernel_launch(void* const* d_in, const int* in_sizes, int n_in, void* d_out, int out_size, void* d_ws,
                              size_t ws_size, hipStream_t stream) {
  const u16* x = (const u16*)d_in[0];
  const u16* WA = (const u16*)d_in[1];
  const u16* WoA = (const u16*)d_in[2];
  const u16* WB = (const u16*)d_in[4];
  const u16* WoB = (const u16*)d_in[5];
  const u16* gw = (const u16*)d_in[6];
  (void)hipFuncSetAttribute((const void*)fused_block, hipFuncAttributeMaxDynamicSharedMemorySize, LDS_BYTES);
  fused_block<<<256, 512, LDS_BYTES, stream>>>(x, WA, WB, gw, WoA, WoB, (char*)d_ws, (u16*)d_out);
}
```

```cpp
#include <hip/hip_runtime.h>
#include <hip/hip_bf16.h>
#include <stdint.h>
#include <type_traits>

typedef short bf16x8 __attribute__((ext_vector_type(8)));
typedef short s16x4 __attribute__((ext_vector_type(4)));
typedef float f32x16 __attribute__((ext_vector_type(16)));
typedef float f32x4 __attribute__((ext_vector_type(4)));
typedef float f32x2 __attribute__((ext_vector_type(2)));
typedef unsigned u32x4 __attribute__((ext_vector_type(4)));
typedef unsigned u32x2 __attribute__((ext_vector_type(2)));
typedef __bf16 bf2 __attribute__((ext_vector_type(2)));
typedef unsigned short u16;

#define DI __device__ __forceinline__
#define MFMA(a, b, c) __builtin_amdgcn_mfma_f32_32x32x16_bf16((a), (b), (c), 0, 0, 0)
#define LDSP __attribute__((address_space(3)))
#define GLBP __attribute__((address_space(1)))

constexpr int SEQ = 2048, DM = 448, NH = 16, DH = 28, N3 = 1344;
constexpr float QSCALE = 0.18898223650461362f * 1.4426950408889634f;

constexpr unsigned WS_Q = 0;
constexpr unsigned WS_K = 4u << 20;
constexpr unsigned WS_V = 8u << 20;
constexpr unsigned WS_O = 12u << 20;
constexpr unsigned WS_G = 16u << 20;
constexpr unsigned WS_W = 17u << 20;
constexpr unsigned WS_C = 19u << 20;
constexpr unsigned F1B = WS_C + 4096, F1A = WS_C + 8192, F2A = WS_C + 12288, F2B = WS_C + 16384;
constexpr unsigned QK_BR = 64 * 56 * 512;

DI unsigned pack2(float lo, float hi) {
  f32x2 v = {lo, hi};
  return __builtin_bit_cast(unsigned, __builtin_convertvector(v, bf2));
}
DI bf16x8 as_frag(u32x4 v) { return __builtin_bit_cast(bf16x8, v); }
DI float bf_lo(unsigned v) { return __uint_as_float(v << 16); }
DI float bf_hi(unsigned v) { return __uint_as_float(v & 0xffff0000u); }
DI float swap_max(float v) {
  auto rr = __builtin_amdgcn_permlane32_swap(__float_as_uint(v), __float_as_uint(v), false, false);
  return fmaxf(__uint_as_float(rr[0]), __uint_as_float(rr[1]));
}
DI s16x4 tr_read(const char LDSP* p) {
  return __builtin_amdgcn_ds_read_tr16_b64_v4i16((s16x4 LDSP*)p);
}
DI bf16x8 cat(s16x4 a, s16x4 b) { return __builtin_shufflevector(a, b, 0, 1, 2, 3, 4, 5, 6, 7); }
template <int S>
DI u32x4 pack_step(const f32x16& x, float sc) {
  u32x4 p;
  p[0] = pack2(x[8 * S + 0] * sc, x[8 * S + 1] * sc);
  p[1] = pack2(x[8 * S + 2] * sc, x[8 * S + 3] * sc);
  p[2] = pack2(x[8 * S + 4] * sc, x[8 * S + 5] * sc);
  p[3] = pack2(x[8 * S + 6] * sc, x[8 * S + 7] * sc);
  return p;
}
template <int S>
DI u32x4 pack_step1(const f32x16& x) {
  u32x4 p;
  p[0] = pack2(x[8 * S + 0], x[8 * S + 1]);
  p[1] = pack2(x[8 * S + 2], x[8 * S + 3]);
  p[2] = pack2(x[8 * S + 4], x[8 * S + 5]);
  p[3] = pack2(x[8 * S + 6], x[8 * S + 7]);
  return p;
}

#define RAWBAR() do { asm volatile("" ::: "memory"); __builtin_amdgcn_s_barrier(); asm volatile("" ::: "memory"); } while (0)
#define WAITV(n) ((((n) & 15) | (((n) >> 4) << 14)) | 0x0F70)
#define WAITVL(n) ((((n) & 15) | (((n) >> 4) << 14)) | 0x0070)
DI u32x4 widen(unsigned a0, unsigned a1, unsigned b0, unsigned b1) {
  auto r0 = __builtin_amdgcn_permlane32_swap(a0, b0, false, false);
  auto r1 = __builtin_amdgcn_permlane32_swap(a1, b1, false, false);
  return u32x4{r0[0], r1[0], r0[1], r1[1]};
}

typedef __amdgpu_buffer_rsrc_t rsrc_t;
typedef unsigned v4u_t __attribute__((ext_vector_type(4)));
typedef unsigned long long u64;
typedef GLBP u64 gu64;
DI u32x4 bload(rsrc_t rs, unsigned voff, unsigned soff) {
  return __builtin_bit_cast(u32x4, __builtin_amdgcn_raw_buffer_load_b128(rs, voff, soff, 16));
}
DI void bstore(u32x4 v, rsrc_t rs, unsigned voff, unsigned soff) {
  __builtin_amdgcn_raw_buffer_store_b128(__builtin_bit_cast(v4u_t, v), rs, voff + soff, 0, 16);
}
DI void set_flag(char* ws, unsigned off, u64 tag) {
  __hip_atomic_store((gu64*)(ws + off), tag, __ATOMIC_RELAXED, __HIP_MEMORY_SCOPE_AGENT);
}
DI u64 read_tag(const char* ws) {
  u64 e;
  asm volatile("s_load_dwordx2 %0, %1, 0x0\n\ts_waitcnt lgkmcnt(0)" : "=s"(e) : "s"(ws + WS_C) : "memory");
  return e + 1;
}
DI void wait_flags256(char* ws, unsigned off, u64 tag, int lane) {
  gu64* f = (gu64*)(ws + off) + lane;
  for (unsigned spins = 0; spins < (1u << 20); ++spins) {
    bool ok = true;
#pragma unroll
    for (int k = 0; k < 4; ++k) ok &= __hip_atomic_load(f + 64 * k, __ATOMIC_RELAXED, __HIP_MEMORY_SCOPE_AGENT) == tag;
    if (__all(ok)) break;
    __builtin_amdgcn_s_sleep(2);
  }
  asm volatile("" ::: "memory");
}
DI void wait_flags16(char* ws, unsigned off, u64 tag, int lane) {
  gu64* f = (gu64*)(ws + off) + (lane & 15);
  for (unsigned spins = 0; spins < (1u << 20); ++spins) {
    if (__all(__hip_atomic_load(f, __ATOMIC_RELAXED, __HIP_MEMORY_SCOPE_AGENT) == tag)) break;
    __builtin_amdgcn_s_sleep(1);
  }
  asm volatile("" ::: "memory");
}

constexpr int P1_WSLOT = 64 * 192;
constexpr int P1_SLOT = 16384 + P1_WSLOT;
constexpr int P1_CTL = 5 * P1_SLOT + P1_WSLOT;
constexpr int P1_SINK = P1_CTL + 64;
constexpr int LDS_BYTES = P1_SINK + 256;

constexpr unsigned DROP = 0x80000000u;
template <bool V>
DI void p1_pack(const f32x16& a, int br, int ct, int tile, int lane, u32x4* sv, unsigned* so) {
  const int type = ct / 14, cbase = (ct % 14) * 32;
  if (!V) {
    const float sc = type == 0 ? QSCALE : 1.f;
    const unsigned o = (type == 0 ? WS_Q : WS_K) + br * QK_BR + (tile * 56 + (cbase >> 3)) * 512 + lane * 16;
#pragma unroll
    for (int g = 0; g < 2; ++g) {
      sv[g] = widen(pack2(a[8 * g] * sc, a[8 * g + 1] * sc), pack2(a[8 * g + 2] * sc, a[8 * g + 3] * sc),
                    pack2(a[8 * g + 4] * sc, a[8 * g + 5] * sc), pack2(a[8 * g + 6] * sc, a[8 * g + 7] * sc));
      so[g] = o + g * 1024;
    }
  } else {
    const unsigned f = cbase + (lane & 31), head = f / 28, d = f % 28;
    const unsigned o = WS_V + ((br * 16 * 64 + tile) * 2) * 1024 + (head * 64 * 2 * 64 + d + (lane & 32)) * 16;
    sv[0] = pack_step1<0>(a); so[0] = o;
    sv[1] = pack_step1<1>(a); so[1] = o + 1024;
    const unsigned c = d == 0 ? 0x3F803F80u : 0u;
    sv[2] = u32x4{c, c, c, c}; so[2] = d < 4 ? o + 28 * 16 : DROP;
    sv[3] = sv[2]; so[3] = d < 4 ? o + 28 * 16 + 1024 : DROP;
  }
}

DI void warm_l2(const u16* x, const u16* WA, const u16* WB, char LDSP* smem) {
  const int b = blockIdx.x, xcd = b & 7;
  const unsigned g = ((b >> 3) - 28) * 512 + threadIdx.x;
#pragma unroll
  for (int e = 0; e < 4; ++e) {
    const unsigned i = e * 2048 + g;
    const char* p;
    if (i < 1024) {
      p = (const char*)x + ((xcd & 3) * 512 + (i >> 1)) * 896 + 640 + (i & 1) * 128;
    } else {
      const unsigned idx = i - 1024, row = idx / 12, c = idx % 12;
      unsigned off = (row < 128 ? row + 320 : row - 128) * 2688 + (xcd >> 2) * 1280 + c * 128;
      off = off < (unsigned)(DM * N3 * 2 - 4) ? off : 0u;
      p = (const char*)(row < 128 ? WB : WA) + off;
    }
    __builtin_amdgcn_global_load_lds((const GLBP void*)p, (LDSP void*)(smem + P1_SINK), 4, 0, 0);
  }
}

DI void lds_arrive(char LDSP* smem, int idx, int last, char* ws, unsigned flag_off, u64 tag, int lane) {
  if (lane == 0) {
    if (__hip_atomic_fetch_add((int LDSP*)(smem + P1_CTL) + idx, 1, __ATOMIC_RELAXED, __HIP_MEMORY_SCOPE_WORKGROUP) == last) set_flag(ws, flag_off, tag);
  }
}

DI void p1_first(const u16* __restrict__ x, const u16* __restrict__ WA, const u16* __restrict__ WB, int mt, int nt, char LDSP* smem) {
  const int lane = threadIdx.x & 63;
  const int w = __builtin_amdgcn_readfirstlane(threadIdx.x >> 6);
  const int wi0 = w < 4 ? w : 2 * w - 4;
  const u16* xs[2];
  int wso[2];
#pragma unroll
  for (int i = 0; i < 2; ++i) {
    int lr = (w * 2 + i) * 8 + (lane >> 3);
    int logical = (lane & 7) ^ ((lr >> 1) & 7);
    xs[i] = x + (mt * 128 + lr) * DM + logical * 8;
    const int id = (wi0 + i) * 64 + lane;
    wso[i] = (id / 12) * N3 + nt * 96 + (id % 12) * 8;
  }
#pragma unroll
  for (int c = 0; c < 6; ++c) {
    if (c < 5) {
#pragma unroll
      for (int i = 0; i < 2; ++i)
        __builtin_amdgcn_global_load_lds((const GLBP void*)(xs[i] + c * 64), (LDSP void*)(smem + c * P1_SLOT + (w * 2 + i) * 1024), 16, 0, 0);
    }
    const u16* Wc = c < 5 ? WB + c * 64 * N3 : WA + 6 * 64 * N3;
    char LDSP* dst = smem + (c < 5 ? c * P1_SLOT + 16384 : 5 * P1_SLOT) + wi0 * 1024;
    __builtin_amdgcn_global_load_lds((const GLBP void*)(Wc + wso[0]), (LDSP void*)dst, 16, 0, 0);
    if (w >= 4) __builtin_amdgcn_global_load_lds((const GLBP void*)(Wc + wso[1]), (LDSP void*)(dst + 1024), 16, 0, 0);
  }
}

template <int NT, int NV>
DI void p1_body(const u16* __restrict__ x, const u16* __restrict__ WA, const u16* __restrict__ WB, char* __restrict__ ws, rsrc_t rs,
                int mt, int nt, char LDSP* smem, u64& tag, u32x4 (&sv)[8], unsigned (&so)[8]) {
  const int tid = threadIdx.x, lane = tid & 63;
  const int w = __builtin_amdgcn_readfirstlane(tid >> 6);
  const int msub = w & 3;
  const int j0 = NT == 2 ? 0 : 2;
  const int r = lane & 31, h = lane >> 5;
  constexpr int NS = 2 * (NT - NV) + 4 * NV;
  constexpr int NW = NT == 2 ? 1 : 2;
  const int wi0 = NT == 2 ? w : 2 * w - 4;
  const u16* xs[2];
  int wso[2];
#pragma unroll
  for (int i = 0; i < 2; ++i) {
    int lr = (w * 2 + i) * 8 + (lane >> 3);
    int logical = (lane & 7) ^ ((lr >> 1) & 7);
    xs[i] = x + (mt * 128 + lr) * DM + logical * 8;
    const int id = (wi0 + i) * 64 + lane;
    wso[i] = (id / 12) * N3 + nt * 96 + (id % 12) * 8;
  }
  auto wbase = [&](int s) {
    return s < 7 ? (s % 5) * P1_SLOT + 16384 : (s < 13 ? (2 + (s - 7) / 2) * P1_SLOT + ((s - 7) & 1) * P1_WSLOT : 5 * P1_SLOT);
  };
  auto issue_x = [&](int c, int i) {
    __builtin_amdgcn_global_load_lds((const GLBP void*)(xs[i] + c * 64), (LDSP void*)(smem + (c % 5) * P1_SLOT + (w * 2 + i) * 1024), 16, 0, 0);
  };
  auto issue_w = [&](int s) {
#pragma unroll
    for (int i = 0; i < NW; ++i)
      __builtin_amdgcn_global_load_lds((const GLBP void*)((s < 7 ? WB : WA) + wso[i] + (s % 7) * 64 * N3),
                                       (LDSP void*)(smem + wbase(s) + (wi0 + i) * 1024), 16, 0, 0);
  };
  int xoff[4];
  {
    int lr = 32 * msub + r, sw = (lr >> 1) & 7;
#pragma unroll
    for (int t = 0; t < 4; ++t) xoff[t] = lr * 128 + (((2 * t + h) ^ sw) << 4);
  }
  const int q4 = (lane & 15) >> 2, p4 = lane & 3, g16 = (lane >> 4) & 1;
  const int woff = (8 * h + q4) * 192 + 64 * j0 + 32 * g16 + 8 * p4;

  f32x16 acc[NT] = {};
  bf16x8 xf[7][4];
  s16x4 wl[2][4][NT], wh[2][4][NT];
  auto readf = [&](int s, int m) {
    const int t = m / NT, j = m % NT;
    if (j == 0 && s < 7) xf[s][t] = *(const bf16x8 LDSP*)(smem + (s % 5) * P1_SLOT + xoff[t]);
    const char LDSP* wp = smem + woff + wbase(s) + t * 16 * 192 + j * 64;
    wl[s & 1][t][j] = tr_read(wp);
    wh[s & 1][t][j] = tr_read(wp + 4 * 192);
  };
  tag = read_tag(ws);
  if (tid < 8) ((int LDSP*)(smem + P1_CTL))[tid] = 0;
  __builtin_amdgcn_s_waitcnt(WAITVL(4 * (NW + 2) + NW));
  RAWBAR();
#pragma unroll
  for (int m = 0; m < 4 * NT; ++m) readf(0, m);
#pragma unroll
  for (int s = 0; s < 14; ++s) {
    if (s < 7 || s == 8) {
      if (s < 3) __builtin_amdgcn_s_waitcnt(WAITVL(3 * (NW + 2) + NW));
      else if (s == 3) __builtin_amdgcn_s_waitcnt(WAITVL(2 * (NW + 2) + 3 * NW));
      else if (s == 4) __builtin_amdgcn_s_waitcnt(WAITVL(NW + 2 + 4 * NW));
      else if (s == 5) __builtin_amdgcn_s_waitcnt(WAITVL(6 * NW));
      else if (s == 6) __builtin_amdgcn_s_waitcnt(WAITVL(4 * NW));
      else __builtin_amdgcn_s_waitcnt(WAITVL(NS));
      RAWBAR();
    }
    if (s == 12) {
      __builtin_amdgcn_s_waitcnt(WAITV(0));
      asm volatile("" ::: "memory");
      lds_arrive(smem, 4, 7, ws, F1B + (nt * 16 + mt) * 8, tag, lane);
    }
    __builtin_amdgcn_sched_barrier(0);
#pragma unroll
    for (int m = 0; m < 4 * NT; ++m) {
      const int t = m / NT, j = m % NT;
      const bf16x8 wfr = cat(wl[s & 1][t][j], wh[s & 1][t][j]);
      if (j < NT - NV) acc[j] = MFMA(wfr, xf[s % 7][t], acc[j]);
      else acc[j] = MFMA(xf[s % 7][t], wfr, acc[j]);
      __builtin_amdgcn_sched_barrier(0);
      if (s < 13) readf(s + 1, m);
      if (j == 0) {
        if (s < 2) { if (t < 2) issue_x(s + 5, t); else if (t == 2) issue_w(s + 5); }
        else if (s < 5 && t < 2) issue_w(7 + 2 * (s - 2) + t);
      }
      __builtin_amdgcn_sched_barrier(0);
    }
    if (s == 6 || s == 13) {
#pragma unroll
      for (int e = 0; e < 8; ++e) so[e] = DROP;
#pragma unroll
      for (int j = 0; j < NT; ++j) {
        if (j < NT - NV) p1_pack<false>(acc[j], s == 6, nt * 3 + j0 + j, mt * 4 + msub, lane, sv + 4 * j, so + 4 * j);
        else p1_pack<true>(acc[j], s == 6, nt * 3 + j0 + j, mt * 4 + msub, lane, sv + 4 * j, so + 4 * j);
        if (s == 6) {
#pragma unroll
          for (int e = 0; e < (j < NT - NV ? 2 : 4); ++e) bstore(sv[4 * j + e], rs, so[4 * j + e], 0);
        }
        acc[j] = f32x16{};
      }
      __builtin_amdgcn_sched_barrier(0);
    }
  }
}

DI void p1_spare(const u16* __restrict__ x, const u16* __restrict__ gate_w, const u16* __restrict__ WoA, const u16* __restrict__ WoB,
                 char* __restrict__ ws, rsrc_t rs, int sp, char LDSP* smem, u64& tag) {
  const int tid = threadIdx.x, lane = tid & 63, h = lane >> 5;
  const int w = __builtin_amdgcn_readfirstlane(tid >> 6);
  {
    const u16* Wo = (sp & 1) ? WoB : WoA;
    const int col0 = 56 * (sp >> 2) + 32 * ((sp >> 1) & 1);
#pragma unroll
    for (int e = 0; e < 4; ++e) {
      const int id = (w * 4 + e) * 64 + lane;
      int idx = (id >> 2) * DM + col0 + (id & 3) * 8;
      idx = idx < DM * DM - 8 ? idx : DM * DM - 8;
      __builtin_amdgcn_global_load_lds((const GLBP void*)(Wo + idx), (LDSP void*)(smem + (w * 4 + e) * 1024), 16, 0, 0);
    }
  }
  tag = read_tag(ws);
  if (tid < 8) ((int LDSP*)(smem + P1_CTL))[tid] = 0;
  __builtin_amdgcn_s_waitcnt(0xC07F);
  RAWBAR();
  if (tid == 0) set_flag(ws, F1B + ((14 + (sp & 1)) * 16 + (sp >> 1)) * 8, tag);
  const int row = sp * 64 + (tid >> 3), part = tid & 7;
  const u32x4* xp = (const u32x4*)(x + row * DM) + part;
  const u32x4* gp = (const u32x4*)(gate_w) + part;
  float acc = 0.f;
#pragma unroll
  for (int i = 0; i < 7; ++i) {
    u32x4 a = xp[i * 8], bb = gp[i * 8];
#pragma unroll
    for (int e = 0; e < 4; ++e) {
      acc = fmaf(bf_lo(a[e]), bf_lo(bb[e]), acc);
      acc = fmaf(bf_hi(a[e]), bf_hi(bb[e]), acc);
    }
  }
  acc += __shfl_xor(acc, 1);
  acc += __shfl_xor(acc, 2);
  acc += __shfl_xor(acc, 4);
  if (part == 0)
    __hip_atomic_store((GLBP float*)(ws + WS_G) + row, 1.f / (1.f + __expf(-acc)), __ATOMIC_RELAXED, __HIP_MEMORY_SCOPE_AGENT);
  asm volatile("s_waitcnt vmcnt(0)" ::: "memory");
  __syncthreads();
  {
    const int q4 = (lane & 15) >> 2, p4 = lane & 3, g16 = (lane >> 4) & 1;
#pragma unroll
    for (int e = 0; e < 4; ++e) {
      const int f = w * 4 + e;
      const char LDSP* p = smem + (28 * (f >> 1) + 16 * (f & 1) + 4 * h + q4) * 64 + 32 * g16 + 8 * p4;
      bstore(__builtin_bit_cast(u32x4, cat(tr_read(p), tr_read(p + 8 * 64))), rs, WS_W + (sp * 32 + f) * 1024 + lane * 16, 0);
    }
  }
}

constexpr unsigned long long lmask(int i, bool prev) {
  unsigned long long m = 0;
  for (int l = 0; l < 64; ++l) {
    int r = l & 31, h = l >> 5;
    int kr = (i & 3) + 8 * (i >> 2) + 4 * h;
    int diff = r - kr + (prev ? 32 : 0);
    if (diff >= 0 && diff <= 14 && (diff & 1) == 0) m |= 1ull << l;
  }
  return m;
}
DI float sel_mask(float val, float other, unsigned long long mask) {
  return __builtin_amdgcn_inverse_ballot_w64(mask) ? val : other;
}
template <int ODD>
DI void set_foreign(u32x4& f0, u32x4& f1, int h, unsigned a, unsigned b) {
  if (ODD) { if (!h) { f0[0] = a; f0[1] = b; } }
  else { if (h) { f1[2] = a; f1[3] = b; } }
}
DI float gload(char* ws, int idx) { return __hip_atomic_load((GLBP float*)(ws + WS_G) + idx, __ATOMIC_RELAXED, __HIP_MEMORY_SCOPE_AGENT); }

template <int ODD>
DI void p2_body(char* __restrict__ ws, u16* __restrict__ out, rsrc_t rs, char LDSP* smem, const u64 tag, const int fidx,
                const u32x4 (&sv)[8], const unsigned (&so)[8]) {
  f32x4 LDSP* part = (f32x4 LDSP*)smem;
  float LDSP* cw = (float LDSP*)(smem + 131072);
  volatile int LDSP* ctl = (volatile int LDSP*)(smem + P1_CTL);
  const int tid = threadIdx.x;
  int lane = tid & 63;
  const int w = __builtin_amdgcn_readfirstlane(tid >> 6);
  int r = lane & 31, h = lane >> 5;
  const int b = blockIdx.x;
  const int head = (b & 7) * 2 + ODD, qb = b >> 4;
  const int oqb = (b & 7) * 2 + (b >> 7), oqh = (b >> 3) & 1, nb = (b >> 4) & 7;
  const unsigned p0 = (7 * head) >> 1;
  unsigned l16 = lane * 16;
  const unsigned qoff = WS_Q + QK_BR + (qb * 4 * 56 + p0) * 512;
  const unsigned koff = WS_K + QK_BR + (w * 8 * 56 + p0) * 512;
  const unsigned voff = WS_V + ((16 + head) * 64 + w * 8) * 2048;
  __syncthreads();
  if (w < 4) {
#pragma unroll
    for (int e = 0; e < 8; ++e) {
      ((u32x4 LDSP*)(smem + 65536))[(w * 8 + e) * 64 + lane] = sv[e];
      ((unsigned LDSP*)(smem + 65536 + 32768))[(w * 8 + e) * 64 + lane] = so[e];
    }
  }
  if (w == 0) {
    const int sel = lane >> 4;
    const int c0 = sel < 2 ? 448 + 8 * (int)p0 + (sel & 1) * 31 : 896 + 28 * head + (sel & 1) * 27;
    gu64* f = (gu64*)(ws + F1B) + (c0 / 96) * 16 + (lane & 15);
    gu64* fq = (gu64*)(ws + F1B) + ((8 * (int)p0 + (lane & 1) * 31) / 96) * 16 + qb;
    for (unsigned spins = 0; spins < (1u << 20); ++spins) {
      bool ok = __hip_atomic_load(f, __ATOMIC_RELAXED, __HIP_MEMORY_SCOPE_AGENT) == tag;
      ok &= __hip_atomic_load(fq, __ATOMIC_RELAXED, __HIP_MEMORY_SCOPE_AGENT) == tag;
      if (__all(ok)) break;
      __builtin_amdgcn_s_sleep(1);
    }
    asm volatile("" ::: "memory");
  }
  __syncthreads();
  u32x4 q[4][2];
  u32x4 kf[4][2], vf[4][2];
  auto loadkv = [&](int t) {
    kf[t & 3][0] = bload(rs, l16, koff + t * 28672);
    kf[t & 3][1] = bload(rs, l16 + 1024, koff + t * 28672);
    vf[t & 3][0] = bload(rs, l16, voff + t * 2048);
    vf[t & 3][1] = bload(rs, l16 + 1024, voff + t * 2048);
  };
  auto store_a = [&]() {
#pragma unroll
    for (int e = 0; e < 4; ++e) bstore(sv[e], rs, so[e], 0);
#pragma unroll
    for (int e = 0; e < 8; ++e)
      bstore(((u32x4 LDSP*)(smem + 65536))[((w - 4) * 8 + e) * 64 + lane], rs, ((unsigned LDSP*)(smem + 65536 + 32768))[((w - 4) * 8 + e) * 64 + lane], 0);
  };
  if (w >= 4) {
    store_a();
    asm volatile("s_waitcnt vmcnt(0)" ::: "memory");
    lds_arrive(smem, 2, 3, ws, F1A + fidx * 8, tag, lane);
    if (b == 0 && w == 5) {
      wait_flags256(ws, F1B, tag, lane);
      if (lane == 0) set_flag(ws, WS_C, tag);
    }
    __builtin_amdgcn_s_setprio(1);
  }
  kf[0][0] = bload(rs, l16, koff);
  kf[0][1] = bload(rs, l16 + 1024, koff);
#pragma unroll
  for (int i = 0; i < 4; ++i) { q[i][0] = bload(rs, l16, qoff + i * 28672); q[i][1] = bload(rs, l16 + 1024, qoff + i * 28672); }
  vf[0][0] = bload(rs, l16, voff);
  vf[0][1] = bload(rs, l16 + 1024, voff);
  loadkv(1);
  loadkv(2);
  __builtin_amdgcn_sched_barrier(0);

  auto fold_shift = [&](int i, float m) {
    m = swap_max(m);
    if (!h) cw[(i * 32 + r) * 8 + w] = m;
    const float c = -m;
    unsigned p01 = pack2(c, 0.f);
    float r1 = c - bf_lo(p01);
    unsigned p1 = pack2(r1, 0.f);
    float r2 = r1 - bf_lo(p1);
    unsigned p2 = pack2(r2, 0.f);
    set_foreign<ODD>(q[i][0], q[i][1], h, (p01 & 0xffffu) | (p1 << 16), p2 & 0xffffu);
  };
  const int ri = w >> 1, rs1 = w & 1;
  f32x4 a0, a1;
  float l;
  auto write_part = [&](f32x16 (&o)[4]) {
#pragma unroll
    for (int i = 0; i < 4; ++i)
#pragma unroll
      for (int g = 0; g < 4; ++g)
        part[((w * 4 + i) * 4 + g) * 64 + lane] = f32x4{o[i][4 * g], o[i][4 * g + 1], o[i][4 * g + 2], o[i][4 * g + 3]};
  };
  auto reduce = [&]() {
    const f32x4 c0 = *(f32x4 LDSP*)(cw + (ri * 32 + r) * 8), c1 = *(f32x4 LDSP*)(cw + (ri * 32 + r) * 8 + 4);
    const float cm = fmaxf(fmaxf(fmaxf(c0[0], c0[1]), fmaxf(c0[2], c0[3])), fmaxf(fmaxf(c1[0], c1[1]), fmaxf(c1[2], c1[3])));
    a0 = f32x4{}; a1 = f32x4{}; l = 0.f;
#pragma unroll
    for (int ww = 0; ww < 8; ++ww) {
      const float wt = __builtin_amdgcn_exp2f((ww < 4 ? c0[ww & 3] : c1[ww & 3]) - cm);
      a0 += part[((ww * 4 + ri) * 4 + 2 * rs1) * 64 + lane] * wt;
      a1 += part[((ww * 4 + ri) * 4 + 2 * rs1 + 1) * 64 + lane] * wt;
      l += ((float LDSP*)(part + ((ww * 4 + ri) * 4 + 3) * 64 + 32 + r))[0] * wt;
    }
  };
  const int kq = w & 3;
  u32x4 wfr[2][2][8];
  auto loadW = [&]() {
#pragma unroll
    for (int br = 0; br < 2; ++br)
#pragma unroll
      for (int nt = 0; nt < 2; ++nt) {
        const unsigned wo = WS_W + (((nb * 2 + nt) * 32 + br * 16 + 4 * kq) * 2) * 1024;
#pragma unroll
        for (int e = 0; e < 8; ++e) wfr[br][nt][e] = bload(rs, l16 + (e & 3) * 1024, wo + (e >> 2) * 4096);
      }
  };

#pragma unroll
  for (int i = 0; i < 4; ++i) {
    set_foreign<ODD>(q[i][0], q[i][1], h, 0u, 0u);
    f32x16 s = MFMA(as_frag(kf[0][0]), as_frag(q[i][0]), f32x16{});
    s = MFMA(as_frag(kf[0][1]), as_frag(q[i][1]), s);
    fold_shift(i, fmaxf(fmaxf(s[0], s[1]), s[2]));
  }
  __builtin_amdgcn_s_setprio(0);
  {
    f32x16 o[4] = {};
    set_foreign<ODD>(kf[0][0], kf[0][1], h, 0x3F803F80u, 0x00003F80u);
    f32x16 sa = MFMA(as_frag(kf[0][0]), as_frag(q[0][0]), f32x16{});
    sa = MFMA(as_frag(kf[0][1]), as_frag(q[0][1]), sa);
    f32x16 sb;
    u32x4 pa;
    u32x4 pbp;
    float e3[4];
#define SB __builtin_amdgcn_sched_barrier(0)
#define EXPQ(S, g) { S[4 * g] = __builtin_amdgcn_exp2f(S[4 * g]); S[4 * g + 1] = __builtin_amdgcn_exp2f(S[4 * g + 1]); \
                     S[4 * g + 2] = __builtin_amdgcn_exp2f(S[4 * g + 2]); S[4 * g + 3] = __builtin_amdgcn_exp2f(S[4 * g + 3]); }
    auto step = [&](auto nc, f32x16& sc, f32x16& sn) {
      constexpr int n = decltype(nc)::value;
      constexpr int t = n >> 2, i = n & 3;
      constexpr int t1 = ((n + 1) >> 2) & 3, i1 = (n + 1) & 3;
      constexpr int tp = n > 0 ? ((n - 1) >> 2) & 3 : 0, ip = n > 0 ? (n - 1) & 3 : 0;
      if (i == 1 && t + 3 < 8) loadkv(t + 3);
      if (i == 3 && n + 1 < 32) set_foreign<ODD>(kf[t1][0], kf[t1][1], h, 0x3F803F80u, 0x00003F80u);
      SB;
      if (n + 1 < 32) sn = MFMA(as_frag(kf[t1][0]), as_frag(q[i1][0]), f32x16{});
      SB;
      if (n > 0) { pbp[2] = pack2(e3[0], e3[1]); pbp[3] = pack2(e3[2], e3[3]); }
      SB;
      EXPQ(sc, 0);
      SB;
      if (n + 1 < 32) sn = MFMA(as_frag(kf[t1][1]), as_frag(q[i1][1]), sn);
      SB;
      pa[0] = pack2(sc[0], sc[1]); pa[1] = pack2(sc[2], sc[3]);
      SB;
      EXPQ(sc, 1);
      SB;
      if (n > 0) o[ip] = MFMA(as_frag(vf[tp][1]), as_frag(pbp), o[ip]);
      SB;
      pa[2] = pack2(sc[4], sc[5]); pa[3] = pack2(sc[6], sc[7]);
      SB;
      EXPQ(sc, 2);
      SB;
      o[i] = MFMA(as_frag(vf[t & 3][0]), as_frag(pa), o[i]);
      SB;
      pbp[0] = pack2(sc[8], sc[9]); pbp[1] = pack2(sc[10], sc[11]);
      SB;
      EXPQ(sc, 3);
      e3[0] = sc[12]; e3[1] = sc[13]; e3[2] = sc[14]; e3[3] = sc[15];
      SB;
    };
#define STEP2(n) step(std::integral_constant<int, n>{}, sa, sb); step(std::integral_constant<int, n + 1>{}, sb, sa);
    STEP2(0) STEP2(2) STEP2(4) STEP2(6) STEP2(8) STEP2(10) STEP2(12) STEP2(14)
    STEP2(16) STEP2(18) STEP2(20) STEP2(22) STEP2(24) STEP2(26) STEP2(28) STEP2(30)
#undef STEP2
    pbp[2] = pack2(e3[0], e3[1]); pbp[3] = pack2(e3[2], e3[3]);
    o[3] = MFMA(as_frag(vf[3][1]), as_frag(pbp), o[3]);
    asm volatile("" : "+v"(lane));
    r = lane & 31; h = lane >> 5; l16 = lane * 16;
    {
      float chk = 0.f;
#pragma unroll
      for (int i = 0; i < 4; ++i)
#pragma unroll
        for (int j = 0; j < 16; ++j) chk = fmaf(o[i][j], 0.f, chk);
      const float lmax = fmaxf(fmaxf(o[0][12], o[1][12]), fmaxf(o[2][12], o[3][12]));
      const bool bad = !(chk == 0.f) || (h && !(lmax < 1.0e24f));
      if (__any(bad)) {
#pragma unroll
        for (int i = 0; i < 4; ++i) set_foreign<ODD>(q[i][0], q[i][1], h, 0u, 0u);
        float m[4] = {-3.0e38f, -3.0e38f, -3.0e38f, -3.0e38f};
#pragma unroll 1
        for (int t = 0; t < 8; ++t) {
          u32x4 k0 = bload(rs, l16, koff + t * 28672), k1 = bload(rs, l16 + 1024, koff + t * 28672);
#pragma unroll
          for (int i = 0; i < 4; ++i) {
            f32x16 s = MFMA(as_frag(k0), as_frag(q[i][0]), f32x16{});
            s = MFMA(as_frag(k1), as_frag(q[i][1]), s);
#pragma unroll
            for (int j = 0; j < 16; ++j) m[i] = fmaxf(m[i], s[j]);
          }
        }
#pragma unroll
        for (int i = 0; i < 4; ++i) { fold_shift(i, m[i]); o[i] = f32x16{}; }
#pragma unroll 1
        for (int t = 0; t < 8; ++t) {
          u32x4 k0 = bload(rs, l16, koff + t * 28672), k1 = bload(rs, l16 + 1024, koff + t * 28672);
          u32x4 v0 = bload(rs, l16, voff + t * 2048), v1 = bload(rs, l16 + 1024, voff + t * 2048);
          set_foreign<ODD>(k0, k1, h, 0x3F803F80u, 0x00003F80u);
#pragma unroll
          for (int i = 0; i < 4; ++i) {
            f32x16 s = MFMA(as_frag(k0), as_frag(q[i][0]), f32x16{});
            s = MFMA(as_frag(k1), as_frag(q[i][1]), s);
#pragma unroll
            for (int j = 0; j < 16; ++j) s[j] = __builtin_amdgcn_exp2f(s[j]);
            o[i] = MFMA(as_frag(v0), as_frag(pack_step1<0>(s)), o[i]);
            o[i] = MFMA(as_frag(v1), as_frag(pack_step1<1>(s)), o[i]);
          }
        }
      }
    }
    write_part(o);
  }
  SB;
  if (w < 4) {
    {
      gu64* f = (gu64*)(ws + F1A) + (qb > 0 ? qb - 1 : 0) * 16 + (lane & 31) - ((lane & 15) >= 14 ? 2 : 0);
      for (unsigned spins = 0; spins < (1u << 20); ++spins) {
        if (__all(__hip_atomic_load(f, __ATOMIC_RELAXED, __HIP_MEMORY_SCOPE_AGENT) == tag)) break;
        __builtin_amdgcn_s_sleep(2);
      }
      asm volatile("" ::: "memory");
    const int ltile = qb * 4 + (w & 3);
    const unsigned lqo = WS_Q + (ltile * 56 + p0) * 512, lko = WS_K + (ltile * 56 + p0) * 512, lvo = WS_V + (head * 64 + ltile) * 2048;
    const unsigned pd = ltile > 0 ? 1 : 0;
    u32x4 lq0 = bload(rs, l16, lqo), lq1 = bload(rs, l16 + 1024, lqo);
    u32x4 lk0 = bload(rs, l16, lko), lk1 = bload(rs, l16 + 1024, lko);
    u32x4 pk0 = bload(rs, l16, lko - pd * 28672), pk1 = bload(rs, l16 + 1024, lko - pd * 28672);
    u32x4 lv0 = bload(rs, l16, lvo), lv1 = bload(rs, l16 + 1024, lvo);
    u32x4 pv1 = bload(rs, l16 + 1024, lvo - pd * 2048);
    set_foreign<ODD>(lq0, lq1, h, 0u, 0u);
    f32x16 sp = MFMA(as_frag(pk0), as_frag(lq0), f32x16{});
    sp = MFMA(as_frag(pk1), as_frag(lq1), sp);
    f32x16 sc = MFMA(as_frag(lk0), as_frag(lq0), f32x16{});
    sc = MFMA(as_frag(lk1), as_frag(lq1), sc);
    const bool first = (ltile == 0);
    const float NEG = -3.0e38f;
    float m = NEG;
#define LM_C(i) { sc[i] = sel_mask(sc[i], NEG, lmask(i, false)); m = fmaxf(m, sc[i]); }
#define LM_P(i) { sp[i] = sel_mask(sp[i], NEG, first ? 0ull : lmask(i, true)); m = fmaxf(m, sp[i]); }
    LM_C(0) LM_C(1) LM_C(2) LM_C(3) LM_C(4) LM_C(5) LM_C(6) LM_C(7)
    LM_C(8) LM_C(9) LM_C(10) LM_C(11) LM_C(12) LM_C(13) LM_C(14) LM_C(15)
    LM_P(8) LM_P(9) LM_P(10) LM_P(11) LM_P(12) LM_P(13) LM_P(14) LM_P(15)
#undef LM_C
#undef LM_P
    m = swap_max(m);
#pragma unroll
    for (int i = 0; i < 16; ++i) sc[i] = __builtin_amdgcn_exp2f(sc[i] - m);
#pragma unroll
    for (int i = 8; i < 16; ++i) sp[i] = __builtin_amdgcn_exp2f(sp[i] - m);
    f32x16 o = MFMA(as_frag(pv1), as_frag(pack_step1<1>(sp)), f32x16{});
    o = MFMA(as_frag(lv0), as_frag(pack_step1<0>(sc)), o);
    o = MFMA(as_frag(lv1), as_frag(pack_step1<1>(sc)), o);
    float ll = __shfl(o[12], 32 + r);
    float inv = 1.f / ll;
    u32x4 o0 = pack_step<0>(o, inv), o1 = pack_step<1>(o, inv);
    if (h) { o1[2] = 0; o1[3] = 0; }
    const unsigned oo = WS_O + ((ltile * 32 + head) * 2) * 1024;
    bstore(o0, rs, l16, oo);
    bstore(o1, rs, l16 + 1024, oo);
    asm volatile("s_waitcnt vmcnt(0)" ::: "memory");
    lds_arrive(smem, 1, 3, ws, F2A + (qb * 16 + head) * 8, tag, lane);
    }
    {
      gu64* f = (gu64*)(ws + F1A) + ((lane & 31) >> 1) * 16 + 14 + (lane & 1);
      for (unsigned spins = 0; spins < (1u << 20); ++spins) {
        if (__all(__hip_atomic_load(f, __ATOMIC_RELAXED, __HIP_MEMORY_SCOPE_AGENT) == tag)) break;
        __builtin_amdgcn_s_sleep(2);
      }
      asm volatile("" ::: "memory");
    }
    loadW();
  }
  auto publish = [&]() {
    const float inv = 1.f / l;
    u32x4 ov;
    ov[0] = pack2(a0[0] * inv, a0[1] * inv);
    ov[1] = pack2(a0[2] * inv, a0[3] * inv);
    ov[2] = pack2(a1[0] * inv, a1[1] * inv);
    ov[3] = pack2(a1[2] * inv, a1[3] * inv);
    if (h && rs1) { ov[2] = 0; ov[3] = 0; }
    bstore(ov, rs, l16, WS_O + (((qb * 4 + ri) * 32 + 16 + head) * 2 + rs1) * 1024);
  };
  __syncthreads();
  reduce();
  publish();
  asm volatile("s_waitcnt vmcnt(0)" ::: "memory");
  __syncthreads();
  if (tid == 256) set_flag(ws, F2B + (qb * 16 + head) * 8, tag);
  {
    const int oqt = oqb * 4 + oqh * 2;
    const unsigned ofo = WS_O + ((oqt * 32 + 4 * kq) * 2) * 1024;
    auto proj = [&](int br) {
      u32x4 ofr[2][8];
      float gt[2];
#pragma unroll
      for (int qq = 0; qq < 2; ++qq) {
#pragma unroll
        for (int e = 0; e < 8; ++e) ofr[qq][e] = bload(rs, l16 + (e & 3) * 1024, ofo + qq * 65536 + br * 32768 + (e >> 2) * 4096);
        gt[qq] = gload(ws, (oqt + qq) * 32 + r);
      }
#pragma unroll
      for (int qq = 0; qq < 2; ++qq) {
        f32x16 acc[2] = {};
        const float gg = br ? 1.f - gt[qq] : gt[qq];
#pragma unroll
        for (int e = 0; e < 8; ++e)
#pragma unroll
          for (int nt = 0; nt < 2; ++nt) acc[nt] = MFMA(as_frag(wfr[br][nt][e]), as_frag(ofr[qq][e]), acc[nt]);
#pragma unroll
        for (int nt = 0; nt < 2; ++nt)
#pragma unroll
          for (int g = 0; g < 4; ++g)
            part[(((nt * 2 + qq) * 8 + br * 4 + kq) * 4 + g) * 64 + lane] =
                f32x4{acc[nt][4 * g] * gg, acc[nt][4 * g + 1] * gg, acc[nt][4 * g + 2] * gg, acc[nt][4 * g + 3] * gg};
      }
    };
    if (w < 4) {
      wait_flags16(ws, F2A + oqb * 128, tag, lane);
      proj(0);
    }
    if (w == 4) wait_flags16(ws, F2B + oqb * 128, tag, lane);
    __syncthreads();
    if (w < 4) proj(1);
  }
  __syncthreads();
  {
    const int ft = w & 3, fg = w >> 2;
    f32x4 s0 = {}, s1 = {};
#pragma unroll
    for (int k = 0; k < 8; ++k) {
      s0 += part[((ft * 8 + k) * 4 + 2 * fg) * 64 + lane];
      s1 += part[((ft * 8 + k) * 4 + 2 * fg + 1) * 64 + lane];
    }
    const u32x4 ov = widen(pack2(s0[0], s0[1]), pack2(s0[2], s0[3]), pack2(s1[0], s1[1]), pack2(s1[2], s1[3]));
    const int col = 32 * (ft >> 1) + 16 * fg + 8 * h;
    if (col < 56) *(u32x4*)(out + (oqb * 128 + oqh * 64 + (ft & 1) * 32 + r) * DM + nb * 56 + col) = ov;
  }
}

__global__ __launch_bounds__(512, 2) void fused_block(const u16* __restrict__ x, const u16* __restrict__ WA, const u16* __restrict__ WB,
                                                      const u16* __restrict__ gate_w, const u16* __restrict__ WoA,
                                                      const u16* __restrict__ WoB, char* __restrict__ ws, u16* __restrict__ out) {
  extern __shared__ __attribute__((aligned(16))) char smem_[];
  char LDSP* smem = (char LDSP*)smem_;
  const rsrc_t rs = __builtin_amdgcn_make_buffer_rsrc(ws, 0, 32u << 20, 0x00020000);
  u64 tag;
  asm volatile("s_getpc_b64 %0" : "=s"(tag));
  __builtin_amdgcn_global_load_lds((const GLBP void*)((const char*)tag + (((blockIdx.x >> 3) * 8 + (threadIdx.x >> 6)) * 2 + (threadIdx.x & 1)) * 128),
                                   (LDSP void*)(smem + P1_SINK), 4, 0, 0);
  u32x4 sv[8];
  unsigned so[8];
  const int b = blockIdx.x, xcd = b & 7, j = b >> 3;
  if (__builtin_expect(j < 28, 1)) {
    const int mt = 4 * (xcd & 3) + (j & 3);
    const int nt = 7 * (xcd >> 2) + (j >> 2);
    p1_first(x, WA, WB, mt, nt, smem);
    if (__builtin_amdgcn_readfirstlane(threadIdx.x >> 8) == 0) {
      if (nt < 9) p1_body<2, 0>(x, WA, WB, ws, rs, mt, nt, smem, tag, sv, so);
      else if (nt == 9) p1_body<2, 1>(x, WA, WB, ws, rs, mt, nt, smem, tag, sv, so);
      else p1_body<2, 2>(x, WA, WB, ws, rs, mt, nt, smem, tag, sv, so);
    } else {
      if (nt < 9) p1_body<1, 0>(x, WA, WB, ws, rs, mt, nt, smem, tag, sv, so);
      else p1_body<1, 1>(x, WA, WB, ws, rs, mt, nt, smem, tag, sv, so);
    }
  } else {
    warm_l2(x, WA, WB, smem);
    p1_spare(x, gate_w, WoA, WoB, ws, rs, xcd * 4 + j - 28, smem, tag);
    asm volatile("s_waitcnt vmcnt(0)" ::: "memory");
#pragma unroll
    for (int e = 0; e < 8; ++e) { sv[e] = u32x4{}; so[e] = DROP; }
  }
  const int sp = xcd * 4 + j - 28;
  const int fidx = j < 28 ? (4 * (xcd & 3) + (j & 3)) * 16 + 7 * (xcd >> 2) + (j >> 2) : (sp >> 1) * 16 + 14 + (sp & 1);
  if ((b >> 3) & 1) p2_body<1>(ws, out, rs, smem, tag, fidx, sv, so);
  else p2_body<0>(ws, out, rs, smem, tag, fidx, sv, so);
}

extern "C" void kernel_launch(void* const* d_in, const int* in_sizes, int n_in, void* d_out, int out_size, void* d_ws,
                              size_t ws_size, hipStream_t stream) {
  const u16* x = (const u16*)d_in[0];
  const u16* WA = (const u16*)d_in[1];
  const u16* WoA = (const u16*)d_in[2];
  const u16* WB = (const u16*)d_in[4];
  const u16* WoB = (const u16*)d_in[5];
  const u16* gw = (const u16*)d_in[6];
  (void)hipFuncSetAttribute((const void*)fused_block, hipFuncAttributeMaxDynamicSharedMemorySize, LDS_BYTES);
  fused_block<<<256, 512, LDS_BYTES, stream>>>(x, WA, WB, gw, WoA, WoB, (char*)d_ws, (u16*)d_out);
}
```

```cpp
#include <hip/hip_runtime.h>
#include <hip/hip_bf16.h>
#include <stdint.h>
#include <type_traits>

typedef short bf16x8 __attribute__((ext_vector_type(8)));
typedef short s16x4 __attribute__((ext_vector_type(4)));
typedef float f32x16 __attribute__((ext_vector_type(16)));
typedef float f32x4 __attribute__((ext_vector_type(4)));
typedef float f32x2 __attribute__((ext_vector_type(2)));
typedef unsigned u32x4 __attribute__((ext_vector_type(4)));
typedef unsigned u32x2 __attribute__((ext_vector_type(2)));
typedef __bf16 bf2 __attribute__((ext_vector_type(2)));
typedef unsigned short u16;

#define DI __device__ __forceinline__
#define MFMA(a, b, c) __builtin_amdgcn_mfma_f32_32x32x16_bf16((a), (b), (c), 0, 0, 0)
#define LDSP __attribute__((address_space(3)))
#define GLBP __attribute__((address_space(1)))

constexpr int SEQ = 2048, DM = 448, NH = 16, DH = 28, N3 = 1344;
constexpr float QSCALE = 0.18898223650461362f * 1.4426950408889634f;

constexpr unsigned WS_Q = 0;
constexpr unsigned WS_K = 4u << 20;
constexpr unsigned WS_V = 8u << 20;
constexpr unsigned WS_O = 12u << 20;
constexpr unsigned WS_G = 16u << 20;
constexpr unsigned WS_W = 17u << 20;
constexpr unsigned WS_C = 19u << 20;
constexpr unsigned F1B = WS_C + 4096, F1A = WS_C + 8192, F2A = WS_C + 12288, F2B = WS_C + 16384;
constexpr unsigned QK_BR = 64 * 56 * 512;

DI unsigned pack2(float lo, float hi) {
  f32x2 v = {lo, hi};
  return __builtin_bit_cast(unsigned, __builtin_convertvector(v, bf2));
}
DI bf16x8 as_frag(u32x4 v) { return __builtin_bit_cast(bf16x8, v); }
DI float bf_lo(unsigned v) { return __uint_as_float(v << 16); }
DI float bf_hi(unsigned v) { return __uint_as_float(v & 0xffff0000u); }
DI float swap_max(float v) {
  auto rr = __builtin_amdgcn_permlane32_swap(__float_as_uint(v), __float_as_uint(v), false, false);
  return fmaxf(__uint_as_float(rr[0]), __uint_as_float(rr[1]));
}
DI s16x4 tr_read(const char LDSP* p) {
  return __builtin_amdgcn_ds_read_tr16_b64_v4i16((s16x4 LDSP*)p);
}
DI bf16x8 cat(s16x4 a, s16x4 b) { return __builtin_shufflevector(a, b, 0, 1, 2, 3, 4, 5, 6, 7); }
template <int S>
DI u32x4 pack_step(const f32x16& x, float sc) {
  u32x4 p;
  p[0] = pack2(x[8 * S + 0] * sc, x[8 * S + 1] * sc);
  p[1] = pack2(x[8 * S + 2] * sc, x[8 * S + 3] * sc);
  p[2] = pack2(x[8 * S + 4] * sc, x[8 * S + 5] * sc);
  p[3] = pack2(x[8 * S + 6] * sc, x[8 * S + 7] * sc);
  return p;
}
template <int S>
DI u32x4 pack_step1(const f32x16& x) {
  u32x4 p;
  p[0] = pack2(x[8 * S + 0], x[8 * S + 1]);
  p[1] = pack2(x[8 * S + 2], x[8 * S + 3]);
  p[2] = pack2(x[8 * S + 4], x[8 * S + 5]);
  p[3] = pack2(x[8 * S + 6], x[8 * S + 7]);
  return p;
}

#define RAWBAR() do { asm volatile("" ::: "memory"); __builtin_amdgcn_s_barrier(); asm volatile("" ::: "memory"); } while (0)
#define WAITV(n) ((((n) & 15) | (((n) >> 4) << 14)) | 0x0F70)
#define WAITVL(n) ((((n) & 15) | (((n) >> 4) << 14)) | 0x0070)
DI u32x4 widen(unsigned a0, unsigned a1, unsigned b0, unsigned b1) {
  auto r0 = __builtin_amdgcn_permlane32_swap(a0, b0, false, false);
  auto r1 = __builtin_amdgcn_permlane32_swap(a1, b1, false, false);
  return u32x4{r0[0], r1[0], r0[1], r1[1]};
}

typedef __amdgpu_buffer_rsrc_t rsrc_t;
typedef unsigned v4u_t __attribute__((ext_vector_type(4)));
typedef unsigned long long u64;
typedef GLBP u64 gu64;
DI u32x4 bload(rsrc_t rs, unsigned voff, unsigned soff) {
  return __builtin_bit_cast(u32x4, __builtin_amdgcn_raw_buffer_load_b128(rs, voff, soff, 16));
}
DI void bstore(u32x4 v, rsrc_t rs, unsigned voff, unsigned soff) {
  __builtin_amdgcn_raw_buffer_store_b128(__builtin_bit_cast(v4u_t, v), rs, voff + soff, 0, 16);
}
DI void set_flag(char* ws, unsigned off, u64 tag) {
  __hip_atomic_store((gu64*)(ws + off), tag, __ATOMIC_RELAXED, __HIP_MEMORY_SCOPE_AGENT);
}
DI u64 read_tag(const char* ws) {
  u64 e;
  asm volatile("s_load_dwordx2 %0, %1, 0x0\n\ts_waitcnt lgkmcnt(0)" : "=s"(e) : "s"(ws + WS_C) : "memory");
  return e + 1;
}
DI void wait_flags256(char* ws, unsigned off, u64 tag, int lane) {
  gu64* f = (gu64*)(ws + off) + lane;
  for (unsigned spins = 0; spins < (1u << 20); ++spins) {
    bool ok = true;
#pragma unroll
    for (int k = 0; k < 4; ++k) ok &= __hip_atomic_load(f + 64 * k, __ATOMIC_RELAXED, __HIP_MEMORY_SCOPE_AGENT) == tag;
    if (__all(ok)) break;
    __builtin_amdgcn_s_sleep(2);
  }
  asm volatile("" ::: "memory");
}
DI void wait_flags16(char* ws, unsigned off, u64 tag, int lane) {
  gu64* f = (gu64*)(ws + off) + (lane & 15);
  for (unsigned spins = 0; spins < (1u << 20); ++spins) {
    if (__all(__hip_atomic_load(f, __ATOMIC_RELAXED, __HIP_MEMORY_SCOPE_AGENT) == tag)) break;
    __builtin_amdgcn_s_sleep(1);
  }
  asm volatile("" ::: "memory");
}

constexpr int P1_WSLOT = 64 * 192;
constexpr int P1_SLOT = 16384 + P1_WSLOT;
constexpr int P1_CTL = 5 * P1_SLOT + P1_WSLOT;
constexpr int P1_SINK = P1_CTL + 64;
constexpr int LDS_BYTES = P1_SINK + 256;

constexpr unsigned DROP = 0x80000000u;
template <bool V>
DI void p1_pack(const f32x16& a, int br, int ct, int tile, int lane, u32x4* sv, unsigned* so) {
  const int type = ct / 14, cbase = (ct % 14) * 32;
  if (!V) {
    const float sc = type == 0 ? QSCALE : 1.f;
    const unsigned o = (type == 0 ? WS_Q : WS_K) + br * QK_BR + (tile * 56 + (cbase >> 3)) * 512 + lane * 16;
#pragma unroll
    for (int g = 0; g < 2; ++g) {
      sv[g] = widen(pack2(a[8 * g] * sc, a[8 * g + 1] * sc), pack2(a[8 * g + 2] * sc, a[8 * g + 3] * sc),
                    pack2(a[8 * g + 4] * sc, a[8 * g + 5] * sc), pack2(a[8 * g + 6] * sc, a[8 * g + 7] * sc));
      so[g] = o + g * 1024;
    }
  } else {
    const unsigned f = cbase + (lane & 31), head = f / 28, d = f % 28;
    const unsigned o = WS_V + ((br * 16 * 64 + tile) * 2) * 1024 + (head * 64 * 2 * 64 + d + (lane & 32)) * 16;
    sv[0] = pack_step1<0>(a); so[0] = o;
    sv[1] = pack_step1<1>(a); so[1] = o + 1024;
    const unsigned c = d == 0 ? 0x3F803F80u : 0u;
    sv[2] = u32x4{c, c, c, c}; so[2] = d < 4 ? o + 28 * 16 : DROP;
    sv[3] = sv[2]; so[3] = d < 4 ? o + 28 * 16 + 1024 : DROP;
  }
}

DI void warm_l2(const u16* x, const u16* WA, const u16* WB, char LDSP* smem) {
  const int b = blockIdx.x, xcd = b & 7;
  const unsigned g = ((b >> 3) - 28) * 512 + threadIdx.x;
#pragma unroll
  for (int e = 0; e < 4; ++e) {
    const unsigned i = e * 2048 + g;
    const char* p;
    if (i < 1024) {
      p = (const char*)x + ((xcd & 3) * 512 + (i >> 1)) * 896 + 640 + (i & 1) * 128;
    } else {
      const unsigned idx = i - 1024, row = idx / 12, c = idx % 12;
      unsigned off = (row < 128 ? row + 320 : row - 128) * 2688 + (xcd >> 2) * 1280 + c * 128;
      off = off < (unsigned)(DM * N3 * 2 - 4) ? off : 0u;
      p = (const char*)(row < 128 ? WB : WA) + off;
    }
    __builtin_amdgcn_global_load_lds((const GLBP void*)p, (LDSP void*)(smem + P1_SINK), 4, 0, 0);
  }
}

DI void lds_arrive(char LDSP* smem, int idx, int last, char* ws, unsigned flag_off, u64 tag, int lane) {
  if (lane == 0) {
    if (__hip_atomic_fetch_add((int LDSP*)(smem + P1_CTL) + idx, 1, __ATOMIC_RELAXED, __HIP_MEMORY_SCOPE_WORKGROUP) == last) set_flag(ws, flag_off, tag);
  }
}

DI void p1_first(const u16* __restrict__ x, const u16* __restrict__ WA, const u16* __restrict__ WB, int mt, int nt, char LDSP* smem) {
  const int lane = threadIdx.x & 63;
  const int w = __builtin_amdgcn_readfirstlane(threadIdx.x >> 6);
  const int wi0 = w < 4 ? w : 2 * w - 4;
  const u16* xs[2];
  int wso[2];
#pragma unroll
  for (int i = 0; i < 2; ++i) {
    int lr = (w * 2 + i) * 8 + (lane >> 3);
    int logical = (lane & 7) ^ ((lr >> 1) & 7);
    xs[i] = x + (mt * 128 + lr) * DM + logical * 8;
    const int id = (wi0 + i) * 64 + lane;
    wso[i] = (id / 12) * N3 + nt * 96 + (id % 12) * 8;
  }
#pragma unroll
  for (int c = 0; c < 6; ++c) {
    if (c < 5) {
#pragma unroll
      for (int i = 0; i < 2; ++i)
        __builtin_amdgcn_global_load_lds((const GLBP void*)(xs[i] + c * 64), (LDSP void*)(smem + c * P1_SLOT + (w * 2 + i) * 1024), 16, 0, 0);
    }
    const u16* Wc = c < 5 ? WB + c * 64 * N3 : WA + 6 * 64 * N3;
    char LDSP* dst = smem + (c < 5 ? c * P1_SLOT + 16384 : 5 * P1_SLOT) + wi0 * 1024;
    __builtin_amdgcn_global_load_lds((const GLBP void*)(Wc + wso[0]), (LDSP void*)dst, 16, 0, 0);
    if (w >= 4) __builtin_amdgcn_global_load_lds((const GLBP void*)(Wc + wso[1]), (LDSP void*)(dst + 1024), 16, 0, 0);
  }
}

template <int NT, int NV>
DI void p1_body(const u16* __restrict__ x, const u16* __restrict__ WA, const u16* __restrict__ WB, char* __restrict__ ws, rsrc_t rs,
                int mt, int nt, char LDSP* smem, u64& tag, u32x4 (&sv)[8], unsigned (&so)[8]) {
  const int tid = threadIdx.x, lane = tid & 63;
  const int w = __builtin_amdgcn_readfirstlane(tid >> 6);
  const int msub = w & 3;
  const int j0 = NT == 2 ? 0 : 2;
  const int r = lane & 31, h = lane >> 5;
  constexpr int NS = 2 * (NT - NV) + 4 * NV;
  constexpr int NW = NT == 2 ? 1 : 2;
  const int wi0 = NT == 2 ? w : 2 * w - 4;
  const u16* xs[2];
  int wso[2];
#pragma unroll
  for (int i = 0; i < 2; ++i) {
    int lr = (w * 2 + i) * 8 + (lane >> 3);
    int logical = (lane & 7) ^ ((lr >> 1) & 7);
    xs[i] = x + (mt * 128 + lr) * DM + logical * 8;
    const int id = (wi0 + i) * 64 + lane;
    wso[i] = (id / 12) * N3 + nt * 96 + (id % 12) * 8;
  }
  auto wbase = [&](int s) {
    return s < 7 ? (s % 5) * P1_SLOT + 16384 : (s < 13 ? (2 + (s - 7) / 2) * P1_SLOT + ((s - 7) & 1) * P1_WSLOT : 5 * P1_SLOT);
  };
  auto issue_x = [&](int c, int i) {
    __builtin_amdgcn_global_load_lds((const GLBP void*)(xs[i] + c * 64), (LDSP void*)(smem + (c % 5) * P1_SLOT + (w * 2 + i) * 1024), 16, 0, 0);
  };
  auto issue_w = [&](int s) {
#pragma unroll
    for (int i = 0; i < NW; ++i)
      __builtin_amdgcn_global_load_lds((const GLBP void*)((s < 7 ? WB : WA) + wso[i] + (s % 7) * 64 * N3),
                                       (LDSP void*)(smem + wbase(s) + (wi0 + i) * 1024), 16, 0, 0);
  };
  int xoff[4];
  {
    int lr = 32 * msub + r, sw = (lr >> 1) & 7;
#pragma unroll
    for (int t = 0; t < 4; ++t) xoff[t] = lr * 128 + (((2 * t + h) ^ sw) << 4);
  }
  const int q4 = (lane & 15) >> 2, p4 = lane & 3, g16 = (lane >> 4) & 1;
  const int woff = (8 * h + q4) * 192 + 64 * j0 + 32 * g16 + 8 * p4;

  f32x16 acc[NT] = {};
  bf16x8 xf[7][4];
  s16x4 wl[2][4][NT], wh[2][4][NT];
  auto readf = [&](int s, int m) {
    const int t = m / NT, j = m % NT;
    if (j == 0 && s < 7) xf[s][t] = *(const bf16x8 LDSP*)(smem + (s % 5) * P1_SLOT + xoff[t]);
    const char LDSP* wp = smem + woff + wbase(s) + t * 16 * 192 + j * 64;
    wl[s & 1][t][j] = tr_read(wp);
    wh[s & 1][t][j] = tr_read(wp + 4 * 192);
  };
  tag = read_tag(ws);
  if (tid < 8) ((int LDSP*)(smem + P1_CTL))[tid] = 0;
  __builtin_amdgcn_s_waitcnt(WAITVL(4 * (NW + 2) + NW));
  RAWBAR();
#pragma unroll
  for (int m = 0; m < 4 * NT; ++m) readf(0, m);
#pragma unroll
  for (int s = 0; s < 14; ++s) {
    if (s < 7 || s == 8) {
      if (s < 3) __builtin_amdgcn_s_waitcnt(WAITVL(3 * (NW + 2) + NW));
      else if (s == 3) __builtin_amdgcn_s_waitcnt(WAITVL(2 * (NW + 2) + 3 * NW));
      else if (s == 4) __builtin_amdgcn_s_waitcnt(WAITVL(NW + 2 + 4 * NW));
      else if (s == 5) __builtin_amdgcn_s_waitcnt(WAITVL(6 * NW));
      else if (s == 6) __builtin_amdgcn_s_waitcnt(WAITVL(4 * NW));
      else __builtin_amdgcn_s_waitcnt(WAITVL(NS));
      RAWBAR();
    }
    if (s == 12) {
      __builtin_amdgcn_s_waitcnt(WAITV(0));
      asm volatile("" ::: "memory");
      lds_arrive(smem, 4, 7, ws, F1B + (nt * 16 + mt) * 8, tag, lane);
    }
    __builtin_amdgcn_sched_barrier(0);
#pragma unroll
    for (int m = 0; m < 4 * NT; ++m) {
      const int t = m / NT, j = m % NT;
      const bf16x8 wfr = cat(wl[s & 1][t][j], wh[s & 1][t][j]);
      if (j < NT - NV) acc[j] = MFMA(wfr, xf[s % 7][t], acc[j]);
      else acc[j] = MFMA(xf[s % 7][t], wfr, acc[j]);
      __builtin_amdgcn_sched_barrier(0);
      if (s < 13) readf(s + 1, m);
      if (j == 0) {
        if (s < 2) { if (t < 2) issue_x(s + 5, t); else if (t == 2) issue_w(s + 5); }
        else if (s < 5 && t < 2) issue_w(7 + 2 * (s - 2) + t);
      }
      __builtin_amdgcn_sched_barrier(0);
    }
    if (s == 6 || s == 13) {
#pragma unroll
      for (int e = 0; e < 8; ++e) so[e] = DROP;
#pragma unroll
      for (int j = 0; j < NT; ++j) {
        if (j < NT - NV) p1_pack<false>(acc[j], s == 6, nt * 3 + j0 + j, mt * 4 + msub, lane, sv + 4 * j, so + 4 * j);
        else p1_pack<true>(acc[j], s == 6, nt * 3 + j0 + j, mt * 4 + msub, lane, sv + 4 * j, so + 4 * j);
        if (s == 6) {
#pragma unroll
          for (int e = 0; e < (j < NT - NV ? 2 : 4); ++e) bstore(sv[4 * j + e], rs, so[4 * j + e], 0);
        }
        acc[j] = f32x16{};
      }
      __builtin_amdgcn_sched_barrier(0);
    }
  }
}

DI void p1_spare(const u16* __restrict__ x, const u16* __restrict__ gate_w, const u16* __restrict__ WoA, const u16* __restrict__ WoB,
                 char* __restrict__ ws, rsrc_t rs, int sp, char LDSP* smem, u64& tag) {
  const int tid = threadIdx.x, lane = tid & 63, h = lane >> 5;
  const int w = __builtin_amdgcn_readfirstlane(tid >> 6);
  {
    const u16* Wo = (sp & 1) ? WoB : WoA;
    const int col0 = 56 * (sp >> 2) + 32 * ((sp >> 1) & 1);
#pragma unroll
    for (int e = 0; e < 4; ++e) {
      const int id = (w * 4 + e) * 64 + lane;
      int idx = (id >> 2) * DM + col0 + (id & 3) * 8;
      idx = idx < DM * DM - 8 ? idx : DM * DM - 8;
      __builtin_amdgcn_global_load_lds((const GLBP void*)(Wo + idx), (LDSP void*)(smem + (w * 4 + e) * 1024), 16, 0, 0);
    }
  }
  tag = read_tag(ws);
  if (tid < 8) ((int LDSP*)(smem + P1_CTL))[tid] = 0;
  __builtin_amdgcn_s_waitcnt(0xC07F);
  RAWBAR();
  if (tid == 0) set_flag(ws, F1B + ((14 + (sp & 1)) * 16 + (sp >> 1)) * 8, tag);
  const int row = sp * 64 + (tid >> 3), part = tid & 7;
  const u32x4* xp = (const u32x4*)(x + row * DM) + part;
  const u32x4* gp = (const u32x4*)(gate_w) + part;
  float acc = 0.f;
#pragma unroll
  for (int i = 0; i < 7; ++i) {
    u32x4 a = xp[i * 8], bb = gp[i * 8];
#pragma unroll
    for (int e = 0; e < 4; ++e) {
      acc = fmaf(bf_lo(a[e]), bf_lo(bb[e]), acc);
      acc = fmaf(bf_hi(a[e]), bf_hi(bb[e]), acc);
    }
  }
  acc += __shfl_xor(acc, 1);
  acc += __shfl_xor(acc, 2);
  acc += __shfl_xor(acc, 4);
  if (part == 0)
    __hip_atomic_store((GLBP float*)(ws + WS_G) + row, 1.f / (1.f + __expf(-acc)), __ATOMIC_RELAXED, __HIP_MEMORY_SCOPE_AGENT);
  asm volatile("s_waitcnt vmcnt(0)" ::: "memory");
  __syncthreads();
  {
    const int q4 = (lane & 15) >> 2, p4 = lane & 3, g16 = (lane >> 4) & 1;
#pragma unroll
    for (int e = 0; e < 4; ++e) {
      const int f = w * 4 + e;
      const char LDSP* p = smem + (28 * (f >> 1) + 16 * (f & 1) + 4 * h + q4) * 64 + 32 * g16 + 8 * p4;
      bstore(__builtin_bit_cast(u32x4, cat(tr_read(p), tr_read(p + 8 * 64))), rs, WS_W + (sp * 32 + f) * 1024 + lane * 16, 0);
    }
  }
}

constexpr unsigned long long lmask(int i, bool prev) {
  unsigned long long m = 0;
  for (int l = 0; l < 64; ++l) {
    int r = l & 31, h = l >> 5;
    int kr = (i & 3) + 8 * (i >> 2) + 4 * h;
    int diff = r - kr + (prev ? 32 : 0);
    if (diff >= 0 && diff <= 14 && (diff & 1) == 0) m |= 1ull << l;
  }
  return m;
}
DI float sel_mask(float val, float other, unsigned long long mask) {
  return __builtin_amdgcn_inverse_ballot_w64(mask) ? val : other;
}
template <int ODD>
DI void set_foreign(u32x4& f0, u32x4& f1, int h, unsigned a, unsigned b) {
  if (ODD) { if (!h) { f0[0] = a; f0[1] = b; } }
  else { if (h) { f1[2] = a; f1[3] = b; } }
}
DI float gload(char* ws, int idx) { return __hip_atomic_load((GLBP float*)(ws + WS_G) + idx, __ATOMIC_RELAXED, __HIP_MEMORY_SCOPE_AGENT); }

template <int ODD>
DI void p2_body(char* __restrict__ ws, u16* __restrict__ out, rsrc_t rs, char LDSP* smem, const u64 tag, const int fidx,
                const u32x4 (&sv)[8], const unsigned (&so)[8]) {
  f32x4 LDSP* part = (f32x4 LDSP*)smem;
  float LDSP* cw = (float LDSP*)(smem + 131072);
  const int tid = threadIdx.x;
  int lane = tid & 63;
  const int w = __builtin_amdgcn_readfirstlane(tid >> 6);
  int r = lane & 31, h = lane >> 5;
  const int b = blockIdx.x;
  const int head = (b & 7) * 2 + ODD, qb = b >> 4;
  const int oqb = (b & 7) * 2 + (b >> 7), oqh = (b >> 3) & 1, nb = (b >> 4) & 7;
  const unsigned p0 = (7 * head) >> 1;
  unsigned l16 = lane * 16;
  const unsigned qoff = WS_Q + QK_BR + (qb * 4 * 56 + p0) * 512;
  const unsigned koff = WS_K + QK_BR + (w * 8 * 56 + p0) * 512;
  const unsigned voff = WS_V + ((16 + head) * 64 + w * 8) * 2048;
  __syncthreads();
  if (w < 4) {
#pragma unroll
    for (int e = 0; e < 8; ++e) {
      ((u32x4 LDSP*)(smem + (w + 4) * 16384))[e * 64 + lane] = sv[e];
      ((unsigned LDSP*)(smem + (w + 4) * 16384 + 8192))[e * 64 + lane] = so[e];
    }
  }
  if (w == 0) {
    const int sel = lane >> 4;
    const int c0 = sel < 2 ? 448 + 8 * (int)p0 + (sel & 1) * 31 : 896 + 28 * head + (sel & 1) * 27;
    gu64* f = (gu64*)(ws + F1B) + (c0 / 96) * 16 + (lane & 15);
    gu64* fq = (gu64*)(ws + F1B) + ((8 * (int)p0 + (lane & 1) * 31) / 96) * 16 + qb;
    for (unsigned spins = 0; spins < (1u << 20); ++spins) {
      bool ok = __hip_atomic_load(f, __ATOMIC_RELAXED, __HIP_MEMORY_SCOPE_AGENT) == tag;
      ok &= __hip_atomic_load(fq, __ATOMIC_RELAXED, __HIP_MEMORY_SCOPE_AGENT) == tag;
      if (__all(ok)) break;
      __builtin_amdgcn_s_sleep(1);
    }
    asm volatile("" ::: "memory");
  }
  __syncthreads();
  u32x4 q[4][2];
  u32x4 kf[4][2], vf[4][2];
  auto loadkv = [&](int t) {
    kf[t & 3][0] = bload(rs, l16, koff + t * 28672);
    kf[t & 3][1] = bload(rs, l16 + 1024, koff + t * 28672);
    vf[t & 3][0] = bload(rs, l16, voff + t * 2048);
    vf[t & 3][1] = bload(rs, l16 + 1024, voff + t * 2048);
  };
  auto store_a = [&]() {
#pragma unroll
    for (int e = 0; e < 4; ++e) bstore(sv[e], rs, so[e], 0);
#pragma unroll
    for (int e = 0; e < 8; ++e)
      bstore(((u32x4 LDSP*)(smem + w * 16384))[e * 64 + lane], rs, ((unsigned LDSP*)(smem + w * 16384 + 8192))[e * 64 + lane], 0);
  };
  if (w >= 4) {
    store_a();
    asm volatile("s_waitcnt vmcnt(0)" ::: "memory");
    lds_arrive(smem, 2, 3, ws, F1A + fidx * 8, tag, lane);
    if (b == 0 && w == 5) {
      wait_flags256(ws, F1B, tag, lane);
      if (lane == 0) set_flag(ws, WS_C, tag);
    }
    __builtin_amdgcn_s_setprio(1);
  }
  kf[0][0] = bload(rs, l16, koff);
  kf[0][1] = bload(rs, l16 + 1024, koff);
#pragma unroll
  for (int i = 0; i < 4; ++i) { q[i][0] = bload(rs, l16, qoff + i * 28672); q[i][1] = bload(rs, l16 + 1024, qoff + i * 28672); }
  vf[0][0] = bload(rs, l16, voff);
  vf[0][1] = bload(rs, l16 + 1024, voff);
  loadkv(1);
  loadkv(2);
  __builtin_amdgcn_sched_barrier(0);

  auto fold_shift = [&](int i, float m) {
    m = swap_max(m);
    if (!h) cw[(i * 32 + r) * 8 + w] = m;
    const float c = -m;
    unsigned p01 = pack2(c, 0.f);
    float r1 = c - bf_lo(p01);
    unsigned p1 = pack2(r1, 0.f);
    float r2 = r1 - bf_lo(p1);
    unsigned p2 = pack2(r2, 0.f);
    set_foreign<ODD>(q[i][0], q[i][1], h, (p01 & 0xffffu) | (p1 << 16), p2 & 0xffffu);
  };
  const int ri = w >> 1, rs1 = w & 1;
  f32x4 a0, a1;
  float l;
  auto write_part = [&](f32x16 (&o)[4]) {
#pragma unroll
    for (int i = 0; i < 4; ++i)
#pragma unroll
      for (int g = 0; g < 4; ++g)
        part[((w * 4 + i) * 4 + g) * 64 + lane] = f32x4{o[i][4 * g], o[i][4 * g + 1], o[i][4 * g + 2], o[i][4 * g + 3]};
  };
  auto reduce = [&]() {
    const f32x4 c0 = *(f32x4 LDSP*)(cw + (ri * 32 + r) * 8), c1 = *(f32x4 LDSP*)(cw + (ri * 32 + r) * 8 + 4);
    const float cm = fmaxf(fmaxf(fmaxf(c0[0], c0[1]), fmaxf(c0[2], c0[3])), fmaxf(fmaxf(c1[0], c1[1]), fmaxf(c1[2], c1[3])));
    a0 = f32x4{}; a1 = f32x4{}; l = 0.f;
#pragma unroll
    for (int ww = 0; ww < 8; ++ww) {
      const float wt = __builtin_amdgcn_exp2f((ww < 4 ? c0[ww & 3] : c1[ww & 3]) - cm);
      a0 += part[((ww * 4 + ri) * 4 + 2 * rs1) * 64 + lane] * wt;
      a1 += part[((ww * 4 + ri) * 4 + 2 * rs1 + 1) * 64 + lane] * wt;
      l += ((float LDSP*)(part + ((ww * 4 + ri) * 4 + 3) * 64 + 32 + r))[0] * wt;
    }
  };
  const int kq = w & 3;
  u32x4 wfr[2][2][8];
  auto loadW = [&]() {
#pragma unroll
    for (int br = 0; br < 2; ++br)
#pragma unroll
      for (int nt = 0; nt < 2; ++nt) {
        const unsigned wo = WS_W + (((nb * 2 + nt) * 32 + br * 16 + 4 * kq) * 2) * 1024;
#pragma unroll
        for (int e = 0; e < 8; ++e) wfr[br][nt][e] = bload(rs, l16 + (e & 3) * 1024, wo + (e >> 2) * 4096);
      }
  };

#pragma unroll
  for (int i = 0; i < 4; ++i) {
    set_foreign<ODD>(q[i][0], q[i][1], h, 0u, 0u);
    f32x16 s = MFMA(as_frag(kf[0][0]), as_frag(q[i][0]), f32x16{});
    s = MFMA(as_frag(kf[0][1]), as_frag(q[i][1]), s);
    fold_shift(i, fmaxf(fmaxf(s[0], s[1]), s[2]));
  }
  __builtin_amdgcn_s_setprio(0);
  {
    f32x16 o[4] = {};
    set_foreign<ODD>(kf[0][0], kf[0][1], h, 0x3F803F80u, 0x00003F80u);
    f32x16 sa = MFMA(as_frag(kf[0][0]), as_frag(q[0][0]), f32x16{});
    sa = MFMA(as_frag(kf[0][1]), as_frag(q[0][1]), sa);
    f32x16 sb;
    u32x4 pa;
    u32x4 pbp;
    float e3[4];
#define SB __builtin_amdgcn_sched_barrier(0)
#define EXPQ(S, g) { S[4 * g] = __builtin_amdgcn_exp2f(S[4 * g]); S[4 * g + 1] = __builtin_amdgcn_exp2f(S[4 * g + 1]); \
                     S[4 * g + 2] = __builtin_amdgcn_exp2f(S[4 * g + 2]); S[4 * g + 3] = __builtin_amdgcn_exp2f(S[4 * g + 3]); }
    auto step = [&](auto nc, f32x16& sc, f32x16& sn) {
      constexpr int n = decltype(nc)::value;
      constexpr int t = n >> 2, i = n & 3;
      constexpr int t1 = ((n + 1) >> 2) & 3, i1 = (n + 1) & 3;
      constexpr int tp = n > 0 ? ((n - 1) >> 2) & 3 : 0, ip = n > 0 ? (n - 1) & 3 : 0;
      if (i == 1 && t + 3 < 8) loadkv(t + 3);
      if (i == 3 && n + 1 < 32) set_foreign<ODD>(kf[t1][0], kf[t1][1], h, 0x3F803F80u, 0x00003F80u);
      SB;
      if (n + 1 < 32) sn = MFMA(as_frag(kf[t1][0]), as_frag(q[i1][0]), f32x16{});
      SB;
      if (n > 0) { pbp[2] = pack2(e3[0], e3[1]); pbp[3] = pack2(e3[2], e3[3]); }
      SB;
      EXPQ(sc, 0);
      SB;
      if (n + 1 < 32) sn = MFMA(as_frag(kf[t1][1]), as_frag(q[i1][1]), sn);
      SB;
      pa[0] = pack2(sc[0], sc[1]); pa[1] = pack2(sc[2], sc[3]);
      SB;
      EXPQ(sc, 1);
      SB;
      if (n > 0) o[ip] = MFMA(as_frag(vf[tp][1]), as_frag(pbp), o[ip]);
      SB;
      pa[2] = pack2(sc[4], sc[5]); pa[3] = pack2(sc[6], sc[7]);
      SB;
      EXPQ(sc, 2);
      SB;
      o[i] = MFMA(as_frag(vf[t & 3][0]), as_frag(pa), o[i]);
      SB;
      pbp[0] = pack2(sc[8], sc[9]); pbp[1] = pack2(sc[10], sc[11]);
      SB;
      EXPQ(sc, 3);
      e3[0] = sc[12]; e3[1] = sc[13]; e3[2] = sc[14]; e3[3] = sc[15];
      SB;
    };
#define STEP2(n) step(std::integral_constant<int, n>{}, sa, sb); step(std::integral_constant<int, n + 1>{}, sb, sa);
    STEP2(0) STEP2(2) STEP2(4) STEP2(6) STEP2(8) STEP2(10) STEP2(12) STEP2(14)
    STEP2(16) STEP2(18) STEP2(20) STEP2(22) STEP2(24) STEP2(26) STEP2(28) STEP2(30)
#undef STEP2
    pbp[2] = pack2(e3[0], e3[1]); pbp[3] = pack2(e3[2], e3[3]);
    o[3] = MFMA(as_frag(vf[3][1]), as_frag(pbp), o[3]);
    asm volatile("" : "+v"(lane));
    r = lane & 31; h = lane >> 5; l16 = lane * 16;
    {
      float chk = 0.f;
#pragma unroll
      for (int i = 0; i < 4; ++i)
#pragma unroll
        for (int j = 0; j < 16; ++j) chk = fmaf(o[i][j], 0.f, chk);
      const float lmax = fmaxf(fmaxf(o[0][12], o[1][12]), fmaxf(o[2][12], o[3][12]));
      const bool bad = !(chk == 0.f) || (h && !(lmax < 1.0e24f));
      if (__any(bad)) {
#pragma unroll
        for (int i = 0; i < 4; ++i) set_foreign<ODD>(q[i][0], q[i][1], h, 0u, 0u);
        float m[4] = {-3.0e38f, -3.0e38f, -3.0e38f, -3.0e38f};
#pragma unroll 1
        for (int t = 0; t < 8; ++t) {
          u32x4 k0 = bload(rs, l16, koff + t * 28672), k1 = bload(rs, l16 + 1024, koff + t * 28672);
#pragma unroll
          for (int i = 0; i < 4; ++i) {
            f32x16 s = MFMA(as_frag(k0), as_frag(q[i][0]), f32x16{});
            s = MFMA(as_frag(k1), as_frag(q[i][1]), s);
#pragma unroll
            for (int j = 0; j < 16; ++j) m[i] = fmaxf(m[i], s[j]);
          }
        }
#pragma unroll
        for (int i = 0; i < 4; ++i) { fold_shift(i, m[i]); o[i] = f32x16{}; }
#pragma unroll 1
        for (int t = 0; t < 8; ++t) {
          u32x4 k0 = bload(rs, l16, koff + t * 28672), k1 = bload(rs, l16 + 1024, koff + t * 28672);
          u32x4 v0 = bload(rs, l16, voff + t * 2048), v1 = bload(rs, l16 + 1024, voff + t * 2048);
          set_foreign<ODD>(k0, k1, h, 0x3F803F80u, 0x00003F80u);
#pragma unroll
          for (int i = 0; i < 4; ++i) {
            f32x16 s = MFMA(as_frag(k0), as_frag(q[i][0]), f32x16{});
            s = MFMA(as_frag(k1), as_frag(q[i][1]), s);
#pragma unroll
            for (int j = 0; j < 16; ++j) s[j] = __builtin_amdgcn_exp2f(s[j]);
            o[i] = MFMA(as_frag(v0), as_frag(pack_step1<0>(s)), o[i]);
            o[i] = MFMA(as_frag(v1), as_frag(pack_step1<1>(s)), o[i]);
          }
        }
      }
    }
    write_part(o);
  }
  SB;
  if (w < 4) {
    {
      gu64* f = (gu64*)(ws + F1A) + (qb > 0 ? qb - 1 : 0) * 16 + (lane & 31) - ((lane & 15) >= 14 ? 2 : 0);
      for (unsigned spins = 0; spins < (1u << 20); ++spins) {
        if (__all(__hip_atomic_load(f, __ATOMIC_RELAXED, __HIP_MEMORY_SCOPE_AGENT) == tag)) break;
        __builtin_amdgcn_s_sleep(2);
      }
      asm volatile("" ::: "memory");
    const int ltile = qb * 4 + (w & 3);
    const unsigned lqo = WS_Q + (ltile * 56 + p0) * 512, lko = WS_K + (ltile * 56 + p0) * 512, lvo = WS_V + (head * 64 + ltile) * 2048;
    const unsigned pd = ltile > 0 ? 1 : 0;
    u32x4 lq0 = bload(rs, l16, lqo), lq1 = bload(rs, l16 + 1024, lqo);
    u32x4 lk0 = bload(rs, l16, lko), lk1 = bload(rs, l16 + 1024, lko);
    u32x4 pk0 = bload(rs, l16, lko - pd * 28672), pk1 = bload(rs, l16 + 1024, lko - pd * 28672);
    u32x4 lv0 = bload(rs, l16, lvo), lv1 = bload(rs, l16 + 1024, lvo);
    u32x4 pv1 = bload(rs, l16 + 1024, lvo - pd * 2048);
    set_foreign<ODD>(lq0, lq1, h, 0u, 0u);
    f32x16 sp = MFMA(as_frag(pk0), as_frag(lq0), f32x16{});
    sp = MFMA(as_frag(pk1), as_frag(lq1), sp);
    f32x16 sc = MFMA(as_frag(lk0), as_frag(lq0), f32x16{});
    sc = MFMA(as_frag(lk1), as_frag(lq1), sc);
    const bool first = (ltile == 0);
    const float NEG = -3.0e38f;
    float m = NEG;
#define LM_C(i) { sc[i] = sel_mask(sc[i], NEG, lmask(i, false)); m = fmaxf(m, sc[i]); }
#define LM_P(i) { sp[i] = sel_mask(sp[i], NEG, first ? 0ull : lmask(i, true)); m = fmaxf(m, sp[i]); }
    LM_C(0) LM_C(1) LM_C(2) LM_C(3) LM_C(4) LM_C(5) LM_C(6) LM_C(7)
    LM_C(8) LM_C(9) LM_C(10) LM_C(11) LM_C(12) LM_C(13) LM_C(14) LM_C(15)
    LM_P(8) LM_P(9) LM_P(10) LM_P(11) LM_P(12) LM_P(13) LM_P(14) LM_P(15)
#undef LM_C
#undef LM_P
    m = swap_max(m);
#pragma unroll
    for (int i = 0; i < 16; ++i) sc[i] = __builtin_amdgcn_exp2f(sc[i] - m);
#pragma unroll
    for (int i = 8; i < 16; ++i) sp[i] = __builtin_amdgcn_exp2f(sp[i] - m);
    f32x16 o = MFMA(as_frag(pv1), as_frag(pack_step1<1>(sp)), f32x16{});
    o = MFMA(as_frag(lv0), as_frag(pack_step1<0>(sc)), o);
    o = MFMA(as_frag(lv1), as_frag(pack_step1<1>(sc)), o);
    float ll = __shfl(o[12], 32 + r);
    float inv = 1.f / ll;
    u32x4 o0 = pack_step<0>(o, inv), o1 = pack_step<1>(o, inv);
    if (h) { o1[2] = 0; o1[3] = 0; }
    const unsigned oo = WS_O + ((ltile * 32 + head) * 2) * 1024;
    bstore(o0, rs, l16, oo);
    bstore(o1, rs, l16 + 1024, oo);
    asm volatile("s_waitcnt vmcnt(0)" ::: "memory");
    lds_arrive(smem, 1, 3, ws, F2A + (qb * 16 + head) * 8, tag, lane);
    }
    {
      gu64* f = (gu64*)(ws + F1A) + ((lane & 31) >> 1) * 16 + 14 + (lane & 1);
      for (unsigned spins = 0; spins < (1u << 20); ++spins) {
        if (__all(__hip_atomic_load(f, __ATOMIC_RELAXED, __HIP_MEMORY_SCOPE_AGENT) == tag)) break;
        __builtin_amdgcn_s_sleep(2);
      }
      asm volatile("" ::: "memory");
    }
    loadW();
  }
  auto publish = [&]() {
    const float inv = 1.f / l;
    u32x4 ov;
    ov[0] = pack2(a0[0] * inv, a0[1] * inv);
    ov[1] = pack2(a0[2] * inv, a0[3] * inv);
    ov[2] = pack2(a1[0] * inv, a1[1] * inv);
    ov[3] = pack2(a1[2] * inv, a1[3] * inv);
    if (h && rs1) { ov[2] = 0; ov[3] = 0; }
    bstore(ov, rs, l16, WS_O + (((qb * 4 + ri) * 32 + 16 + head) * 2 + rs1) * 1024);
  };
  __syncthreads();
  reduce();
  publish();
  if (w < 4) wait_flags16(ws, F2A + oqb * 128, tag, lane);
  asm volatile("s_waitcnt vmcnt(0)" ::: "memory");
  __syncthreads();
  if (tid == 256) set_flag(ws, F2B + (qb * 16 + head) * 8, tag);
  {
    const int oqt = oqb * 4 + oqh * 2;
    const unsigned ofo = WS_O + ((oqt * 32 + 4 * kq) * 2) * 1024;
    auto proj = [&](int br) {
      u32x4 ofr[2][8];
      float gt[2];
#pragma unroll
      for (int qq = 0; qq < 2; ++qq) {
#pragma unroll
        for (int e = 0; e < 8; ++e) ofr[qq][e] = bload(rs, l16 + (e & 3) * 1024, ofo + qq * 65536 + br * 32768 + (e >> 2) * 4096);
        gt[qq] = gload(ws, (oqt + qq) * 32 + r);
      }
#pragma unroll
      for (int qq = 0; qq < 2; ++qq) {
        f32x16 acc[2] = {};
        const float gg = br ? 1.f - gt[qq] : gt[qq];
#pragma unroll
        for (int e = 0; e < 8; ++e)
#pragma unroll
          for (int nt = 0; nt < 2; ++nt) acc[nt] = MFMA(as_frag(wfr[br][nt][e]), as_frag(ofr[qq][e]), acc[nt]);
#pragma unroll
        for (int nt = 0; nt < 2; ++nt)
#pragma unroll
          for (int g = 0; g < 4; ++g)
            part[(((nt * 2 + qq) * 8 + br * 4 + kq) * 4 + g) * 64 + lane] =
                f32x4{acc[nt][4 * g] * gg, acc[nt][4 * g + 1] * gg, acc[nt][4 * g + 2] * gg, acc[nt][4 * g + 3] * gg};
      }
    };
    if (w < 4) proj(0);
    if (w == 4) wait_flags16(ws, F2B + oqb * 128, tag, lane);
    __syncthreads();
    if (w < 4) proj(1);
  }
  __syncthreads();
  {
    const int ft = w & 3, fg = w >> 2;
    f32x4 s0 = {}, s1 = {};
#pragma unroll
    for (int k = 0; k < 8; ++k) {
      s0 += part[((ft * 8 + k) * 4 + 2 * fg) * 64 + lane];
      s1 += part[((ft * 8 + k) * 4 + 2 * fg + 1) * 64 + lane];
    }
    const u32x4 ov = widen(pack2(s0[0], s0[1]), pack2(s0[2], s0[3]), pack2(s1[0], s1[1]), pack2(s1[2], s1[3]));
    const int col = 32 * (ft >> 1) + 16 * fg + 8 * h;
    if (col < 56) *(u32x4*)(out + (oqb * 128 + oqh * 64 + (ft & 1) * 32 + r) * DM + nb * 56 + col) = ov;
  }
}

__global__ __launch_bounds__(512, 2) void fused_block(const u16* __restrict__ x, const u16* __restrict__ WA, const u16* __restrict__ WB,
                                                      const u16* __restrict__ gate_w, const u16* __restrict__ WoA,
                                                      const u16* __restrict__ WoB, char* __restrict__ ws, u16* __restrict__ out) {
  extern __shared__ __attribute__((aligned(16))) char smem_[];
  char LDSP* smem = (char LDSP*)smem_;
  const rsrc_t rs = __builtin_amdgcn_make_buffer_rsrc(ws, 0, 32u << 20, 0x00020000);
  u64 tag;
  asm volatile("s_getpc_b64 %0" : "=s"(tag));
  __builtin_amdgcn_global_load_lds((const GLBP void*)((const char*)tag + (((blockIdx.x >> 3) * 8 + (threadIdx.x >> 6)) * 2 + (threadIdx.x & 1)) * 128),
                                   (LDSP void*)(smem + P1_SINK), 4, 0, 0);
  u32x4 sv[8];
  unsigned so[8];
  const int b = blockIdx.x, xcd = b & 7, j = b >> 3;
  if (__builtin_expect(j < 28, 1)) {
    const int mt = 4 * (xcd & 3) + (j & 3);
    const int nt = 7 * (xcd >> 2) + (j >> 2);
    p1_first(x, WA, WB, mt, nt, smem);
    if (__builtin_amdgcn_readfirstlane(threadIdx.x >> 8) == 0) {
      if (nt < 9) p1_body<2, 0>(x, WA, WB, ws, rs, mt, nt, smem, tag, sv, so);
      else if (nt == 9) p1_body<2, 1>(x, WA, WB, ws, rs, mt, nt, smem, tag, sv, so);
      else p1_body<2, 2>(x, WA, WB, ws, rs, mt, nt, smem, tag, sv, so);
    } else {
      if (nt < 9) p1_body<1, 0>(x, WA, WB, ws, rs, mt, nt, smem, tag, sv, so);
      else p1_body<1, 1>(x, WA, WB, ws, rs, mt, nt, smem, tag, sv, so);
    }
  } else {
    warm_l2(x, WA, WB, smem);
    p1_spare(x, gate_w, WoA, WoB, ws, rs, xcd * 4 + j - 28, smem, tag);
    asm volatile("s_waitcnt vmcnt(0)" ::: "memory");
#pragma unroll
    for (int e = 0; e < 8; ++e) { sv[e] = u32x4{}; so[e] = DROP; }
  }
  const int sp = xcd * 4 + j - 28;
  const int fidx = j < 28 ? (4 * (xcd & 3) + (j & 3)) * 16 + 7 * (xcd >> 2) + (j >> 2) : (sp >> 1) * 16 + 14 + (sp & 1);
  if ((b >> 3) & 1) p2_body<1>(ws, out, rs, smem, tag, fidx, sv, so);
  else p2_body<0>(ws, out, rs, smem, tag, fidx, sv, so);
}

extern "C" void kernel_launch(void* const* d_in, const int* in_sizes, int n_in, void* d_out, int out_size, void* d_ws,
                              size_t ws_size, hipStream_t stream) {
  const u16* x = (const u16*)d_in[0];
  const u16* WA = (const u16*)d_in[1];
  const u16* WoA = (const u16*)d_in[2];
  const u16* WB = (const u16*)d_in[4];
  const u16* WoB = (const u16*)d_in[5];
  const u16* gw = (const u16*)d_in[6];
  (void)hipFuncSetAttribute((const void*)fused_block, hipFuncAttributeMaxDynamicSharedMemorySize, LDS_BYTES);
  fused_block<<<256, 512, LDS_BYTES, stream>>>(x, WA, WB, gw, WoA, WoB, (char*)d_ws, (u16*)d_out);
}
```
